```python
import torch

def _softmax(x, axis=-1):
    return torch.softmax(x, dim=axis)
def _gelu(x):
    return torch.nn.functional.gelu(x, approximate="tanh")
def _silu(x):
    return torch.nn.functional.silu(x)
def _sigmoid(x):
    return torch.sigmoid(x)
def _layernorm(x, axis=-1, eps=1e-5):
    m = x.mean(dim=axis, keepdim=True)
    v = x.var(dim=axis, keepdim=True, unbiased=False)
    return (x - m) / torch.sqrt(v + eps)
def _scatter_add(tgt, idx, src, axis=0):
    return tgt.clone().index_add_(axis, idx, src)
def _segment_sum(src, seg, n):
    out = torch.zeros((n,) + src.shape[1:], dtype=src.dtype)
    return out.index_add_(0, seg, src)
def _pad(x, pw):
    flat = tuple(v for pair in reversed(pw) for v in pair)
    return torch.nn.functional.pad(x, flat)

kernel_name = 'interleave_layers__gqa_block_cached__dense_activation_silu__decode'

def setup_inputs():

    _w = (lambda a: (lambda t: t.to(torch.bfloat16) if t.is_floating_point() else t)(torch.as_tensor(a)))
    def _A_setup():
        B, Sq, D = 8, 1, 512
        Hq, Hkv, Sk, Dh = 32, 8, 8192, 128
        assert Hq % Hkv == 0, (Hq, Hkv)
        return {
            "x": _w((lambda *s: torch.randn(*s, dtype=torch.bfloat16) * 0.1)(B, Sq, D)),
            "k_cache": _w((lambda *s: torch.randn(*s, dtype=torch.bfloat16) * 0.8)(B, Hkv, Sk, Dh)),
            "v_cache": _w((lambda *s: torch.randn(*s, dtype=torch.bfloat16) * 3.2)(B, Hkv, Sk, Dh)),
            "wq": _w((lambda *s: torch.randn(*s, dtype=torch.bfloat16) * 0.8)(D, Hq * Dh) * (D ** -0.5)),
            "wo": _w((lambda *s: torch.randn(*s, dtype=torch.bfloat16) * 3.2)(Hq * Dh, D) * ((Hq * Dh) ** -0.5)),
        }
    _A_in = _A_setup()

    def _B_setup():
        B, S, D, F = 8, 1, 512, 1792
        return {
            "x": _w((lambda *s: torch.randn(*s, dtype=torch.bfloat16) * 0.1)(B, S, D)),
            "w1": _w((lambda *s: torch.randn(*s, dtype=torch.bfloat16) * 1.6)(D, F) * (D ** -0.5)),
            "w2": _w((lambda *s: torch.randn(*s, dtype=torch.bfloat16) * 0.4)(F, D) * (F ** -0.5)),
        }
    _B_in = _B_setup()

    return {
        "A_x": _A_in["x"],
        "A_k_cache": _A_in["k_cache"],
        "A_v_cache": _A_in["v_cache"],
        "A_wq": _A_in["wq"],
        "A_wo": _A_in["wo"],
        "B_x": _B_in["x"],
        "B_w1": _B_in["w1"],
        "B_w2": _B_in["w2"],
    }

def reference(A_x, A_k_cache, A_v_cache, A_wq, A_wo, B_x, B_w1, B_w2):
    def _A_ref(x, k_cache, v_cache, wq, wo):
        B, Sq, D = x.shape
        Hq, Dh = 32, 128
        Hkv = k_cache.shape[1]
        rep = Hq // Hkv
        q = (lambda x, *d: x.permute(*d))((x @ wq).reshape(B, Sq, Hq, Dh), 0, 2, 1, 3)
        k = (lambda x, n, axis: torch.repeat_interleave(x, n, dim=axis))(k_cache, rep, axis=1)
        v = (lambda x, n, axis: torch.repeat_interleave(x, n, dim=axis))(v_cache, rep, axis=1)
        scores = torch.einsum("bhqd,bhkd->bhqk", q, k) * (Dh ** -0.5)
        p = _softmax(scores, axis=-1)
        o = torch.einsum("bhqk,bhkd->bhqd", p, v)
        return (lambda x, *d: x.permute(*d))(o, 0, 2, 1, 3).reshape(B, Sq, Hq * Dh) @ wo

    def _B_ref(x, w1, w2):
        h = torch.einsum("bsd,df->bsf", x, w1)
        h = _silu(h)
        out = torch.einsum("bsf,fd->bsd", h, w2)
        return out

    x = A_x
    x = x + _A_ref(_layernorm(x, axis=-1), A_k_cache, A_v_cache, A_wq, A_wo)
    x = x + _B_ref(_layernorm(x, axis=-1), B_w1, B_w2)
    x = x + _A_ref(_layernorm(x, axis=-1), A_k_cache, A_v_cache, A_wq, A_wo)
    x = x + _B_ref(_layernorm(x, axis=-1), B_w1, B_w2)
    return x
```

```cpp
#include <hip/hip_runtime.h>
#include <hip/hip_bf16.h>
#include <stdint.h>

typedef unsigned u32x4 __attribute__((ext_vector_type(4)));
typedef float f32x4 __attribute__((ext_vector_type(4)));
using bf16x2 = __attribute__((ext_vector_type(2))) __bf16;

constexpr int B = 8, D = 512, HQ = 32, HKV = 8, SK = 8192, DH = 128, F = 1792;
constexpr int NSPLIT = 8;

__device__ __forceinline__ float bf2f(unsigned short b) { return __builtin_bit_cast(float, (unsigned)b << 16); }
__device__ __forceinline__ float dot2(unsigned k, unsigned q, float acc) {
  return __builtin_amdgcn_fdot2_f32_bf16(__builtin_bit_cast(bf16x2, k), __builtin_bit_cast(bf16x2, q), acc, false);
}
__device__ __forceinline__ unsigned short f2bf(float f) { __bf16 h = (__bf16)f; return __builtin_bit_cast(unsigned short, h); }

template <int K, int N, bool LN, int EPI>
__global__ void __launch_bounds__(256) gemv8(const float* in, const unsigned short* W, float* out, const float* res, float scale) {
  extern __shared__ float smem[];
  float* xs = smem;
  float* red = smem + 8 * K;
  float* st = red + 16 * 8 * 16;
  int tid = threadIdx.x, lane = tid & 63, w = tid >> 6;
  for (int i = tid; i < 8 * K; i += 256) xs[i] = in[i];
  __syncthreads();
  if (LN) {
    for (int b = w; b < 8; b += 4) {
      float s = 0; for (int k = lane; k < K; k += 64) s += xs[b * K + k];
      for (int o = 32; o; o >>= 1) s += __shfl_xor(s, o);
      float m = s / K; float v = 0;
      for (int k = lane; k < K; k += 64) { float d = xs[b * K + k] - m; v += d * d; }
      for (int o = 32; o; o >>= 1) v += __shfl_xor(v, o);
      if (lane == 0) { st[b * 2] = m; st[b * 2 + 1] = 1.0f / sqrtf(v / K + 1e-5f); }
    }
    __syncthreads();
    for (int i = tid; i < 8 * K; i += 256) { int b = i / K; xs[i] = bf2f(f2bf((xs[i] - st[b * 2]) * st[b * 2 + 1])); }
    __syncthreads();
  }
  int c = tid & 15, g = tid >> 4;
  int n = blockIdx.x * 16 + c;
  float acc[8] = {0, 0, 0, 0, 0, 0, 0, 0};
  for (int k = g; k < K; k += 16) {
    float wv = bf2f(W[(size_t)k * N + n]);
#pragma unroll
    for (int b = 0; b < 8; ++b) acc[b] += xs[b * K + k] * wv;
  }
  for (int b = 0; b < 8; ++b) red[(g * 8 + b) * 16 + c] = acc[b];
  __syncthreads();
  if (tid < 128) {
    int b = tid >> 4;
    float s = 0; for (int gg = 0; gg < 16; ++gg) s += red[(gg * 8 + b) * 16 + c];
    if (EPI == 0) out[b * N + n] = s * scale;
    if (EPI == 1) out[b * N + n] = bf2f(f2bf(s / (1.0f + __expf(-s))));
    if (EPI == 2) out[b * N + n] = res[b * N + n] + s;
  }
}
template <int K, int N, bool LN, int EPI>
static void launch_gemv(const float* in, const unsigned short* W, float* out, const float* res, float scale, hipStream_t stream) {
  size_t sm = (8 * K + 16 * 8 * 16 + 16) * 4;
  hipFuncSetAttribute((const void*)gemv8<K, N, LN, EPI>, hipFuncAttributeMaxDynamicSharedMemorySize, (int)sm);
  gemv8<K, N, LN, EPI><<<N / 16, 256, sm, stream>>>(in, W, out, res, scale);
}

__global__ void cvt_in(const unsigned short* x, float* o) { int i = blockIdx.x * 256 + threadIdx.x; if (i < B * D) o[i] = bf2f(x[i]); }
__global__ void cvt_out(const float* x, unsigned short* o) { int i = blockIdx.x * 256 + threadIdx.x; if (i < B * D) o[i] = f2bf(x[i]); }

__global__ void __launch_bounds__(256) attn_split(const float* q, const unsigned short* kc, const unsigned short* vc, float* pml, float* po) {
  __shared__ float sc[4][SK / NSPLIT];
  __shared__ float qs[4][DH];
  __shared__ float red[4][4][DH];
  __shared__ float ml[4][2];
  const int s = blockIdx.x, kvh = blockIdx.y, b = blockIdx.z;
  const int tid = threadIdx.x, lane = tid & 63, w = tid >> 6;
  constexpr int KS = SK / NSPLIT;
  for (int i = tid; i < 4 * DH; i += 256) qs[i / DH][i % DH] = q[b * HQ * DH + kvh * 4 * DH + i];
  __syncthreads();
  const unsigned short* kb = kc + ((size_t)(b * HKV + kvh) * SK + s * KS) * DH;
  const unsigned short* vb = vc + ((size_t)(b * HKV + kvh) * SK + s * KS) * DH;
  const int r = lane >> 4, c = lane & 15;
  float qf[4][8];
  for (int h = 0; h < 4; ++h) for (int j = 0; j < 8; ++j) qf[h][j] = qs[h][8 * c + j];
  for (int k0 = w * 4; k0 < KS; k0 += 16) {
    u32x4 kv = __builtin_nontemporal_load((const u32x4*)(kb + (size_t)(k0 + r) * DH + 8 * c));
    float kf[8];
    for (int j = 0; j < 4; ++j) { kf[2 * j] = __builtin_bit_cast(float, kv[j] << 16); kf[2 * j + 1] = __builtin_bit_cast(float, kv[j] & 0xffff0000u); }
    for (int h = 0; h < 4; ++h) {
      float d = 0; for (int j = 0; j < 8; ++j) d += kf[j] * qf[h][j];
      d += __shfl_xor(d, 1); d += __shfl_xor(d, 2); d += __shfl_xor(d, 4); d += __shfl_xor(d, 8);
      if (c == 0) sc[h][k0 + r] = d;
    }
  }
  __syncthreads();
  {
    float m = -INFINITY; for (int k = lane; k < KS; k += 64) m = fmaxf(m, sc[w][k]);
    for (int o = 32; o; o >>= 1) m = fmaxf(m, __shfl_xor(m, o));
    float l = 0; for (int k = lane; k < KS; k += 64) { float p = __expf(sc[w][k] - m); sc[w][k] = p; l += p; }
    for (int o = 32; o; o >>= 1) l += __shfl_xor(l, o);
    if (lane == 0) { ml[w][0] = m; ml[w][1] = l; }
  }
  __syncthreads();
  float acc[4][8];
  for (int h = 0; h < 4; ++h) for (int j = 0; j < 8; ++j) acc[h][j] = 0;
  for (int k0 = w * 4; k0 < KS; k0 += 16) {
    u32x4 vv = __builtin_nontemporal_load((const u32x4*)(vb + (size_t)(k0 + r) * DH + 8 * c));
    float vf[8];
    for (int j = 0; j < 4; ++j) { vf[2 * j] = __builtin_bit_cast(float, vv[j] << 16); vf[2 * j + 1] = __builtin_bit_cast(float, vv[j] & 0xffff0000u); }
    for (int h = 0; h < 4; ++h) { float p = sc[h][k0 + r]; for (int j = 0; j < 8; ++j) acc[h][j] += p * vf[j]; }
  }
  for (int h = 0; h < 4; ++h) for (int j = 0; j < 8; ++j) { float t = acc[h][j]; t += __shfl_xor(t, 16); t += __shfl_xor(t, 32); acc[h][j] = t; }
  if (r == 0) for (int h = 0; h < 4; ++h) for (int j = 0; j < 8; ++j) red[w][h][8 * c + j] = acc[h][j];
  __syncthreads();
  for (int i = tid; i < 4 * DH; i += 256) {
    int h = i / DH, d = i % DH;
    float t = red[0][h][d] + red[1][h][d] + red[2][h][d] + red[3][h][d];
    int row = b * HQ + kvh * 4 + h;
    po[((size_t)row * NSPLIT + s) * DH + d] = t;
    if (d == 0) { pml[(row * NSPLIT + s) * 2] = ml[h][0]; pml[(row * NSPLIT + s) * 2 + 1] = ml[h][1]; }
  }
}
__global__ void __launch_bounds__(128) attn_comb(const float* pml, const float* po, float* o) {
  int row = blockIdx.x, d = threadIdx.x;
  float M = -INFINITY; for (int s = 0; s < NSPLIT; ++s) M = fmaxf(M, pml[(row * NSPLIT + s) * 2]);
  float L = 0, a = 0;
  for (int s = 0; s < NSPLIT; ++s) { float w = __expf(pml[(row * NSPLIT + s) * 2] - M); L += w * pml[(row * NSPLIT + s) * 2 + 1]; a += w * po[((size_t)row * NSPLIT + s) * DH + d]; }
  o[row * DH + d] = bf2f(f2bf(a / L));
}

extern "C" void kernel_launch(void* const* d_in, const int* in_sizes, int n_in, void* d_out, int out_size, void* d_ws, size_t ws_size, hipStream_t stream) {
  const unsigned short* x = (const unsigned short*)d_in[0];
  const unsigned short* kc = (const unsigned short*)d_in[1];
  const unsigned short* vc = (const unsigned short*)d_in[2];
  const unsigned short* wq = (const unsigned short*)d_in[3];
  const unsigned short* wo = (const unsigned short*)d_in[4];
  const unsigned short* w1 = (const unsigned short*)d_in[6];
  const unsigned short* w2 = (const unsigned short*)d_in[7];
  float* ws = (float*)d_ws;
  float* xa = ws; float* xb = ws + 4096; float* q = ws + 8192; float* o = q + B * 4096; float* h = o + B * 4096;
  float* pml = h + B * 2048; float* po = pml + B * HQ * NSPLIT * 2;
  const float scale = 0.08838834764831845f;
  cvt_in<<<16, 256, 0, stream>>>(x, xa);
  for (int l = 0; l < 2; ++l) {
    launch_gemv<D, HQ * DH, true, 0>(xa, wq, q, nullptr, scale, stream);
    attn_split<<<dim3(NSPLIT, HKV, B), 256, 0, stream>>>(q, kc, vc, pml, po);
    attn_comb<<<B * HQ, 128, 0, stream>>>(pml, po, o);
    launch_gemv<HQ * DH, D, false, 2>(o, wo, xb, xa, 1.f, stream);
    launch_gemv<D, F, true, 1>(xb, w1, h, nullptr, 1.f, stream);
    launch_gemv<F, D, false, 2>(h, w2, xa, xb, 1.f, stream);
  }

  cvt_out<<<16, 256, 0, stream>>>(xa, (unsigned short*)d_out);
}
```

```cpp
#include <hip/hip_runtime.h>
#include <hip/hip_bf16.h>
#include <stdint.h>

#define XMAP 0
namespace ik {
typedef unsigned short u16;
typedef unsigned u32x4 __attribute__((ext_vector_type(4)));
typedef unsigned u32x2 __attribute__((ext_vector_type(2)));
typedef float f32x4 __attribute__((ext_vector_type(4)));
typedef float f32x2 __attribute__((ext_vector_type(2)));
typedef __bf16 bf16x8 __attribute__((ext_vector_type(8)));
typedef __bf16 bf16x2 __attribute__((ext_vector_type(2)));
typedef short s16x4 __attribute__((ext_vector_type(4)));
#define LAS __attribute__((address_space(3)))
#define GAS __attribute__((address_space(1)))

constexpr int NB = 8, NKVH = 8, NH = 32, DH = 128, SK = 8192, DM = 512, FF = 1792;
constexpr int CPH = 26;
constexpr int NW_CU = 16, NQ_CU = 16, NM_CU = 14;
constexpr int NCHAIN_R = 6;
constexpr float SM_C = 0.08838834764831845f * 1.4426950408889634f;
constexpr unsigned WS_EPOCH = 0, WS_ABORT = 256;
constexpr unsigned WS_Q = 4096;
constexpr unsigned WS_PML = WS_Q + 2 * 8 * 8 * 2048;
constexpr unsigned WS_PO = WS_PML + 2 * 8 * 32 * 32 * 16;
constexpr unsigned WS_Y = WS_PO + 2 * 8 * 32 * CPH * 512;
constexpr unsigned WS_Z = WS_Y + 2 * 8 * NW_CU * 2048;
constexpr unsigned WS_X1 = WS_Z + 2 * 8 * NM_CU * 2048;
constexpr unsigned WS_X2 = WS_X1 + 2 * 8 * 4096;
constexpr unsigned WS_STAMP = WS_X2 + 8 * 4096;
constexpr unsigned WS_END = WS_STAMP + 256 * 64 * 8;
constexpr unsigned SPIN_CAP = 1u << 16;
constexpr int AUX_SC1 = 16;

struct Params { const u16* x; const u16* kc; const u16* vc; const u16* wq; const u16* wo; const u16* w1; const u16* w2; u16* out; unsigned char* ws; };

typedef __amdgpu_buffer_rsrc_t rsrc_t;
__device__ __forceinline__ u32x4 ld128(rsrc_t r, unsigned off) { return __builtin_amdgcn_raw_buffer_load_b128(r, off, 0, AUX_SC1); }
__device__ __forceinline__ void st128(rsrc_t r, unsigned off, u32x4 v) { __builtin_amdgcn_raw_buffer_store_b128(v, r, off, 0, AUX_SC1); }
__device__ __forceinline__ void st64(rsrc_t r, unsigned off, u32x2 v) { __builtin_amdgcn_raw_buffer_store_b64(v, r, off, 0, AUX_SC1); }
__device__ __forceinline__ f32x4 mfma32(u32x4 a, u32x4 b, f32x4 c) { return __builtin_amdgcn_mfma_f32_16x16x32_bf16(__builtin_bit_cast(bf16x8, a), __builtin_bit_cast(bf16x8, b), c, 0, 0, 0); }
__device__ __forceinline__ f32x4 mfma16(u32x2 a, u32x2 b, f32x4 c) { return __builtin_amdgcn_mfma_f32_16x16x16bf16_1k(__builtin_bit_cast(s16x4, a), __builtin_bit_cast(s16x4, b), c, 0, 0, 0); }
__device__ __forceinline__ float bf_lo(unsigned v) { return __builtin_bit_cast(float, v << 16); }
__device__ __forceinline__ float bf_hi(unsigned v) { return __builtin_bit_cast(float, v & 0xffff0000u); }
__device__ __forceinline__ unsigned pk_bf16(float lo, float hi) { bf16x2 t = {(__bf16)lo, (__bf16)hi}; return __builtin_bit_cast(unsigned, t); }
__device__ __forceinline__ unsigned f2u(float f) { return __builtin_bit_cast(unsigned, f); }
__device__ __forceinline__ float u2f(unsigned u) { return __builtin_bit_cast(float, u); }
__device__ __forceinline__ unsigned perm_lo(unsigned a, unsigned b) { return __builtin_amdgcn_perm(b, a, 0x05040100u); }
__device__ __forceinline__ unsigned perm_hi(unsigned a, unsigned b) { return __builtin_amdgcn_perm(b, a, 0x07060302u); }
template <int CTRL> __device__ __forceinline__ float dpp(float x) { return __builtin_bit_cast(float, __builtin_amdgcn_update_dpp(0, __builtin_bit_cast(int, x), CTRL, 0xf, 0xf, true)); }
__device__ __forceinline__ float xrow_max(float x) {
  auto s = __builtin_amdgcn_permlane16_swap(f2u(x), f2u(x), false, false); x = fmaxf(u2f(s[0]), u2f(s[1]));
  auto t = __builtin_amdgcn_permlane32_swap(f2u(x), f2u(x), false, false); return fmaxf(u2f(t[0]), u2f(t[1]));
}
__device__ __forceinline__ float xrow_sum(float x) {
  auto s = __builtin_amdgcn_permlane16_swap(f2u(x), f2u(x), false, false); x = u2f(s[0]) + u2f(s[1]);
  auto t = __builtin_amdgcn_permlane32_swap(f2u(x), f2u(x), false, false); return u2f(t[0]) + u2f(t[1]);
}
__device__ __forceinline__ float row16_sum(float x) {
  x += dpp<0xB1>(x); x += dpp<0x4E>(x); x += dpp<0x141>(x); x += dpp<0x128>(x); return x;
}
__device__ __forceinline__ float row16_max(float x) {
  x = fmaxf(x, dpp<0xB1>(x)); x = fmaxf(x, dpp<0x4E>(x)); x = fmaxf(x, dpp<0x141>(x)); x = fmaxf(x, dpp<0x128>(x)); return x;
}
__device__ __forceinline__ float wave_sum(float x) { return xrow_sum(row16_sum(x)); }
__device__ __forceinline__ float wave_max(float x) { return xrow_max(row16_max(x)); }
__device__ __forceinline__ unsigned long long rt() { return __builtin_amdgcn_s_memrealtime(); }
#define STAMP(idx) do {} while (0)
#define STAMP_WAVE 0
__device__ __forceinline__ bool spin_fail(unsigned& spins, rsrc_t ws) {
  __builtin_amdgcn_s_sleep(1);
  if (++spins > SPIN_CAP) { __builtin_amdgcn_raw_buffer_store_b32(1u, ws, WS_ABORT, 0, AUX_SC1); return true; }
  return false;
}

constexpr int LDS_BYTES = 160 * 1024;
__device__ __forceinline__ int lane_id() { int l; asm volatile("v_mbcnt_lo_u32_b32 %0, -1, 0\n\tv_mbcnt_hi_u32_b32 %0, -1, %0" : "=v"(l)); return l; }
__device__ __forceinline__ u32x4 uni4(u32x4 v) {
  return u32x4{(unsigned)__builtin_amdgcn_readfirstlane(v[0]), (unsigned)__builtin_amdgcn_readfirstlane(v[1]), (unsigned)__builtin_amdgcn_readfirstlane(v[2]), (unsigned)__builtin_amdgcn_readfirstlane(v[3])};
}
__device__ __forceinline__ u32x4 mk_srd(const void* base, unsigned long long byte_off, unsigned bytes) {
  const unsigned long long a = (unsigned long long)base + byte_off;
  return uni4(u32x4{(unsigned)a, (unsigned)(a >> 32) & 0xffffu, bytes, 0x00020000u});
}
__device__ __forceinline__ unsigned qrec_off(int h4, int dq) {
  const int d = 4 * dq, i = d >> 6, hf = (d >> 5) & 1, g = (d >> 3) & 3, q = (d >> 1) & 3;
  const int k = 2 * i + (q >> 1), n8 = 4 * hf + h4;
  return (unsigned)(((k * 4 + g) * 8 + n8) * 16);
}
#define CHAIN_IN(V0, V1, V2, V3, SX, SA, SB) \
      : "{v[0:3]}"(V0), "{v[4:7]}"(V1), "{v[8:11]}"(V2), "{v[12:15]}"(V3), "{s[8:11]}"(SX), "{s[12:15]}"(s12), "{s[16:19]}"(SA), "{s[20:23]}"(SB), "{s[24:27]}"(sWS), "{s[48:49]}"(s48)
#define CHAIN_COMMON \
  const int lane = lane_id(), c = lane & 15, g = lane >> 4, q32 = lane & 31, ph = lane >> 5; \
  const u32x4 sWS = mk_srd(p.ws, 0, WS_END); \
  const u32x4 s12 = uni4(u32x4{tag, (unsigned)w, (unsigned)idx, tag}); \
  const u32x2 s48 = u32x2{(unsigned)__builtin_amdgcn_readfirstlane(WS_STAMP + tag * 512), 0u}; \
  const unsigned vSt = w == 0 ? 0u : 0x7ffffff0u; \
  (void)c; (void)g; (void)q32; (void)ph;

__device__ __forceinline__ void role_w(const Params& p, int idx, unsigned tag, int w) {
  CHAIN_COMMON
  const unsigned hh = w >> 1, half = w & 1, head = 2 * idx + hh;
  const unsigned vW = 8 * g * 1024 + w * 256 + c * 16;
  unsigned vO[7];
#pragma unroll
  for (int u = 0; u < 7; ++u) { int s = 4 * u + g; s = s < CPH ? s : CPH - 1; vO[u] = (head * CPH + s) * 512 + half * 256 + c * 16; }
  const unsigned vM0 = (head * 32 + g) * 16, vM6 = (head * 32 + (24 + g < CPH ? 24 + g : CPH - 1)) * 16;
  const unsigned eg = ((g & 1) << 1) | (g >> 1);
  const unsigned vOBw = (hh * 128 + 64 * (g >> 1) + 32 * half + 2 * c + (g & 1)) * 2, vOBr = g * 16; (void)eg;
  const unsigned vY = idx * 2048 + (64 * w + 4 * c + g) * 8;
  const u32x4 sA = mk_srd(p.wo, (unsigned long long)idx * 256 * 1024, 256 * 1024);
  asm volatile(R"ASM(
s_nop 4
s_mov_b32 s34, 0x5040100
s_mov_b32 s35, 0x7060302
s_mov_b32 s44, 0
s_mov_b32 s45, -1
s_mov_b32 s28, 0x5a493201
s_mov_b32 s29, 0xf8ed7c6b
s_mov_b32 s30, 0
v_mov_b32 v253, 0
buffer_load_dword v252, v253, s[24:27], 0 offen sc1
s_getpc_b64 s[56:57]
Lrc0w_%=:
s_and_b32 s57, s57, 0xffff
s_mov_b32 s58, Lrcew_%=-Lrc0w_%=
s_mov_b32 s59, 0x20000
v_lshlrev_b32 v251, 7, v0
s_lshl_b32 s46, s13, 13
v_add_u32 v251, s46, v251
buffer_load_dword v250, v251, s[56:59], 0 offen
v_and_b32 v16, 16, v0
v_cmp_ne_u32 s[40:41], 0, v16
v_and_b32 v16, 32, v0
v_cmp_ne_u32 s[42:43], 0, v16
s_mov_b32 s46, 0
buffer_load_dwordx4 a[0:3], v2, s[16:19], s46 offen 
s_mov_b32 s46, 0x400
buffer_load_dwordx4 a[4:7], v2, s[16:19], s46 offen 
s_mov_b32 s46, 0x800
buffer_load_dwordx4 a[8:11], v2, s[16:19], s46 offen 
s_mov_b32 s46, 0xc00
buffer_load_dwordx4 a[12:15], v2, s[16:19], s46 offen 
s_mov_b32 s46, 0x1000
buffer_load_dwordx4 a[16:19], v2, s[16:19], s46 offen 
s_mov_b32 s46, 0x1400
buffer_load_dwordx4 a[20:23], v2, s[16:19], s46 offen 
s_mov_b32 s46, 0x1800
buffer_load_dwordx4 a[24:27], v2, s[16:19], s46 offen 
s_mov_b32 s46, 0x1c00
buffer_load_dwordx4 a[28:31], v2, s[16:19], s46 offen 
s_mov_b32 s46, 0x8000
buffer_load_dwordx4 a[32:35], v2, s[16:19], s46 offen 
s_mov_b32 s46, 0x8400
buffer_load_dwordx4 a[36:39], v2, s[16:19], s46 offen 
s_mov_b32 s46, 0x8800
buffer_load_dwordx4 a[40:43], v2, s[16:19], s46 offen 
s_mov_b32 s46, 0x8c00
buffer_load_dwordx4 a[44:47], v2, s[16:19], s46 offen 
s_mov_b32 s46, 0x9000
buffer_load_dwordx4 a[48:51], v2, s[16:19], s46 offen 
s_mov_b32 s46, 0x9400
buffer_load_dwordx4 a[52:55], v2, s[16:19], s46 offen 
s_mov_b32 s46, 0x9800
buffer_load_dwordx4 a[56:59], v2, s[16:19], s46 offen 
s_mov_b32 s46, 0x9c00
buffer_load_dwordx4 a[60:63], v2, s[16:19], s46 offen 
s_mov_b32 s46, 0x10000
buffer_load_dwordx4 a[64:67], v2, s[16:19], s46 offen 
s_mov_b32 s46, 0x10400
buffer_load_dwordx4 a[68:71], v2, s[16:19], s46 offen 
s_mov_b32 s46, 0x10800
buffer_load_dwordx4 a[72:75], v2, s[16:19], s46 offen 
s_mov_b32 s46, 0x10c00
buffer_load_dwordx4 a[76:79], v2, s[16:19], s46 offen 
s_mov_b32 s46, 0x11000
buffer_load_dwordx4 a[80:83], v2, s[16:19], s46 offen 
s_mov_b32 s46, 0x11400
buffer_load_dwordx4 a[84:87], v2, s[16:19], s46 offen 
s_mov_b32 s46, 0x11800
buffer_load_dwordx4 a[88:91], v2, s[16:19], s46 offen 
s_mov_b32 s46, 0x11c00
buffer_load_dwordx4 a[92:95], v2, s[16:19], s46 offen 
s_mov_b32 s46, 0x18000
buffer_load_dwordx4 a[96:99], v2, s[16:19], s46 offen 
s_mov_b32 s46, 0x18400
buffer_load_dwordx4 a[100:103], v2, s[16:19], s46 offen 
s_mov_b32 s46, 0x18800
buffer_load_dwordx4 a[104:107], v2, s[16:19], s46 offen 
s_mov_b32 s46, 0x18c00
buffer_load_dwordx4 a[108:111], v2, s[16:19], s46 offen 
s_mov_b32 s46, 0x19000
buffer_load_dwordx4 a[112:115], v2, s[16:19], s46 offen 
s_mov_b32 s46, 0x19400
buffer_load_dwordx4 a[116:119], v2, s[16:19], s46 offen 
s_mov_b32 s46, 0x19800
buffer_load_dwordx4 a[120:123], v2, s[16:19], s46 offen 
s_mov_b32 s46, 0x19c00
buffer_load_dwordx4 a[124:127], v2, s[16:19], s46 offen 
s_mov_b32 s46, 0x20000
buffer_load_dwordx4 a[128:131], v2, s[16:19], s46 offen 
s_mov_b32 s46, 0x20400
buffer_load_dwordx4 a[132:135], v2, s[16:19], s46 offen 
s_mov_b32 s46, 0x20800
buffer_load_dwordx4 a[136:139], v2, s[16:19], s46 offen 
s_mov_b32 s46, 0x20c00
buffer_load_dwordx4 a[140:143], v2, s[16:19], s46 offen 
s_mov_b32 s46, 0x21000
buffer_load_dwordx4 a[144:147], v2, s[16:19], s46 offen 
s_mov_b32 s46, 0x21400
buffer_load_dwordx4 a[148:151], v2, s[16:19], s46 offen 
s_mov_b32 s46, 0x21800
buffer_load_dwordx4 a[152:155], v2, s[16:19], s46 offen 
s_mov_b32 s46, 0x21c00
buffer_load_dwordx4 a[156:159], v2, s[16:19], s46 offen 
s_mov_b32 s46, 0x28000
buffer_load_dwordx4 a[160:163], v2, s[16:19], s46 offen 
s_mov_b32 s46, 0x28400
buffer_load_dwordx4 a[164:167], v2, s[16:19], s46 offen 
s_mov_b32 s46, 0x28800
buffer_load_dwordx4 a[168:171], v2, s[16:19], s46 offen 
s_mov_b32 s46, 0x28c00
buffer_load_dwordx4 a[172:175], v2, s[16:19], s46 offen 
s_mov_b32 s46, 0x29000
buffer_load_dwordx4 a[176:179], v2, s[16:19], s46 offen 
s_mov_b32 s46, 0x29400
buffer_load_dwordx4 a[180:183], v2, s[16:19], s46 offen 
s_mov_b32 s46, 0x29800
buffer_load_dwordx4 a[184:187], v2, s[16:19], s46 offen 
s_mov_b32 s46, 0x29c00
buffer_load_dwordx4 a[188:191], v2, s[16:19], s46 offen 
s_mov_b32 s46, 0x30000
buffer_load_dwordx4 a[192:195], v2, s[16:19], s46 offen 
s_mov_b32 s46, 0x30400
buffer_load_dwordx4 a[196:199], v2, s[16:19], s46 offen 
s_mov_b32 s46, 0x30800
buffer_load_dwordx4 a[200:203], v2, s[16:19], s46 offen 
s_mov_b32 s46, 0x30c00
buffer_load_dwordx4 a[204:207], v2, s[16:19], s46 offen 
s_mov_b32 s46, 0x31000
buffer_load_dwordx4 a[208:211], v2, s[16:19], s46 offen 
s_mov_b32 s46, 0x31400
buffer_load_dwordx4 a[212:215], v2, s[16:19], s46 offen 
s_mov_b32 s46, 0x31800
buffer_load_dwordx4 a[216:219], v2, s[16:19], s46 offen 
s_mov_b32 s46, 0x31c00
buffer_load_dwordx4 a[220:223], v2, s[16:19], s46 offen 
s_mov_b32 s46, 0x38000
buffer_load_dwordx4 a[224:227], v2, s[16:19], s46 offen 
s_mov_b32 s46, 0x38400
buffer_load_dwordx4 a[228:231], v2, s[16:19], s46 offen 
s_mov_b32 s46, 0x38800
buffer_load_dwordx4 a[232:235], v2, s[16:19], s46 offen 
s_mov_b32 s46, 0x38c00
buffer_load_dwordx4 a[236:239], v2, s[16:19], s46 offen 
s_mov_b32 s46, 0x39000
buffer_load_dwordx4 a[240:243], v2, s[16:19], s46 offen 
s_mov_b32 s46, 0x39400
buffer_load_dwordx4 a[244:247], v2, s[16:19], s46 offen 
s_mov_b32 s46, 0x39800
buffer_load_dwordx4 a[248:251], v2, s[16:19], s46 offen 
s_mov_b32 s46, 0x39c00
buffer_load_dwordx4 a[252:255], v2, s[16:19], s46 offen 
s_memrealtime s[58:59]
s_waitcnt lgkmcnt(0)
s_mov_b32 s60, s58
s_waitcnt vmcnt(62)
v_readfirstlane_b32 s12, v252
s_nop 3
s_add_u32 s12, s12, 1
s_waitcnt vmcnt(56)
v_accvgpr_read_b32 v32, a0
v_accvgpr_read_b32 v33, a1
v_accvgpr_read_b32 v34, a2
v_accvgpr_read_b32 v35, a3
v_accvgpr_read_b32 v36, a4
v_accvgpr_read_b32 v37, a5
v_accvgpr_read_b32 v38, a6
v_accvgpr_read_b32 v39, a7
v_accvgpr_read_b32 v40, a8
v_accvgpr_read_b32 v41, a9
v_accvgpr_read_b32 v42, a10
v_accvgpr_read_b32 v43, a11
v_accvgpr_read_b32 v44, a12
v_accvgpr_read_b32 v45, a13
v_accvgpr_read_b32 v46, a14
v_accvgpr_read_b32 v47, a15
v_accvgpr_read_b32 v48, a16
v_accvgpr_read_b32 v49, a17
v_accvgpr_read_b32 v50, a18
v_accvgpr_read_b32 v51, a19
v_accvgpr_read_b32 v52, a20
v_accvgpr_read_b32 v53, a21
v_accvgpr_read_b32 v54, a22
v_accvgpr_read_b32 v55, a23
v_accvgpr_read_b32 v56, a24
v_accvgpr_read_b32 v57, a25
v_accvgpr_read_b32 v58, a26
v_accvgpr_read_b32 v59, a27
v_accvgpr_read_b32 v60, a28
v_accvgpr_read_b32 v61, a29
v_accvgpr_read_b32 v62, a30
v_accvgpr_read_b32 v63, a31
v_perm_b32 v64, v36, v32, s34
v_perm_b32 v65, v44, v40, s34
v_perm_b32 v66, v52, v48, s34
v_perm_b32 v67, v60, v56, s34
v_accvgpr_write_b32 a0, v64
v_accvgpr_write_b32 a1, v65
v_accvgpr_write_b32 a2, v66
v_accvgpr_write_b32 a3, v67
v_perm_b32 v64, v36, v32, s35
v_perm_b32 v65, v44, v40, s35
v_perm_b32 v66, v52, v48, s35
v_perm_b32 v67, v60, v56, s35
v_accvgpr_write_b32 a4, v64
v_accvgpr_write_b32 a5, v65
v_accvgpr_write_b32 a6, v66
v_accvgpr_write_b32 a7, v67
v_perm_b32 v64, v37, v33, s34
v_perm_b32 v65, v45, v41, s34
v_perm_b32 v66, v53, v49, s34
v_perm_b32 v67, v61, v57, s34
v_accvgpr_write_b32 a8, v64
v_accvgpr_write_b32 a9, v65
v_accvgpr_write_b32 a10, v66
v_accvgpr_write_b32 a11, v67
v_perm_b32 v64, v37, v33, s35
v_perm_b32 v65, v45, v41, s35
v_perm_b32 v66, v53, v49, s35
v_perm_b32 v67, v61, v57, s35
v_accvgpr_write_b32 a12, v64
v_accvgpr_write_b32 a13, v65
v_accvgpr_write_b32 a14, v66
v_accvgpr_write_b32 a15, v67
v_perm_b32 v64, v38, v34, s34
v_perm_b32 v65, v46, v42, s34
v_perm_b32 v66, v54, v50, s34
v_perm_b32 v67, v62, v58, s34
v_accvgpr_write_b32 a16, v64
v_accvgpr_write_b32 a17, v65
v_accvgpr_write_b32 a18, v66
v_accvgpr_write_b32 a19, v67
v_perm_b32 v64, v38, v34, s35
v_perm_b32 v65, v46, v42, s35
v_perm_b32 v66, v54, v50, s35
v_perm_b32 v67, v62, v58, s35
v_accvgpr_write_b32 a20, v64
v_accvgpr_write_b32 a21, v65
v_accvgpr_write_b32 a22, v66
v_accvgpr_write_b32 a23, v67
v_perm_b32 v64, v39, v35, s34
v_perm_b32 v65, v47, v43, s34
v_perm_b32 v66, v55, v51, s34
v_perm_b32 v67, v63, v59, s34
v_accvgpr_write_b32 a24, v64
v_accvgpr_write_b32 a25, v65
v_accvgpr_write_b32 a26, v66
v_accvgpr_write_b32 a27, v67
v_perm_b32 v64, v39, v35, s35
v_perm_b32 v65, v47, v43, s35
v_perm_b32 v66, v55, v51, s35
v_perm_b32 v67, v63, v59, s35
v_accvgpr_write_b32 a28, v64
v_accvgpr_write_b32 a29, v65
v_accvgpr_write_b32 a30, v66
v_accvgpr_write_b32 a31, v67
s_waitcnt vmcnt(48)
v_accvgpr_read_b32 v32, a32
v_accvgpr_read_b32 v33, a33
v_accvgpr_read_b32 v34, a34
v_accvgpr_read_b32 v35, a35
v_accvgpr_read_b32 v36, a36
v_accvgpr_read_b32 v37, a37
v_accvgpr_read_b32 v38, a38
v_accvgpr_read_b32 v39, a39
v_accvgpr_read_b32 v40, a40
v_accvgpr_read_b32 v41, a41
v_accvgpr_read_b32 v42, a42
v_accvgpr_read_b32 v43, a43
v_accvgpr_read_b32 v44, a44
v_accvgpr_read_b32 v45, a45
v_accvgpr_read_b32 v46, a46
v_accvgpr_read_b32 v47, a47
v_accvgpr_read_b32 v48, a48
v_accvgpr_read_b32 v49, a49
v_accvgpr_read_b32 v50, a50
v_accvgpr_read_b32 v51, a51
v_accvgpr_read_b32 v52, a52
v_accvgpr_read_b32 v53, a53
v_accvgpr_read_b32 v54, a54
v_accvgpr_read_b32 v55, a55
v_accvgpr_read_b32 v56, a56
v_accvgpr_read_b32 v57, a57
v_accvgpr_read_b32 v58, a58
v_accvgpr_read_b32 v59, a59
v_accvgpr_read_b32 v60, a60
v_accvgpr_read_b32 v61, a61
v_accvgpr_read_b32 v62, a62
v_accvgpr_read_b32 v63, a63
v_perm_b32 v64, v36, v32, s34
v_perm_b32 v65, v44, v40, s34
v_perm_b32 v66, v52, v48, s34
v_perm_b32 v67, v60, v56, s34
v_accvgpr_write_b32 a32, v64
v_accvgpr_write_b32 a33, v65
v_accvgpr_write_b32 a34, v66
v_accvgpr_write_b32 a35, v67
v_perm_b32 v64, v36, v32, s35
v_perm_b32 v65, v44, v40, s35
v_perm_b32 v66, v52, v48, s35
v_perm_b32 v67, v60, v56, s35
v_accvgpr_write_b32 a36, v64
v_accvgpr_write_b32 a37, v65
v_accvgpr_write_b32 a38, v66
v_accvgpr_write_b32 a39, v67
v_perm_b32 v64, v37, v33, s34
v_perm_b32 v65, v45, v41, s34
v_perm_b32 v66, v53, v49, s34
v_perm_b32 v67, v61, v57, s34
v_accvgpr_write_b32 a40, v64
v_accvgpr_write_b32 a41, v65
v_accvgpr_write_b32 a42, v66
v_accvgpr_write_b32 a43, v67
v_perm_b32 v64, v37, v33, s35
v_perm_b32 v65, v45, v41, s35
v_perm_b32 v66, v53, v49, s35
v_perm_b32 v67, v61, v57, s35
v_accvgpr_write_b32 a44, v64
v_accvgpr_write_b32 a45, v65
v_accvgpr_write_b32 a46, v66
v_accvgpr_write_b32 a47, v67
v_perm_b32 v64, v38, v34, s34
v_perm_b32 v65, v46, v42, s34
v_perm_b32 v66, v54, v50, s34
v_perm_b32 v67, v62, v58, s34
v_accvgpr_write_b32 a48, v64
v_accvgpr_write_b32 a49, v65
v_accvgpr_write_b32 a50, v66
v_accvgpr_write_b32 a51, v67
v_perm_b32 v64, v38, v34, s35
v_perm_b32 v65, v46, v42, s35
v_perm_b32 v66, v54, v50, s35
v_perm_b32 v67, v62, v58, s35
v_accvgpr_write_b32 a52, v64
v_accvgpr_write_b32 a53, v65
v_accvgpr_write_b32 a54, v66
v_accvgpr_write_b32 a55, v67
v_perm_b32 v64, v39, v35, s34
v_perm_b32 v65, v47, v43, s34
v_perm_b32 v66, v55, v51, s34
v_perm_b32 v67, v63, v59, s34
v_accvgpr_write_b32 a56, v64
v_accvgpr_write_b32 a57, v65
v_accvgpr_write_b32 a58, v66
v_accvgpr_write_b32 a59, v67
v_perm_b32 v64, v39, v35, s35
v_perm_b32 v65, v47, v43, s35
v_perm_b32 v66, v55, v51, s35
v_perm_b32 v67, v63, v59, s35
v_accvgpr_write_b32 a60, v64
v_accvgpr_write_b32 a61, v65
v_accvgpr_write_b32 a62, v66
v_accvgpr_write_b32 a63, v67
s_waitcnt vmcnt(40)
v_accvgpr_read_b32 v32, a64
v_accvgpr_read_b32 v33, a65
v_accvgpr_read_b32 v34, a66
v_accvgpr_read_b32 v35, a67
v_accvgpr_read_b32 v36, a68
v_accvgpr_read_b32 v37, a69
v_accvgpr_read_b32 v38, a70
v_accvgpr_read_b32 v39, a71
v_accvgpr_read_b32 v40, a72
v_accvgpr_read_b32 v41, a73
v_accvgpr_read_b32 v42, a74
v_accvgpr_read_b32 v43, a75
v_accvgpr_read_b32 v44, a76
v_accvgpr_read_b32 v45, a77
v_accvgpr_read_b32 v46, a78
v_accvgpr_read_b32 v47, a79
v_accvgpr_read_b32 v48, a80
v_accvgpr_read_b32 v49, a81
v_accvgpr_read_b32 v50, a82
v_accvgpr_read_b32 v51, a83
v_accvgpr_read_b32 v52, a84
v_accvgpr_read_b32 v53, a85
v_accvgpr_read_b32 v54, a86
v_accvgpr_read_b32 v55, a87
v_accvgpr_read_b32 v56, a88
v_accvgpr_read_b32 v57, a89
v_accvgpr_read_b32 v58, a90
v_accvgpr_read_b32 v59, a91
v_accvgpr_read_b32 v60, a92
v_accvgpr_read_b32 v61, a93
v_accvgpr_read_b32 v62, a94
v_accvgpr_read_b32 v63, a95
v_perm_b32 v64, v36, v32, s34
v_perm_b32 v65, v44, v40, s34
v_perm_b32 v66, v52, v48, s34
v_perm_b32 v67, v60, v56, s34
v_accvgpr_write_b32 a64, v64
v_accvgpr_write_b32 a65, v65
v_accvgpr_write_b32 a66, v66
v_accvgpr_write_b32 a67, v67
v_perm_b32 v64, v36, v32, s35
v_perm_b32 v65, v44, v40, s35
v_perm_b32 v66, v52, v48, s35
v_perm_b32 v67, v60, v56, s35
v_accvgpr_write_b32 a68, v64
v_accvgpr_write_b32 a69, v65
v_accvgpr_write_b32 a70, v66
v_accvgpr_write_b32 a71, v67
v_perm_b32 v64, v37, v33, s34
v_perm_b32 v65, v45, v41, s34
v_perm_b32 v66, v53, v49, s34
v_perm_b32 v67, v61, v57, s34
v_accvgpr_write_b32 a72, v64
v_accvgpr_write_b32 a73, v65
v_accvgpr_write_b32 a74, v66
v_accvgpr_write_b32 a75, v67
v_perm_b32 v64, v37, v33, s35
v_perm_b32 v65, v45, v41, s35
v_perm_b32 v66, v53, v49, s35
v_perm_b32 v67, v61, v57, s35
v_accvgpr_write_b32 a76, v64
v_accvgpr_write_b32 a77, v65
v_accvgpr_write_b32 a78, v66
v_accvgpr_write_b32 a79, v67
v_perm_b32 v64, v38, v34, s34
v_perm_b32 v65, v46, v42, s34
v_perm_b32 v66, v54, v50, s34
v_perm_b32 v67, v62, v58, s34
v_accvgpr_write_b32 a80, v64
v_accvgpr_write_b32 a81, v65
v_accvgpr_write_b32 a82, v66
v_accvgpr_write_b32 a83, v67
v_perm_b32 v64, v38, v34, s35
v_perm_b32 v65, v46, v42, s35
v_perm_b32 v66, v54, v50, s35
v_perm_b32 v67, v62, v58, s35
v_accvgpr_write_b32 a84, v64
v_accvgpr_write_b32 a85, v65
v_accvgpr_write_b32 a86, v66
v_accvgpr_write_b32 a87, v67
v_perm_b32 v64, v39, v35, s34
v_perm_b32 v65, v47, v43, s34
v_perm_b32 v66, v55, v51, s34
v_perm_b32 v67, v63, v59, s34
v_accvgpr_write_b32 a88, v64
v_accvgpr_write_b32 a89, v65
v_accvgpr_write_b32 a90, v66
v_accvgpr_write_b32 a91, v67
v_perm_b32 v64, v39, v35, s35
v_perm_b32 v65, v47, v43, s35
v_perm_b32 v66, v55, v51, s35
v_perm_b32 v67, v63, v59, s35
v_accvgpr_write_b32 a92, v64
v_accvgpr_write_b32 a93, v65
v_accvgpr_write_b32 a94, v66
v_accvgpr_write_b32 a95, v67
s_waitcnt vmcnt(32)
v_accvgpr_read_b32 v32, a96
v_accvgpr_read_b32 v33, a97
v_accvgpr_read_b32 v34, a98
v_accvgpr_read_b32 v35, a99
v_accvgpr_read_b32 v36, a100
v_accvgpr_read_b32 v37, a101
v_accvgpr_read_b32 v38, a102
v_accvgpr_read_b32 v39, a103
v_accvgpr_read_b32 v40, a104
v_accvgpr_read_b32 v41, a105
v_accvgpr_read_b32 v42, a106
v_accvgpr_read_b32 v43, a107
v_accvgpr_read_b32 v44, a108
v_accvgpr_read_b32 v45, a109
v_accvgpr_read_b32 v46, a110
v_accvgpr_read_b32 v47, a111
v_accvgpr_read_b32 v48, a112
v_accvgpr_read_b32 v49, a113
v_accvgpr_read_b32 v50, a114
v_accvgpr_read_b32 v51, a115
v_accvgpr_read_b32 v52, a116
v_accvgpr_read_b32 v53, a117
v_accvgpr_read_b32 v54, a118
v_accvgpr_read_b32 v55, a119
v_accvgpr_read_b32 v56, a120
v_accvgpr_read_b32 v57, a121
v_accvgpr_read_b32 v58, a122
v_accvgpr_read_b32 v59, a123
v_accvgpr_read_b32 v60, a124
v_accvgpr_read_b32 v61, a125
v_accvgpr_read_b32 v62, a126
v_accvgpr_read_b32 v63, a127
v_perm_b32 v64, v36, v32, s34
v_perm_b32 v65, v44, v40, s34
v_perm_b32 v66, v52, v48, s34
v_perm_b32 v67, v60, v56, s34
v_accvgpr_write_b32 a96, v64
v_accvgpr_write_b32 a97, v65
v_accvgpr_write_b32 a98, v66
v_accvgpr_write_b32 a99, v67
v_perm_b32 v64, v36, v32, s35
v_perm_b32 v65, v44, v40, s35
v_perm_b32 v66, v52, v48, s35
v_perm_b32 v67, v60, v56, s35
v_accvgpr_write_b32 a100, v64
v_accvgpr_write_b32 a101, v65
v_accvgpr_write_b32 a102, v66
v_accvgpr_write_b32 a103, v67
v_perm_b32 v64, v37, v33, s34
v_perm_b32 v65, v45, v41, s34
v_perm_b32 v66, v53, v49, s34
v_perm_b32 v67, v61, v57, s34
v_accvgpr_write_b32 a104, v64
v_accvgpr_write_b32 a105, v65
v_accvgpr_write_b32 a106, v66
v_accvgpr_write_b32 a107, v67
v_perm_b32 v64, v37, v33, s35
v_perm_b32 v65, v45, v41, s35
v_perm_b32 v66, v53, v49, s35
v_perm_b32 v67, v61, v57, s35
v_accvgpr_write_b32 a108, v64
v_accvgpr_write_b32 a109, v65
v_accvgpr_write_b32 a110, v66
v_accvgpr_write_b32 a111, v67
v_perm_b32 v64, v38, v34, s34
v_perm_b32 v65, v46, v42, s34
v_perm_b32 v66, v54, v50, s34
v_perm_b32 v67, v62, v58, s34
v_accvgpr_write_b32 a112, v64
v_accvgpr_write_b32 a113, v65
v_accvgpr_write_b32 a114, v66
v_accvgpr_write_b32 a115, v67
v_perm_b32 v64, v38, v34, s35
v_perm_b32 v65, v46, v42, s35
v_perm_b32 v66, v54, v50, s35
v_perm_b32 v67, v62, v58, s35
v_accvgpr_write_b32 a116, v64
v_accvgpr_write_b32 a117, v65
v_accvgpr_write_b32 a118, v66
v_accvgpr_write_b32 a119, v67
v_perm_b32 v64, v39, v35, s34
v_perm_b32 v65, v47, v43, s34
v_perm_b32 v66, v55, v51, s34
v_perm_b32 v67, v63, v59, s34
v_accvgpr_write_b32 a120, v64
v_accvgpr_write_b32 a121, v65
v_accvgpr_write_b32 a122, v66
v_accvgpr_write_b32 a123, v67
v_perm_b32 v64, v39, v35, s35
v_perm_b32 v65, v47, v43, s35
v_perm_b32 v66, v55, v51, s35
v_perm_b32 v67, v63, v59, s35
v_accvgpr_write_b32 a124, v64
v_accvgpr_write_b32 a125, v65
v_accvgpr_write_b32 a126, v66
v_accvgpr_write_b32 a127, v67
s_waitcnt vmcnt(24)
v_accvgpr_read_b32 v32, a128
v_accvgpr_read_b32 v33, a129
v_accvgpr_read_b32 v34, a130
v_accvgpr_read_b32 v35, a131
v_accvgpr_read_b32 v36, a132
v_accvgpr_read_b32 v37, a133
v_accvgpr_read_b32 v38, a134
v_accvgpr_read_b32 v39, a135
v_accvgpr_read_b32 v40, a136
v_accvgpr_read_b32 v41, a137
v_accvgpr_read_b32 v42, a138
v_accvgpr_read_b32 v43, a139
v_accvgpr_read_b32 v44, a140
v_accvgpr_read_b32 v45, a141
v_accvgpr_read_b32 v46, a142
v_accvgpr_read_b32 v47, a143
v_accvgpr_read_b32 v48, a144
v_accvgpr_read_b32 v49, a145
v_accvgpr_read_b32 v50, a146
v_accvgpr_read_b32 v51, a147
v_accvgpr_read_b32 v52, a148
v_accvgpr_read_b32 v53, a149
v_accvgpr_read_b32 v54, a150
v_accvgpr_read_b32 v55, a151
v_accvgpr_read_b32 v56, a152
v_accvgpr_read_b32 v57, a153
v_accvgpr_read_b32 v58, a154
v_accvgpr_read_b32 v59, a155
v_accvgpr_read_b32 v60, a156
v_accvgpr_read_b32 v61, a157
v_accvgpr_read_b32 v62, a158
v_accvgpr_read_b32 v63, a159
v_perm_b32 v64, v36, v32, s34
v_perm_b32 v65, v44, v40, s34
v_perm_b32 v66, v52, v48, s34
v_perm_b32 v67, v60, v56, s34
v_accvgpr_write_b32 a128, v64
v_accvgpr_write_b32 a129, v65
v_accvgpr_write_b32 a130, v66
v_accvgpr_write_b32 a131, v67
v_perm_b32 v64, v36, v32, s35
v_perm_b32 v65, v44, v40, s35
v_perm_b32 v66, v52, v48, s35
v_perm_b32 v67, v60, v56, s35
v_accvgpr_write_b32 a132, v64
v_accvgpr_write_b32 a133, v65
v_accvgpr_write_b32 a134, v66
v_accvgpr_write_b32 a135, v67
v_perm_b32 v64, v37, v33, s34
v_perm_b32 v65, v45, v41, s34
v_perm_b32 v66, v53, v49, s34
v_perm_b32 v67, v61, v57, s34
v_accvgpr_write_b32 a136, v64
v_accvgpr_write_b32 a137, v65
v_accvgpr_write_b32 a138, v66
v_accvgpr_write_b32 a139, v67
v_perm_b32 v64, v37, v33, s35
v_perm_b32 v65, v45, v41, s35
v_perm_b32 v66, v53, v49, s35
v_perm_b32 v67, v61, v57, s35
v_accvgpr_write_b32 a140, v64
v_accvgpr_write_b32 a141, v65
v_accvgpr_write_b32 a142, v66
v_accvgpr_write_b32 a143, v67
v_perm_b32 v64, v38, v34, s34
v_perm_b32 v65, v46, v42, s34
v_perm_b32 v66, v54, v50, s34
v_perm_b32 v67, v62, v58, s34
v_accvgpr_write_b32 a144, v64
v_accvgpr_write_b32 a145, v65
v_accvgpr_write_b32 a146, v66
v_accvgpr_write_b32 a147, v67
v_perm_b32 v64, v38, v34, s35
v_perm_b32 v65, v46, v42, s35
v_perm_b32 v66, v54, v50, s35
v_perm_b32 v67, v62, v58, s35
v_accvgpr_write_b32 a148, v64
v_accvgpr_write_b32 a149, v65
v_accvgpr_write_b32 a150, v66
v_accvgpr_write_b32 a151, v67
v_perm_b32 v64, v39, v35, s34
v_perm_b32 v65, v47, v43, s34
v_perm_b32 v66, v55, v51, s34
v_perm_b32 v67, v63, v59, s34
v_accvgpr_write_b32 a152, v64
v_accvgpr_write_b32 a153, v65
v_accvgpr_write_b32 a154, v66
v_accvgpr_write_b32 a155, v67
v_perm_b32 v64, v39, v35, s35
v_perm_b32 v65, v47, v43, s35
v_perm_b32 v66, v55, v51, s35
v_perm_b32 v67, v63, v59, s35
v_accvgpr_write_b32 a156, v64
v_accvgpr_write_b32 a157, v65
v_accvgpr_write_b32 a158, v66
v_accvgpr_write_b32 a159, v67
s_waitcnt vmcnt(16)
v_accvgpr_read_b32 v32, a160
v_accvgpr_read_b32 v33, a161
v_accvgpr_read_b32 v34, a162
v_accvgpr_read_b32 v35, a163
v_accvgpr_read_b32 v36, a164
v_accvgpr_read_b32 v37, a165
v_accvgpr_read_b32 v38, a166
v_accvgpr_read_b32 v39, a167
v_accvgpr_read_b32 v40, a168
v_accvgpr_read_b32 v41, a169
v_accvgpr_read_b32 v42, a170
v_accvgpr_read_b32 v43, a171
v_accvgpr_read_b32 v44, a172
v_accvgpr_read_b32 v45, a173
v_accvgpr_read_b32 v46, a174
v_accvgpr_read_b32 v47, a175
v_accvgpr_read_b32 v48, a176
v_accvgpr_read_b32 v49, a177
v_accvgpr_read_b32 v50, a178
v_accvgpr_read_b32 v51, a179
v_accvgpr_read_b32 v52, a180
v_accvgpr_read_b32 v53, a181
v_accvgpr_read_b32 v54, a182
v_accvgpr_read_b32 v55, a183
v_accvgpr_read_b32 v56, a184
v_accvgpr_read_b32 v57, a185
v_accvgpr_read_b32 v58, a186
v_accvgpr_read_b32 v59, a187
v_accvgpr_read_b32 v60, a188
v_accvgpr_read_b32 v61, a189
v_accvgpr_read_b32 v62, a190
v_accvgpr_read_b32 v63, a191
v_perm_b32 v64, v36, v32, s34
v_perm_b32 v65, v44, v40, s34
v_perm_b32 v66, v52, v48, s34
v_perm_b32 v67, v60, v56, s34
v_accvgpr_write_b32 a160, v64
v_accvgpr_write_b32 a161, v65
v_accvgpr_write_b32 a162, v66
v_accvgpr_write_b32 a163, v67
v_perm_b32 v64, v36, v32, s35
v_perm_b32 v65, v44, v40, s35
v_perm_b32 v66, v52, v48, s35
v_perm_b32 v67, v60, v56, s35
v_accvgpr_write_b32 a164, v64
v_accvgpr_write_b32 a165, v65
v_accvgpr_write_b32 a166, v66
v_accvgpr_write_b32 a167, v67
v_perm_b32 v64, v37, v33, s34
v_perm_b32 v65, v45, v41, s34
v_perm_b32 v66, v53, v49, s34
v_perm_b32 v67, v61, v57, s34
v_accvgpr_write_b32 a168, v64
v_accvgpr_write_b32 a169, v65
v_accvgpr_write_b32 a170, v66
v_accvgpr_write_b32 a171, v67
v_perm_b32 v64, v37, v33, s35
v_perm_b32 v65, v45, v41, s35
v_perm_b32 v66, v53, v49, s35
v_perm_b32 v67, v61, v57, s35
v_accvgpr_write_b32 a172, v64
v_accvgpr_write_b32 a173, v65
v_accvgpr_write_b32 a174, v66
v_accvgpr_write_b32 a175, v67
v_perm_b32 v64, v38, v34, s34
v_perm_b32 v65, v46, v42, s34
v_perm_b32 v66, v54, v50, s34
v_perm_b32 v67, v62, v58, s34
v_accvgpr_write_b32 a176, v64
v_accvgpr_write_b32 a177, v65
v_accvgpr_write_b32 a178, v66
v_accvgpr_write_b32 a179, v67
v_perm_b32 v64, v38, v34, s35
v_perm_b32 v65, v46, v42, s35
v_perm_b32 v66, v54, v50, s35
v_perm_b32 v67, v62, v58, s35
v_accvgpr_write_b32 a180, v64
v_accvgpr_write_b32 a181, v65
v_accvgpr_write_b32 a182, v66
v_accvgpr_write_b32 a183, v67
v_perm_b32 v64, v39, v35, s34
v_perm_b32 v65, v47, v43, s34
v_perm_b32 v66, v55, v51, s34
v_perm_b32 v67, v63, v59, s34
v_accvgpr_write_b32 a184, v64
v_accvgpr_write_b32 a185, v65
v_accvgpr_write_b32 a186, v66
v_accvgpr_write_b32 a187, v67
v_perm_b32 v64, v39, v35, s35
v_perm_b32 v65, v47, v43, s35
v_perm_b32 v66, v55, v51, s35
v_perm_b32 v67, v63, v59, s35
v_accvgpr_write_b32 a188, v64
v_accvgpr_write_b32 a189, v65
v_accvgpr_write_b32 a190, v66
v_accvgpr_write_b32 a191, v67
s_waitcnt vmcnt(8)
v_accvgpr_read_b32 v32, a192
v_accvgpr_read_b32 v33, a193
v_accvgpr_read_b32 v34, a194
v_accvgpr_read_b32 v35, a195
v_accvgpr_read_b32 v36, a196
v_accvgpr_read_b32 v37, a197
v_accvgpr_read_b32 v38, a198
v_accvgpr_read_b32 v39, a199
v_accvgpr_read_b32 v40, a200
v_accvgpr_read_b32 v41, a201
v_accvgpr_read_b32 v42, a202
v_accvgpr_read_b32 v43, a203
v_accvgpr_read_b32 v44, a204
v_accvgpr_read_b32 v45, a205
v_accvgpr_read_b32 v46, a206
v_accvgpr_read_b32 v47, a207
v_accvgpr_read_b32 v48, a208
v_accvgpr_read_b32 v49, a209
v_accvgpr_read_b32 v50, a210
v_accvgpr_read_b32 v51, a211
v_accvgpr_read_b32 v52, a212
v_accvgpr_read_b32 v53, a213
v_accvgpr_read_b32 v54, a214
v_accvgpr_read_b32 v55, a215
v_accvgpr_read_b32 v56, a216
v_accvgpr_read_b32 v57, a217
v_accvgpr_read_b32 v58, a218
v_accvgpr_read_b32 v59, a219
v_accvgpr_read_b32 v60, a220
v_accvgpr_read_b32 v61, a221
v_accvgpr_read_b32 v62, a222
v_accvgpr_read_b32 v63, a223
v_perm_b32 v64, v36, v32, s34
v_perm_b32 v65, v44, v40, s34
v_perm_b32 v66, v52, v48, s34
v_perm_b32 v67, v60, v56, s34
v_accvgpr_write_b32 a192, v64
v_accvgpr_write_b32 a193, v65
v_accvgpr_write_b32 a194, v66
v_accvgpr_write_b32 a195, v67
v_perm_b32 v64, v36, v32, s35
v_perm_b32 v65, v44, v40, s35
v_perm_b32 v66, v52, v48, s35
v_perm_b32 v67, v60, v56, s35
v_accvgpr_write_b32 a196, v64
v_accvgpr_write_b32 a197, v65
v_accvgpr_write_b32 a198, v66
v_accvgpr_write_b32 a199, v67
v_perm_b32 v64, v37, v33, s34
v_perm_b32 v65, v45, v41, s34
v_perm_b32 v66, v53, v49, s34
v_perm_b32 v67, v61, v57, s34
v_accvgpr_write_b32 a200, v64
v_accvgpr_write_b32 a201, v65
v_accvgpr_write_b32 a202, v66
v_accvgpr_write_b32 a203, v67
v_perm_b32 v64, v37, v33, s35
v_perm_b32 v65, v45, v41, s35
v_perm_b32 v66, v53, v49, s35
v_perm_b32 v67, v61, v57, s35
v_accvgpr_write_b32 a204, v64
v_accvgpr_write_b32 a205, v65
v_accvgpr_write_b32 a206, v66
v_accvgpr_write_b32 a207, v67
v_perm_b32 v64, v38, v34, s34
v_perm_b32 v65, v46, v42, s34
v_perm_b32 v66, v54, v50, s34
v_perm_b32 v67, v62, v58, s34
v_accvgpr_write_b32 a208, v64
v_accvgpr_write_b32 a209, v65
v_accvgpr_write_b32 a210, v66
v_accvgpr_write_b32 a211, v67
v_perm_b32 v64, v38, v34, s35
v_perm_b32 v65, v46, v42, s35
v_perm_b32 v66, v54, v50, s35
v_perm_b32 v67, v62, v58, s35
v_accvgpr_write_b32 a212, v64
v_accvgpr_write_b32 a213, v65
v_accvgpr_write_b32 a214, v66
v_accvgpr_write_b32 a215, v67
v_perm_b32 v64, v39, v35, s34
v_perm_b32 v65, v47, v43, s34
v_perm_b32 v66, v55, v51, s34
v_perm_b32 v67, v63, v59, s34
v_accvgpr_write_b32 a216, v64
v_accvgpr_write_b32 a217, v65
v_accvgpr_write_b32 a218, v66
v_accvgpr_write_b32 a219, v67
v_perm_b32 v64, v39, v35, s35
v_perm_b32 v65, v47, v43, s35
v_perm_b32 v66, v55, v51, s35
v_perm_b32 v67, v63, v59, s35
v_accvgpr_write_b32 a220, v64
v_accvgpr_write_b32 a221, v65
v_accvgpr_write_b32 a222, v66
v_accvgpr_write_b32 a223, v67
s_waitcnt vmcnt(0)
v_accvgpr_read_b32 v32, a224
v_accvgpr_read_b32 v33, a225
v_accvgpr_read_b32 v34, a226
v_accvgpr_read_b32 v35, a227
v_accvgpr_read_b32 v36, a228
v_accvgpr_read_b32 v37, a229
v_accvgpr_read_b32 v38, a230
v_accvgpr_read_b32 v39, a231
v_accvgpr_read_b32 v40, a232
v_accvgpr_read_b32 v41, a233
v_accvgpr_read_b32 v42, a234
v_accvgpr_read_b32 v43, a235
v_accvgpr_read_b32 v44, a236
v_accvgpr_read_b32 v45, a237
v_accvgpr_read_b32 v46, a238
v_accvgpr_read_b32 v47, a239
v_accvgpr_read_b32 v48, a240
v_accvgpr_read_b32 v49, a241
v_accvgpr_read_b32 v50, a242
v_accvgpr_read_b32 v51, a243
v_accvgpr_read_b32 v52, a244
v_accvgpr_read_b32 v53, a245
v_accvgpr_read_b32 v54, a246
v_accvgpr_read_b32 v55, a247
v_accvgpr_read_b32 v56, a248
v_accvgpr_read_b32 v57, a249
v_accvgpr_read_b32 v58, a250
v_accvgpr_read_b32 v59, a251
v_accvgpr_read_b32 v60, a252
v_accvgpr_read_b32 v61, a253
v_accvgpr_read_b32 v62, a254
v_accvgpr_read_b32 v63, a255
v_perm_b32 v64, v36, v32, s34
v_perm_b32 v65, v44, v40, s34
v_perm_b32 v66, v52, v48, s34
v_perm_b32 v67, v60, v56, s34
v_accvgpr_write_b32 a224, v64
v_accvgpr_write_b32 a225, v65
v_accvgpr_write_b32 a226, v66
v_accvgpr_write_b32 a227, v67
v_perm_b32 v64, v36, v32, s35
v_perm_b32 v65, v44, v40, s35
v_perm_b32 v66, v52, v48, s35
v_perm_b32 v67, v60, v56, s35
v_accvgpr_write_b32 a228, v64
v_accvgpr_write_b32 a229, v65
v_accvgpr_write_b32 a230, v66
v_accvgpr_write_b32 a231, v67
v_perm_b32 v64, v37, v33, s34
v_perm_b32 v65, v45, v41, s34
v_perm_b32 v66, v53, v49, s34
v_perm_b32 v67, v61, v57, s34
v_accvgpr_write_b32 a232, v64
v_accvgpr_write_b32 a233, v65
v_accvgpr_write_b32 a234, v66
v_accvgpr_write_b32 a235, v67
v_perm_b32 v64, v37, v33, s35
v_perm_b32 v65, v45, v41, s35
v_perm_b32 v66, v53, v49, s35
v_perm_b32 v67, v61, v57, s35
v_accvgpr_write_b32 a236, v64
v_accvgpr_write_b32 a237, v65
v_accvgpr_write_b32 a238, v66
v_accvgpr_write_b32 a239, v67
v_perm_b32 v64, v38, v34, s34
v_perm_b32 v65, v46, v42, s34
v_perm_b32 v66, v54, v50, s34
v_perm_b32 v67, v62, v58, s34
v_accvgpr_write_b32 a240, v64
v_accvgpr_write_b32 a241, v65
v_accvgpr_write_b32 a242, v66
v_accvgpr_write_b32 a243, v67
v_perm_b32 v64, v38, v34, s35
v_perm_b32 v65, v46, v42, s35
v_perm_b32 v66, v54, v50, s35
v_perm_b32 v67, v62, v58, s35
v_accvgpr_write_b32 a244, v64
v_accvgpr_write_b32 a245, v65
v_accvgpr_write_b32 a246, v66
v_accvgpr_write_b32 a247, v67
v_perm_b32 v64, v39, v35, s34
v_perm_b32 v65, v47, v43, s34
v_perm_b32 v66, v55, v51, s34
v_perm_b32 v67, v63, v59, s34
v_accvgpr_write_b32 a248, v64
v_accvgpr_write_b32 a249, v65
v_accvgpr_write_b32 a250, v66
v_accvgpr_write_b32 a251, v67
v_perm_b32 v64, v39, v35, s35
v_perm_b32 v65, v47, v43, s35
v_perm_b32 v66, v55, v51, s35
v_perm_b32 v67, v63, v59, s35
v_accvgpr_write_b32 a252, v64
v_accvgpr_write_b32 a253, v65
v_accvgpr_write_b32 a254, v66
v_accvgpr_write_b32 a255, v67
s_and_b32 s31, s28, 15
s_lshr_b64 s[28:29], s[28:29], 4
s_mul_i32 s36, s31, 0x68000
s_add_u32 s36, s36, 0x81000
s_lshl_b32 s37, s31, 14
s_add_u32 s37, s37, 0x41000
buffer_load_dwordx4 v[32:35], v3, s[24:27], s36 offen sc1
buffer_load_dwordx4 v[36:39], v4, s[24:27], s36 offen sc1
buffer_load_dwordx4 v[40:43], v5, s[24:27], s36 offen sc1
buffer_load_dwordx4 v[44:47], v6, s[24:27], s36 offen sc1
buffer_load_dwordx4 v[48:51], v7, s[24:27], s36 offen sc1
buffer_load_dwordx4 v[52:55], v8, s[24:27], s36 offen sc1
buffer_load_dwordx4 v[56:59], v9, s[24:27], s36 offen sc1
buffer_load_dwordx4 v[60:63], v10, s[24:27], s37 offen sc1
buffer_load_dwordx4 v[64:67], v10, s[24:27], s37 offen offset:64 sc1
buffer_load_dwordx4 v[68:71], v10, s[24:27], s37 offen offset:128 sc1
buffer_load_dwordx4 v[72:75], v10, s[24:27], s37 offen offset:192 sc1
buffer_load_dwordx4 v[76:79], v10, s[24:27], s37 offen offset:256 sc1
buffer_load_dwordx4 v[80:83], v10, s[24:27], s37 offen offset:320 sc1
buffer_load_dwordx4 v[84:87], v11, s[24:27], s37 offen sc1
s_waitcnt vmcnt(0)
Lwloop_1_%=:
s_lshl_b32 s38, s31, 15
s_add_u32 s38, s38, 0x701000
s_and_b32 s49, s30, 1
s_lshl_b32 s49, s49, 9
s_mov_b32 s33, 0
s_waitcnt vmcnt(1)
v_bitop3_b32 v144, v33, v35, s12 bitop3:0x7e
v_bitop3_b32 v145, v61, v63, s12 bitop3:0x7e
v_bitop3_b32 v146, v37, v39, s12 bitop3:0x7e
v_bitop3_b32 v147, v65, v67, s12 bitop3:0x7e
v_bitop3_b32 v148, v41, v43, s12 bitop3:0x7e
v_bitop3_b32 v149, v69, v71, s12 bitop3:0x7e
v_bitop3_b32 v150, v45, v47, s12 bitop3:0x7e
v_bitop3_b32 v151, v73, v75, s12 bitop3:0x7e
v_bitop3_b32 v152, v49, v51, s12 bitop3:0x7e
v_bitop3_b32 v153, v77, v79, s12 bitop3:0x7e
v_bitop3_b32 v154, v53, v55, s12 bitop3:0x7e
v_bitop3_b32 v155, v81, v83, s12 bitop3:0x7e
v_bitop3_b32 v156, v57, v59, s12 bitop3:0x7e
v_bitop3_b32 v157, v85, v87, s12 bitop3:0x7e
v_or3_b32 v144, v144, v145, v146
v_or3_b32 v147, v147, v148, v149
v_or3_b32 v150, v150, v151, v152
v_or3_b32 v153, v153, v154, v155
v_or_b32 v156, v156, v157
v_or3_b32 v144, v144, v147, v150
v_or_b32 v153, v153, v156
v_or_b32 v144, v144, v153
v_cmp_ne_u32 vcc, 0, v144
s_cbranch_vccz LwA_4_%=
s_lshr_b32 s61, s30, 2
s_mov_b32 s62, 0x1f00008f
s_cmp_eq_u32 s61, 1
s_cselect_b32 s62, 0x2f002f00, s62
s_cmp_eq_u32 s61, 2
s_cselect_b32 s62, 0x2f002f00, s62
s_cmp_eq_u32 s61, 3
s_cselect_b32 s62, 0x43352300, s62
s_and_b32 s61, s30, 3
s_lshl_b32 s61, s61, 3
s_lshr_b32 s62, s62, s61
s_and_b32 s62, s62, 0xff
s_lshl_b32 s62, s62, 3
Lwcg_8_%=:
s_memrealtime s[58:59]
s_waitcnt lgkmcnt(0)
s_sub_u32 s61, s58, s60
s_cmp_ge_u32 s61, s62
s_cbranch_scc1 Lwcx_9_%=
s_sleep 3
s_branch Lwcg_8_%=
Lwcx_9_%=:
buffer_load_dwordx4 v[32:35], v3, s[24:27], s36 offen sc1
buffer_load_dwordx4 v[36:39], v4, s[24:27], s36 offen sc1
buffer_load_dwordx4 v[40:43], v5, s[24:27], s36 offen sc1
buffer_load_dwordx4 v[44:47], v6, s[24:27], s36 offen sc1
buffer_load_dwordx4 v[48:51], v7, s[24:27], s36 offen sc1
buffer_load_dwordx4 v[52:55], v8, s[24:27], s36 offen sc1
buffer_load_dwordx4 v[56:59], v9, s[24:27], s36 offen sc1
buffer_load_dwordx4 v[60:63], v10, s[24:27], s37 offen sc1
buffer_load_dwordx4 v[64:67], v10, s[24:27], s37 offen offset:64 sc1
buffer_load_dwordx4 v[68:71], v10, s[24:27], s37 offen offset:128 sc1
buffer_load_dwordx4 v[72:75], v10, s[24:27], s37 offen offset:192 sc1
buffer_load_dwordx4 v[76:79], v10, s[24:27], s37 offen offset:256 sc1
buffer_load_dwordx4 v[80:83], v10, s[24:27], s37 offen offset:320 sc1
buffer_load_dwordx4 v[84:87], v11, s[24:27], s37 offen sc1
s_sleep 8
Lwl0_2_%=:
buffer_load_dwordx4 v[88:91], v3, s[24:27], s36 offen sc1
buffer_load_dwordx4 v[92:95], v4, s[24:27], s36 offen sc1
buffer_load_dwordx4 v[96:99], v5, s[24:27], s36 offen sc1
buffer_load_dwordx4 v[100:103], v6, s[24:27], s36 offen sc1
buffer_load_dwordx4 v[104:107], v7, s[24:27], s36 offen sc1
buffer_load_dwordx4 v[108:111], v8, s[24:27], s36 offen sc1
buffer_load_dwordx4 v[112:115], v9, s[24:27], s36 offen sc1
buffer_load_dwordx4 v[116:119], v10, s[24:27], s37 offen sc1
buffer_load_dwordx4 v[120:123], v10, s[24:27], s37 offen offset:64 sc1
buffer_load_dwordx4 v[124:127], v10, s[24:27], s37 offen offset:128 sc1
buffer_load_dwordx4 v[128:131], v10, s[24:27], s37 offen offset:192 sc1
buffer_load_dwordx4 v[132:135], v10, s[24:27], s37 offen offset:256 sc1
buffer_load_dwordx4 v[136:139], v10, s[24:27], s37 offen offset:320 sc1
buffer_load_dwordx4 v[140:143], v11, s[24:27], s37 offen sc1
s_add_u32 s33, s33, 1
s_waitcnt vmcnt(14)
v_bitop3_b32 v144, v33, v35, s12 bitop3:0x7e
v_bitop3_b32 v145, v61, v63, s12 bitop3:0x7e
v_bitop3_b32 v146, v37, v39, s12 bitop3:0x7e
v_bitop3_b32 v147, v65, v67, s12 bitop3:0x7e
v_bitop3_b32 v148, v41, v43, s12 bitop3:0x7e
v_bitop3_b32 v149, v69, v71, s12 bitop3:0x7e
v_bitop3_b32 v150, v45, v47, s12 bitop3:0x7e
v_bitop3_b32 v151, v73, v75, s12 bitop3:0x7e
v_bitop3_b32 v152, v49, v51, s12 bitop3:0x7e
v_bitop3_b32 v153, v77, v79, s12 bitop3:0x7e
v_bitop3_b32 v154, v53, v55, s12 bitop3:0x7e
v_bitop3_b32 v155, v81, v83, s12 bitop3:0x7e
v_bitop3_b32 v156, v57, v59, s12 bitop3:0x7e
v_bitop3_b32 v157, v85, v87, s12 bitop3:0x7e
v_or3_b32 v144, v144, v145, v146
v_or3_b32 v147, v147, v148, v149
v_or3_b32 v150, v150, v151, v152
v_or3_b32 v153, v153, v154, v155
v_or_b32 v156, v156, v157
v_or3_b32 v144, v144, v147, v150
v_or_b32 v153, v153, v156
v_or_b32 v144, v144, v153
v_cmp_ne_u32 vcc, 0, v144
s_cbranch_vccz LwA_4_%=
buffer_load_dwordx4 v[32:35], v3, s[24:27], s36 offen sc1
buffer_load_dwordx4 v[36:39], v4, s[24:27], s36 offen sc1
buffer_load_dwordx4 v[40:43], v5, s[24:27], s36 offen sc1
buffer_load_dwordx4 v[44:47], v6, s[24:27], s36 offen sc1
buffer_load_dwordx4 v[48:51], v7, s[24:27], s36 offen sc1
buffer_load_dwordx4 v[52:55], v8, s[24:27], s36 offen sc1
buffer_load_dwordx4 v[56:59], v9, s[24:27], s36 offen sc1
buffer_load_dwordx4 v[60:63], v10, s[24:27], s37 offen sc1
buffer_load_dwordx4 v[64:67], v10, s[24:27], s37 offen offset:64 sc1
buffer_load_dwordx4 v[68:71], v10, s[24:27], s37 offen offset:128 sc1
buffer_load_dwordx4 v[72:75], v10, s[24:27], s37 offen offset:192 sc1
buffer_load_dwordx4 v[76:79], v10, s[24:27], s37 offen offset:256 sc1
buffer_load_dwordx4 v[80:83], v10, s[24:27], s37 offen offset:320 sc1
buffer_load_dwordx4 v[84:87], v11, s[24:27], s37 offen sc1
s_waitcnt vmcnt(14)
v_bitop3_b32 v144, v89, v91, s12 bitop3:0x7e
v_bitop3_b32 v145, v117, v119, s12 bitop3:0x7e
v_bitop3_b32 v146, v93, v95, s12 bitop3:0x7e
v_bitop3_b32 v147, v121, v123, s12 bitop3:0x7e
v_bitop3_b32 v148, v97, v99, s12 bitop3:0x7e
v_bitop3_b32 v149, v125, v127, s12 bitop3:0x7e
v_bitop3_b32 v150, v101, v103, s12 bitop3:0x7e
v_bitop3_b32 v151, v129, v131, s12 bitop3:0x7e
v_bitop3_b32 v152, v105, v107, s12 bitop3:0x7e
v_bitop3_b32 v153, v133, v135, s12 bitop3:0x7e
v_bitop3_b32 v154, v109, v111, s12 bitop3:0x7e
v_bitop3_b32 v155, v137, v139, s12 bitop3:0x7e
v_bitop3_b32 v156, v113, v115, s12 bitop3:0x7e
v_bitop3_b32 v157, v141, v143, s12 bitop3:0x7e
v_or3_b32 v144, v144, v145, v146
v_or3_b32 v147, v147, v148, v149
v_or3_b32 v150, v150, v151, v152
v_or3_b32 v153, v153, v154, v155
v_or_b32 v156, v156, v157
v_or3_b32 v144, v144, v147, v150
v_or_b32 v153, v153, v156
v_or_b32 v144, v144, v153
v_cmp_ne_u32 vcc, 0, v144
s_cbranch_vccz LwB_5_%=
s_cmp_lt_u32 s33, 32768
s_cbranch_scc1 Lwl0_2_%=
s_branch Lwab_7_%=
LwB_5_%=:
s_nop 7
s_nop 1
v_mov_b32 v224, 0xff800000
v_cndmask_b32 v140, v140, v224, s[44:45]
v_max3_f32 v224, v116, v120, v124
v_max3_f32 v225, v128, v132, v136
v_max3_f32 v224, v224, v225, v140
v_mov_b32 v225, v224
v_lshlrev_b32 v144, 16, v88
v_and_b32 v145, 0xffff0000, v88
v_permlane16_swap_b32 v224, v225
v_max_f32 v224, v224, v225
v_mov_b32 v225, v224
v_lshlrev_b32 v146, 16, v90
v_and_b32 v147, 0xffff0000, v90
v_permlane32_swap_b32 v224, v225
v_max_f32 v224, v224, v225
v_sub_f32 v116, v116, v224
v_sub_f32 v120, v120, v224
v_exp_f32 v116, v116
v_sub_f32 v124, v124, v224
v_exp_f32 v120, v120
v_sub_f32 v128, v128, v224
v_exp_f32 v124, v124
v_sub_f32 v132, v132, v224
v_exp_f32 v128, v128
v_pk_mul_f32 v[228:229], v[144:145], v[116:117] op_sel_hi:[1,0]
v_pk_mul_f32 v[230:231], v[146:147], v[116:117] op_sel_hi:[1,0]
v_mul_f32 v226, v116, v118
v_lshlrev_b32 v148, 16, v92
v_and_b32 v149, 0xffff0000, v92
v_lshlrev_b32 v150, 16, v94
v_and_b32 v151, 0xffff0000, v94
v_sub_f32 v136, v136, v224
v_exp_f32 v132, v132
v_pk_fma_f32 v[228:229], v[148:149], v[120:121], v[228:229] op_sel_hi:[1,0,1]
v_pk_fma_f32 v[230:231], v[150:151], v[120:121], v[230:231] op_sel_hi:[1,0,1]
v_fma_f32 v226, v120, v122, v226
v_lshlrev_b32 v144, 16, v96
v_and_b32 v145, 0xffff0000, v96
v_lshlrev_b32 v146, 16, v98
v_and_b32 v147, 0xffff0000, v98
v_sub_f32 v140, v140, v224
v_exp_f32 v136, v136
v_pk_fma_f32 v[228:229], v[144:145], v[124:125], v[228:229] op_sel_hi:[1,0,1]
v_pk_fma_f32 v[230:231], v[146:147], v[124:125], v[230:231] op_sel_hi:[1,0,1]
v_fma_f32 v226, v124, v126, v226
v_lshlrev_b32 v148, 16, v100
v_and_b32 v149, 0xffff0000, v100
v_lshlrev_b32 v150, 16, v102
v_and_b32 v151, 0xffff0000, v102
v_exp_f32 v140, v140
v_pk_fma_f32 v[228:229], v[148:149], v[128:129], v[228:229] op_sel_hi:[1,0,1]
v_pk_fma_f32 v[230:231], v[150:151], v[128:129], v[230:231] op_sel_hi:[1,0,1]
v_fma_f32 v226, v128, v130, v226
v_lshlrev_b32 v144, 16, v104
v_and_b32 v145, 0xffff0000, v104
v_lshlrev_b32 v146, 16, v106
v_and_b32 v147, 0xffff0000, v106
v_pk_fma_f32 v[228:229], v[144:145], v[132:133], v[228:229] op_sel_hi:[1,0,1]
v_pk_fma_f32 v[230:231], v[146:147], v[132:133], v[230:231] op_sel_hi:[1,0,1]
v_fma_f32 v226, v132, v134, v226
v_lshlrev_b32 v148, 16, v108
v_and_b32 v149, 0xffff0000, v108
v_lshlrev_b32 v150, 16, v110
v_and_b32 v151, 0xffff0000, v110
v_pk_fma_f32 v[228:229], v[148:149], v[136:137], v[228:229] op_sel_hi:[1,0,1]
v_pk_fma_f32 v[230:231], v[150:151], v[136:137], v[230:231] op_sel_hi:[1,0,1]
v_fma_f32 v226, v136, v138, v226
v_lshlrev_b32 v144, 16, v112
v_and_b32 v145, 0xffff0000, v112
v_lshlrev_b32 v146, 16, v114
v_and_b32 v147, 0xffff0000, v114
v_pk_fma_f32 v[228:229], v[144:145], v[140:141], v[228:229] op_sel_hi:[1,0,1]
v_pk_fma_f32 v[230:231], v[146:147], v[140:141], v[230:231] op_sel_hi:[1,0,1]
v_fma_f32 v226, v140, v142, v226
s_branch LwJ_6_%=
LwA_4_%=:
s_nop 1
v_mov_b32 v224, 0xff800000
v_cndmask_b32 v84, v84, v224, s[44:45]
v_max3_f32 v224, v60, v64, v68
v_max3_f32 v225, v72, v76, v80
v_max3_f32 v224, v224, v225, v84
v_mov_b32 v225, v224
v_lshlrev_b32 v144, 16, v32
v_and_b32 v145, 0xffff0000, v32
v_permlane16_swap_b32 v224, v225
v_max_f32 v224, v224, v225
v_mov_b32 v225, v224
v_lshlrev_b32 v146, 16, v34
v_and_b32 v147, 0xffff0000, v34
v_permlane32_swap_b32 v224, v225
v_max_f32 v224, v224, v225
v_sub_f32 v60, v60, v224
v_sub_f32 v64, v64, v224
v_exp_f32 v60, v60
v_sub_f32 v68, v68, v224
v_exp_f32 v64, v64
v_sub_f32 v72, v72, v224
v_exp_f32 v68, v68
v_sub_f32 v76, v76, v224
v_exp_f32 v72, v72
v_pk_mul_f32 v[228:229], v[144:145], v[60:61] op_sel_hi:[1,0]
v_pk_mul_f32 v[230:231], v[146:147], v[60:61] op_sel_hi:[1,0]
v_mul_f32 v226, v60, v62
v_lshlrev_b32 v148, 16, v36
v_and_b32 v149, 0xffff0000, v36
v_lshlrev_b32 v150, 16, v38
v_and_b32 v151, 0xffff0000, v38
v_sub_f32 v80, v80, v224
v_exp_f32 v76, v76
v_pk_fma_f32 v[228:229], v[148:149], v[64:65], v[228:229] op_sel_hi:[1,0,1]
v_pk_fma_f32 v[230:231], v[150:151], v[64:65], v[230:231] op_sel_hi:[1,0,1]
v_fma_f32 v226, v64, v66, v226
v_lshlrev_b32 v144, 16, v40
v_and_b32 v145, 0xffff0000, v40
v_lshlrev_b32 v146, 16, v42
v_and_b32 v147, 0xffff0000, v42
v_sub_f32 v84, v84, v224
v_exp_f32 v80, v80
v_pk_fma_f32 v[228:229], v[144:145], v[68:69], v[228:229] op_sel_hi:[1,0,1]
v_pk_fma_f32 v[230:231], v[146:147], v[68:69], v[230:231] op_sel_hi:[1,0,1]
v_fma_f32 v226, v68, v70, v226
v_lshlrev_b32 v148, 16, v44
v_and_b32 v149, 0xffff0000, v44
v_lshlrev_b32 v150, 16, v46
v_and_b32 v151, 0xffff0000, v46
v_exp_f32 v84, v84
v_pk_fma_f32 v[228:229], v[148:149], v[72:73], v[228:229] op_sel_hi:[1,0,1]
v_pk_fma_f32 v[230:231], v[150:151], v[72:73], v[230:231] op_sel_hi:[1,0,1]
v_fma_f32 v226, v72, v74, v226
v_lshlrev_b32 v144, 16, v48
v_and_b32 v145, 0xffff0000, v48
v_lshlrev_b32 v146, 16, v50
v_and_b32 v147, 0xffff0000, v50
v_pk_fma_f32 v[228:229], v[144:145], v[76:77], v[228:229] op_sel_hi:[1,0,1]
v_pk_fma_f32 v[230:231], v[146:147], v[76:77], v[230:231] op_sel_hi:[1,0,1]
v_fma_f32 v226, v76, v78, v226
v_lshlrev_b32 v148, 16, v52
v_and_b32 v149, 0xffff0000, v52
v_lshlrev_b32 v150, 16, v54
v_and_b32 v151, 0xffff0000, v54
v_pk_fma_f32 v[228:229], v[148:149], v[80:81], v[228:229] op_sel_hi:[1,0,1]
v_pk_fma_f32 v[230:231], v[150:151], v[80:81], v[230:231] op_sel_hi:[1,0,1]
v_fma_f32 v226, v80, v82, v226
v_lshlrev_b32 v144, 16, v56
v_and_b32 v145, 0xffff0000, v56
v_lshlrev_b32 v146, 16, v58
v_and_b32 v147, 0xffff0000, v58
v_pk_fma_f32 v[228:229], v[144:145], v[84:85], v[228:229] op_sel_hi:[1,0,1]
v_pk_fma_f32 v[230:231], v[146:147], v[84:85], v[230:231] op_sel_hi:[1,0,1]
v_fma_f32 v226, v84, v86, v226
LwJ_6_%=:
s_nop 1
s_cmp_ge_u32 s31, 8
s_cbranch_scc1 Lmk_10_%=
s_memrealtime s[60:61]
Lmk_10_%=:
s_and_b32 s31, s28, 15
s_lshr_b64 s[28:29], s[28:29], 4
s_mul_i32 s36, s31, 0x68000
s_add_u32 s36, s36, 0x81000
s_lshl_b32 s37, s31, 14
s_add_u32 s37, s37, 0x41000
buffer_load_dwordx4 v[32:35], v3, s[24:27], s36 offen sc1
buffer_load_dwordx4 v[36:39], v4, s[24:27], s36 offen sc1
buffer_load_dwordx4 v[40:43], v5, s[24:27], s36 offen sc1
buffer_load_dwordx4 v[44:47], v6, s[24:27], s36 offen sc1
buffer_load_dwordx4 v[48:51], v7, s[24:27], s36 offen sc1
buffer_load_dwordx4 v[52:55], v8, s[24:27], s36 offen sc1
buffer_load_dwordx4 v[56:59], v9, s[24:27], s36 offen sc1
buffer_load_dwordx4 v[60:63], v10, s[24:27], s37 offen sc1
buffer_load_dwordx4 v[64:67], v10, s[24:27], s37 offen offset:64 sc1
buffer_load_dwordx4 v[68:71], v10, s[24:27], s37 offen offset:128 sc1
buffer_load_dwordx4 v[72:75], v10, s[24:27], s37 offen offset:192 sc1
buffer_load_dwordx4 v[76:79], v10, s[24:27], s37 offen offset:256 sc1
buffer_load_dwordx4 v[80:83], v10, s[24:27], s37 offen offset:320 sc1
buffer_load_dwordx4 v[84:87], v11, s[24:27], s37 offen sc1
v_mov_b32 v227, v226
v_permlane32_swap_b32 v228, v229
v_permlane32_swap_b32 v230, v231
v_permlane16_swap_b32 v226, v227
v_add_f32 v226, v226, v227
v_mov_b32 v227, v226
v_add_f32 v228, v228, v229
v_add_f32 v230, v230, v231
v_permlane32_swap_b32 v226, v227
v_add_f32 v226, v226, v227
v_rcp_f32 v226, v226
v_permlane16_swap_b32 v228, v230
v_add_f32 v228, v228, v230
v_mul_f32 v228, v228, v226
v_cvt_pk_bf16_f32 v228, v228, v228
v_add_u32 v232, s49, v12
v_add_u32 v233, s49, v13
ds_write_b16 v232, v228
s_waitcnt lgkmcnt(0)
s_barrier
ds_read_b128 v[192:195], v233 offset:0
v_mov_b32 v235, s12
ds_read_b128 v[196:199], v233 offset:64
ds_read_b128 v[200:203], v233 offset:128
ds_read_b128 v[204:207], v233 offset:192
ds_read_b128 v[208:211], v233 offset:256
ds_read_b128 v[212:215], v233 offset:320
ds_read_b128 v[216:219], v233 offset:384
ds_read_b128 v[220:223], v233 offset:448
v_mov_b32 v233, s12
s_waitcnt lgkmcnt(7)
v_mfma_f32_16x16x32_bf16 v[160:163], v[192:195], a[0:3], 0
v_mfma_f32_16x16x32_bf16 v[164:167], v[192:195], a[4:7], 0
v_mfma_f32_16x16x32_bf16 v[168:171], v[192:195], a[8:11], 0
v_mfma_f32_16x16x32_bf16 v[172:175], v[192:195], a[12:15], 0
v_mfma_f32_16x16x32_bf16 v[176:179], v[192:195], a[16:19], 0
v_mfma_f32_16x16x32_bf16 v[180:183], v[192:195], a[20:23], 0
v_mfma_f32_16x16x32_bf16 v[184:187], v[192:195], a[24:27], 0
v_mfma_f32_16x16x32_bf16 v[188:191], v[192:195], a[28:31], 0
s_waitcnt lgkmcnt(6)
v_mfma_f32_16x16x32_bf16 v[160:163], v[196:199], a[32:35], v[160:163]
v_mfma_f32_16x16x32_bf16 v[164:167], v[196:199], a[36:39], v[164:167]
v_mfma_f32_16x16x32_bf16 v[168:171], v[196:199], a[40:43], v[168:171]
v_mfma_f32_16x16x32_bf16 v[172:175], v[196:199], a[44:47], v[172:175]
v_mfma_f32_16x16x32_bf16 v[176:179], v[196:199], a[48:51], v[176:179]
v_mfma_f32_16x16x32_bf16 v[180:183], v[196:199], a[52:55], v[180:183]
v_mfma_f32_16x16x32_bf16 v[184:187], v[196:199], a[56:59], v[184:187]
v_mfma_f32_16x16x32_bf16 v[188:191], v[196:199], a[60:63], v[188:191]
s_waitcnt lgkmcnt(5)
v_mfma_f32_16x16x32_bf16 v[160:163], v[200:203], a[64:67], v[160:163]
v_mfma_f32_16x16x32_bf16 v[164:167], v[200:203], a[68:71], v[164:167]
v_mfma_f32_16x16x32_bf16 v[168:171], v[200:203], a[72:75], v[168:171]
v_mfma_f32_16x16x32_bf16 v[172:175], v[200:203], a[76:79], v[172:175]
v_mfma_f32_16x16x32_bf16 v[176:179], v[200:203], a[80:83], v[176:179]
v_mfma_f32_16x16x32_bf16 v[180:183], v[200:203], a[84:87], v[180:183]
v_mfma_f32_16x16x32_bf16 v[184:187], v[200:203], a[88:91], v[184:187]
v_mfma_f32_16x16x32_bf16 v[188:191], v[200:203], a[92:95], v[188:191]
s_waitcnt lgkmcnt(4)
v_mfma_f32_16x16x32_bf16 v[160:163], v[204:207], a[96:99], v[160:163]
v_mfma_f32_16x16x32_bf16 v[164:167], v[204:207], a[100:103], v[164:167]
v_mfma_f32_16x16x32_bf16 v[168:171], v[204:207], a[104:107], v[168:171]
v_mfma_f32_16x16x32_bf16 v[172:175], v[204:207], a[108:111], v[172:175]
v_mfma_f32_16x16x32_bf16 v[176:179], v[204:207], a[112:115], v[176:179]
v_mfma_f32_16x16x32_bf16 v[180:183], v[204:207], a[116:119], v[180:183]
v_mfma_f32_16x16x32_bf16 v[184:187], v[204:207], a[120:123], v[184:187]
v_mfma_f32_16x16x32_bf16 v[188:191], v[204:207], a[124:127], v[188:191]
s_waitcnt lgkmcnt(3)
v_mfma_f32_16x16x32_bf16 v[160:163], v[208:211], a[128:131], v[160:163]
v_mfma_f32_16x16x32_bf16 v[164:167], v[208:211], a[132:135], v[164:167]
v_mfma_f32_16x16x32_bf16 v[168:171], v[208:211], a[136:139], v[168:171]
v_mfma_f32_16x16x32_bf16 v[172:175], v[208:211], a[140:143], v[172:175]
v_mfma_f32_16x16x32_bf16 v[176:179], v[208:211], a[144:147], v[176:179]
v_mfma_f32_16x16x32_bf16 v[180:183], v[208:211], a[148:151], v[180:183]
v_mfma_f32_16x16x32_bf16 v[184:187], v[208:211], a[152:155], v[184:187]
v_mfma_f32_16x16x32_bf16 v[188:191], v[208:211], a[156:159], v[188:191]
s_waitcnt lgkmcnt(2)
v_mfma_f32_16x16x32_bf16 v[160:163], v[212:215], a[160:163], v[160:163]
v_mfma_f32_16x16x32_bf16 v[164:167], v[212:215], a[164:167], v[164:167]
v_mfma_f32_16x16x32_bf16 v[168:171], v[212:215], a[168:171], v[168:171]
v_mfma_f32_16x16x32_bf16 v[172:175], v[212:215], a[172:175], v[172:175]
v_mfma_f32_16x16x32_bf16 v[176:179], v[212:215], a[176:179], v[176:179]
v_mfma_f32_16x16x32_bf16 v[180:183], v[212:215], a[180:183], v[180:183]
v_mfma_f32_16x16x32_bf16 v[184:187], v[212:215], a[184:187], v[184:187]
v_mfma_f32_16x16x32_bf16 v[188:191], v[212:215], a[188:191], v[188:191]
s_waitcnt lgkmcnt(1)
v_mfma_f32_16x16x32_bf16 v[160:163], v[216:219], a[192:195], v[160:163]
v_mfma_f32_16x16x32_bf16 v[164:167], v[216:219], a[196:199], v[164:167]
v_mfma_f32_16x16x32_bf16 v[168:171], v[216:219], a[200:203], v[168:171]
v_mfma_f32_16x16x32_bf16 v[172:175], v[216:219], a[204:207], v[172:175]
v_mfma_f32_16x16x32_bf16 v[176:179], v[216:219], a[208:211], v[176:179]
v_mfma_f32_16x16x32_bf16 v[180:183], v[216:219], a[212:215], v[180:183]
v_mfma_f32_16x16x32_bf16 v[184:187], v[216:219], a[216:219], v[184:187]
v_mfma_f32_16x16x32_bf16 v[188:191], v[216:219], a[220:223], v[188:191]
s_waitcnt lgkmcnt(0)
v_mfma_f32_16x16x32_bf16 v[160:163], v[220:223], a[224:227], v[160:163]
v_mfma_f32_16x16x32_bf16 v[164:167], v[220:223], a[228:231], v[164:167]
v_mfma_f32_16x16x32_bf16 v[168:171], v[220:223], a[232:235], v[168:171]
v_mfma_f32_16x16x32_bf16 v[172:175], v[220:223], a[236:239], v[172:175]
v_mfma_f32_16x16x32_bf16 v[176:179], v[220:223], a[240:243], v[176:179]
v_mfma_f32_16x16x32_bf16 v[180:183], v[220:223], a[244:247], v[180:183]
s_nop 4
v_cndmask_b32 v224, v160, v168, s[40:41]
v_mfma_f32_16x16x32_bf16 v[184:187], v[220:223], a[248:251], v[184:187]
v_cndmask_b32 v228, v164, v172, s[40:41]
v_mfma_f32_16x16x32_bf16 v[188:191], v[220:223], a[252:255], v[188:191]
s_nop 5
v_cndmask_b32 v226, v176, v184, s[40:41]
v_cndmask_b32 v224, v224, v226, s[42:43]
v_cndmask_b32 v230, v180, v188, s[40:41]
v_cndmask_b32 v228, v228, v230, s[42:43]
v_cvt_pk_bf16_f32 v232, v224, v228
v_mov_b32 v234, v232
s_nop 1
v_permlane16_swap_b32 v232, v234
s_nop 1
s_mov_b32 exec_lo, 0xffff
s_mov_b32 exec_hi, 0xffff
buffer_store_dwordx4 v[232:235], v14, s[24:27], s38 offen sc1
s_mov_b64 exec, -1
s_add_u32 s30, s30, 1
s_cmp_lt_u32 s30, 16
s_cbranch_scc1 Lwloop_1_%=
s_waitcnt vmcnt(0)
s_endpgm
Lrcew_%=:
Lwab_7_%=:
v_mov_b32 v252, 256
v_mov_b32 v253, 1
buffer_store_dword v253, v252, s[24:27], 0 offen sc1
s_nop 4
s_branch LwA_4_%=
)ASM" :
      CHAIN_IN((u32x4{(unsigned)lane, vSt, vW, vO[0]}), (u32x4{vO[1], vO[2], vO[3], vO[4]}), (u32x4{vO[5], vO[6], vM0, vM6}), (u32x4{vOBw, vOBr, vY, 0u}), sA, sA, sA)
      : "v16", "v17", "v18", "v19", "v20", "v21", "v22", "v23", "v24", "v25", "v26", "v27", "v28", "v29", "v30", "v31", "v32", "v33", "v34", "v35", "v36", "v37", "v38", "v39", "v40", "v41", "v42", "v43", "v44", "v45", "v46", "v47", "v48", "v49", "v50", "v51", "v52", "v53", "v54", "v55", "v56", "v57", "v58", "v59", "v60", "v61", "v62", "v63", "v64", "v65", "v66", "v67", "v68", "v69", "v70", "v71", "v72", "v73", "v74", "v75", "v76", "v77", "v78", "v79", "v80", "v81", "v82", "v83", "v84", "v85", "v86", "v87", "v88", "v89", "v90", "v91", "v92", "v93", "v94", "v95", "v96", "v97", "v98", "v99", "v100", "v101", "v102", "v103", "v104", "v105", "v106", "v107", "v108", "v109", "v110", "v111", "v112", "v113", "v114", "v115", "v116", "v117", "v118", "v119", "v120", "v121", "v122", "v123", "v124", "v125", "v126", "v127", "v128", "v129", "v130", "v131", "v132", "v133", "v134", "v135", "v136", "v137", "v138", "v139", "v140", "v141", "v142", "v143", "v144", "v145", "v146", "v147", "v148", "v149", "v150", "v151", "v152", "v153", "v154", "v155", "v156", "v157", "v158", "v159", "v160", "v161", "v162", "v163", "v164", "v165", "v166", "v167", "v168", "v169", "v170", "v171", "v172", "v173", "v174", "v175", "v176", "v177", "v178", "v179", "v180", "v181", "v182", "v183", "v184", "v185", "v186", "v187", "v188", "v189", "v190", "v191", "v192", "v193", "v194", "v195", "v196", "v197", "v198", "v199", "v200", "v201", "v202", "v203", "v204", "v205", "v206", "v207", "v208", "v209", "v210", "v211", "v212", "v213", "v214", "v215", "v216", "v217", "v218", "v219", "v220", "v221", "v222", "v223", "v224", "v225", "v226", "v227", "v228", "v229", "v230", "v231", "v232", "v233", "v234", "v235", "v236", "v237", "v238", "v239", "v240", "v241", "v242", "v243", "v244", "v245", "v246", "v247", "v248", "v249", "v250", "v251", "v252", "v253", "v254", "v255", "a0", "a1", "a2", "a3", "a4", "a5", "a6", "a7", "a8", "a9", "a10", "a11", "a12", "a13", "a14", "a15", "a16", "a17", "a18", "a19", "a20", "a21", "a22", "a23", "a24", "a25", "a26", "a27", "a28", "a29", "a30", "a31", "a32", "a33", "a34", "a35", "a36", "a37", "a38", "a39", "a40", "a41", "a42", "a43", "a44", "a45", "a46", "a47", "a48", "a49", "a50", "a51", "a52", "a53", "a54", "a55", "a56", "a57", "a58", "a59", "a60", "a61", "a62", "a63", "a64", "a65", "a66", "a67", "a68", "a69", "a70", "a71", "a72", "a73", "a74", "a75", "a76", "a77", "a78", "a79", "a80", "a81", "a82", "a83", "a84", "a85", "a86", "a87", "a88", "a89", "a90", "a91", "a92", "a93", "a94", "a95", "a96", "a97", "a98", "a99", "a100", "a101", "a102", "a103", "a104", "a105", "a106", "a107", "a108", "a109", "a110", "a111", "a112", "a113", "a114", "a115", "a116", "a117", "a118", "a119", "a120", "a121", "a122", "a123", "a124", "a125", "a126", "a127", "a128", "a129", "a130", "a131", "a132", "a133", "a134", "a135", "a136", "a137", "a138", "a139", "a140", "a141", "a142", "a143", "a144", "a145", "a146", "a147", "a148", "a149", "a150", "a151", "a152", "a153", "a154", "a155", "a156", "a157", "a158", "a159", "a160", "a161", "a162", "a163", "a164", "a165", "a166", "a167", "a168", "a169", "a170", "a171", "a172", "a173", "a174", "a175", "a176", "a177", "a178", "a179", "a180", "a181", "a182", "a183", "a184", "a185", "a186", "a187", "a188", "a189", "a190", "a191", "a192", "a193", "a194", "a195", "a196", "a197", "a198", "a199", "a200", "a201", "a202", "a203", "a204", "a205", "a206", "a207", "a208", "a209", "a210", "a211", "a212", "a213", "a214", "a215", "a216", "a217", "a218", "a219", "a220", "a221", "a222", "a223", "a224", "a225", "a226", "a227", "a228", "a229", "a230", "a231", "a232", "a233", "a234", "a235", "a236", "a237", "a238", "a239", "a240", "a241", "a242", "a243", "a244", "a245", "a246", "a247", "a248", "a249", "a250", "a251", "a252", "a253", "a254", "a255", "s28", "s29", "s30", "s31", "s33", "s34", "s35", "s36", "s37", "s38", "s39", "s40", "s41", "s42", "s43", "s44", "s45", "s46", "s47", "s50", "s51", "s52", "s53", "s54", "s55", "s56", "s57", "s58", "s59", "s60", "s61", "s62", "s63", "vcc", "memory");
  __builtin_unreachable();
}
__device__ __forceinline__ void role_m(const Params& p, int idx, unsigned tag, int w) {
  CHAIN_COMMON
  const unsigned vW1 = (128 * w + 8 * g) * (FF * 2) + c * 16, vW2 = 8 * g * 1024 + w * 256 + c * 16;
  const unsigned n0 = 128 * w + 4 * q32 + 2 * ph;
  const unsigned vG = ph * 8 * 2048 + w * 512 + q32 * 16, vX = n0 * 8;
  const unsigned vXBw = w * 256 + (4 * q32 + 2 * ph) * 2, vXBr = w * 256 + g * 16;
  const unsigned vEXw = 1024 + (w * 128 + 8 * c + 2 * g) * 4, vEXr = 1024 + lane * 8, vSTw = 8192 + w * 8;
  const unsigned vHBw = 12288 + w * 256 + lane * 4, vHBr = 12288 + w * 256 + g * 16;
  const unsigned vZ = idx * 2048 + (64 * w + 4 * c + g) * 8, vXin = n0 * 2;
  const u32x4 sX = mk_srd(p.x, 0, NB * DM * 2);
  const u32x4 sA = mk_srd(p.w1, (unsigned long long)idx * 256, DM * FF * 2 - idx * 256), sB = mk_srd(p.w2, (unsigned long long)idx * 128 * 1024, 128 * 1024);
  asm volatile(R"ASM(
s_nop 4
s_mov_b32 s34, 0x5040100
s_mov_b32 s35, 0x7060302
s_mov_b32 s44, 0
s_mov_b32 s45, -1
s_mov_b32 s28, 0x5a493201
s_mov_b32 s29, 0xf8ed7c6b
s_mov_b32 s30, 0
v_mov_b32 v253, 0
buffer_load_dword v252, v253, s[24:27], 0 offen sc1
s_getpc_b64 s[56:57]
Lrc0m_%=:
s_and_b32 s57, s57, 0xffff
s_mov_b32 s58, Lrcem_%=-Lrc0m_%=
s_mov_b32 s59, 0x20000
v_lshlrev_b32 v251, 7, v0
s_lshl_b32 s46, s13, 13
v_add_u32 v251, s46, v251
buffer_load_dword v250, v251, s[56:59], 0 offen
v_and_b32 v16, 16, v0
v_cmp_ne_u32 s[40:41], 0, v16
v_and_b32 v16, 32, v0
v_cmp_ne_u32 s[42:43], 0, v16
s_mov_b32 s46, 0x400
buffer_load_dword v16, v14, s[8:11], s46 offen 
s_mov_b32 s46, 0
buffer_load_dword v17, v14, s[8:11], s46 offen 
s_mov_b32 s46, 0x800
buffer_load_dword v18, v14, s[8:11], s46 offen 
s_mov_b32 s46, 0xc00
buffer_load_dword v19, v14, s[8:11], s46 offen 
s_mov_b32 s46, 0x1000
buffer_load_dword v20, v14, s[8:11], s46 offen 
s_mov_b32 s46, 0x1400
buffer_load_dword v21, v14, s[8:11], s46 offen 
s_mov_b32 s46, 0x1800
buffer_load_dword v22, v14, s[8:11], s46 offen 
s_mov_b32 s46, 0x1c00
buffer_load_dword v23, v14, s[8:11], s46 offen 
s_mov_b32 s46, 0
buffer_load_dwordx4 a[0:3], v2, s[16:19], s46 offen 
s_mov_b32 s46, 0xe00
buffer_load_dwordx4 a[4:7], v2, s[16:19], s46 offen 
s_mov_b32 s46, 0x1c00
buffer_load_dwordx4 a[8:11], v2, s[16:19], s46 offen 
s_mov_b32 s46, 0x2a00
buffer_load_dwordx4 a[12:15], v2, s[16:19], s46 offen 
s_mov_b32 s46, 0x3800
buffer_load_dwordx4 a[16:19], v2, s[16:19], s46 offen 
s_mov_b32 s46, 0x4600
buffer_load_dwordx4 a[20:23], v2, s[16:19], s46 offen 
s_mov_b32 s46, 0x5400
buffer_load_dwordx4 a[24:27], v2, s[16:19], s46 offen 
s_mov_b32 s46, 0x6200
buffer_load_dwordx4 a[28:31], v2, s[16:19], s46 offen 
s_mov_b32 s46, 0x1c000
buffer_load_dwordx4 a[32:35], v2, s[16:19], s46 offen 
s_mov_b32 s46, 0x1ce00
buffer_load_dwordx4 a[36:39], v2, s[16:19], s46 offen 
s_mov_b32 s46, 0x1dc00
buffer_load_dwordx4 a[40:43], v2, s[16:19], s46 offen 
s_mov_b32 s46, 0x1ea00
buffer_load_dwordx4 a[44:47], v2, s[16:19], s46 offen 
s_mov_b32 s46, 0x1f800
buffer_load_dwordx4 a[48:51], v2, s[16:19], s46 offen 
s_mov_b32 s46, 0x20600
buffer_load_dwordx4 a[52:55], v2, s[16:19], s46 offen 
s_mov_b32 s46, 0x21400
buffer_load_dwordx4 a[56:59], v2, s[16:19], s46 offen 
s_mov_b32 s46, 0x22200
buffer_load_dwordx4 a[60:63], v2, s[16:19], s46 offen 
s_mov_b32 s46, 0x38000
buffer_load_dwordx4 a[64:67], v2, s[16:19], s46 offen 
s_mov_b32 s46, 0x38e00
buffer_load_dwordx4 a[68:71], v2, s[16:19], s46 offen 
s_mov_b32 s46, 0x39c00
buffer_load_dwordx4 a[72:75], v2, s[16:19], s46 offen 
s_mov_b32 s46, 0x3aa00
buffer_load_dwordx4 a[76:79], v2, s[16:19], s46 offen 
s_mov_b32 s46, 0x3b800
buffer_load_dwordx4 a[80:83], v2, s[16:19], s46 offen 
s_mov_b32 s46, 0x3c600
buffer_load_dwordx4 a[84:87], v2, s[16:19], s46 offen 
s_mov_b32 s46, 0x3d400
buffer_load_dwordx4 a[88:91], v2, s[16:19], s46 offen 
s_mov_b32 s46, 0x3e200
buffer_load_dwordx4 a[92:95], v2, s[16:19], s46 offen 
s_mov_b32 s46, 0x54000
buffer_load_dwordx4 a[96:99], v2, s[16:19], s46 offen 
s_mov_b32 s46, 0x54e00
buffer_load_dwordx4 a[100:103], v2, s[16:19], s46 offen 
s_mov_b32 s46, 0x55c00
buffer_load_dwordx4 a[104:107], v2, s[16:19], s46 offen 
s_mov_b32 s46, 0x56a00
buffer_load_dwordx4 a[108:111], v2, s[16:19], s46 offen 
s_mov_b32 s46, 0x57800
buffer_load_dwordx4 a[112:115], v2, s[16:19], s46 offen 
s_mov_b32 s46, 0x58600
buffer_load_dwordx4 a[116:119], v2, s[16:19], s46 offen 
s_mov_b32 s46, 0x59400
buffer_load_dwordx4 a[120:123], v2, s[16:19], s46 offen 
s_mov_b32 s46, 0x5a200
buffer_load_dwordx4 a[124:127], v2, s[16:19], s46 offen 
s_mov_b32 s46, 0
buffer_load_dwordx4 a[128:131], v3, s[20:23], s46 offen 
s_mov_b32 s46, 0x400
buffer_load_dwordx4 a[132:135], v3, s[20:23], s46 offen 
s_mov_b32 s46, 0x800
buffer_load_dwordx4 a[136:139], v3, s[20:23], s46 offen 
s_mov_b32 s46, 0xc00
buffer_load_dwordx4 a[140:143], v3, s[20:23], s46 offen 
s_mov_b32 s46, 0x1000
buffer_load_dwordx4 a[144:147], v3, s[20:23], s46 offen 
s_mov_b32 s46, 0x1400
buffer_load_dwordx4 a[148:151], v3, s[20:23], s46 offen 
s_mov_b32 s46, 0x1800
buffer_load_dwordx4 a[152:155], v3, s[20:23], s46 offen 
s_mov_b32 s46, 0x1c00
buffer_load_dwordx4 a[156:159], v3, s[20:23], s46 offen 
s_mov_b32 s46, 0x8000
buffer_load_dwordx4 a[160:163], v3, s[20:23], s46 offen 
s_mov_b32 s46, 0x8400
buffer_load_dwordx4 a[164:167], v3, s[20:23], s46 offen 
s_mov_b32 s46, 0x8800
buffer_load_dwordx4 a[168:171], v3, s[20:23], s46 offen 
s_mov_b32 s46, 0x8c00
buffer_load_dwordx4 a[172:175], v3, s[20:23], s46 offen 
s_mov_b32 s46, 0x9000
buffer_load_dwordx4 a[176:179], v3, s[20:23], s46 offen 
s_mov_b32 s46, 0x9400
buffer_load_dwordx4 a[180:183], v3, s[20:23], s46 offen 
s_mov_b32 s46, 0x9800
buffer_load_dwordx4 a[184:187], v3, s[20:23], s46 offen 
s_mov_b32 s46, 0x9c00
buffer_load_dwordx4 a[188:191], v3, s[20:23], s46 offen 
s_mov_b32 s46, 0x10000
buffer_load_dwordx4 a[192:195], v3, s[20:23], s46 offen 
s_mov_b32 s46, 0x10400
buffer_load_dwordx4 a[196:199], v3, s[20:23], s46 offen 
s_mov_b32 s46, 0x10800
buffer_load_dwordx4 a[200:203], v3, s[20:23], s46 offen 
s_mov_b32 s46, 0x10c00
buffer_load_dwordx4 a[204:207], v3, s[20:23], s46 offen 
s_mov_b32 s46, 0x11000
buffer_load_dwordx4 a[208:211], v3, s[20:23], s46 offen 
s_mov_b32 s46, 0x11400
buffer_load_dwordx4 a[212:215], v3, s[20:23], s46 offen 
s_mov_b32 s46, 0x11800
buffer_load_dwordx4 a[216:219], v3, s[20:23], s46 offen 
s_mov_b32 s46, 0x11c00
buffer_load_dwordx4 a[220:223], v3, s[20:23], s46 offen 
s_mov_b32 s46, 0x18000
buffer_load_dwordx4 a[224:227], v3, s[20:23], s46 offen 
s_mov_b32 s46, 0x18400
buffer_load_dwordx4 a[228:231], v3, s[20:23], s46 offen 
s_mov_b32 s46, 0x18800
buffer_load_dwordx4 a[232:235], v3, s[20:23], s46 offen 
s_mov_b32 s46, 0x18c00
buffer_load_dwordx4 a[236:239], v3, s[20:23], s46 offen 
s_mov_b32 s46, 0x19000
buffer_load_dwordx4 a[240:243], v3, s[20:23], s46 offen 
s_mov_b32 s46, 0x19400
buffer_load_dwordx4 a[244:247], v3, s[20:23], s46 offen 
s_mov_b32 s46, 0x19800
buffer_load_dwordx4 a[248:251], v3, s[20:23], s46 offen 
s_mov_b32 s46, 0x19c00
buffer_load_dwordx4 a[252:255], v3, s[20:23], s46 offen 
s_memrealtime s[58:59]
s_waitcnt lgkmcnt(0)
s_mov_b32 s60, s58
s_waitcnt vmcnt(62)
v_readfirstlane_b32 s12, v252
s_nop 3
s_add_u32 s12, s12, 1
s_waitcnt vmcnt(56)
v_accvgpr_read_b32 v32, a0
v_accvgpr_read_b32 v33, a1
v_accvgpr_read_b32 v34, a2
v_accvgpr_read_b32 v35, a3
v_accvgpr_read_b32 v36, a4
v_accvgpr_read_b32 v37, a5
v_accvgpr_read_b32 v38, a6
v_accvgpr_read_b32 v39, a7
v_accvgpr_read_b32 v40, a8
v_accvgpr_read_b32 v41, a9
v_accvgpr_read_b32 v42, a10
v_accvgpr_read_b32 v43, a11
v_accvgpr_read_b32 v44, a12
v_accvgpr_read_b32 v45, a13
v_accvgpr_read_b32 v46, a14
v_accvgpr_read_b32 v47, a15
v_accvgpr_read_b32 v48, a16
v_accvgpr_read_b32 v49, a17
v_accvgpr_read_b32 v50, a18
v_accvgpr_read_b32 v51, a19
v_accvgpr_read_b32 v52, a20
v_accvgpr_read_b32 v53, a21
v_accvgpr_read_b32 v54, a22
v_accvgpr_read_b32 v55, a23
v_accvgpr_read_b32 v56, a24
v_accvgpr_read_b32 v57, a25
v_accvgpr_read_b32 v58, a26
v_accvgpr_read_b32 v59, a27
v_accvgpr_read_b32 v60, a28
v_accvgpr_read_b32 v61, a29
v_accvgpr_read_b32 v62, a30
v_accvgpr_read_b32 v63, a31
v_perm_b32 v64, v36, v32, s34
v_perm_b32 v65, v44, v40, s34
v_perm_b32 v66, v52, v48, s34
v_perm_b32 v67, v60, v56, s34
v_accvgpr_write_b32 a0, v64
v_accvgpr_write_b32 a1, v65
v_accvgpr_write_b32 a2, v66
v_accvgpr_write_b32 a3, v67
v_perm_b32 v64, v36, v32, s35
v_perm_b32 v65, v44, v40, s35
v_perm_b32 v66, v52, v48, s35
v_perm_b32 v67, v60, v56, s35
v_accvgpr_write_b32 a4, v64
v_accvgpr_write_b32 a5, v65
v_accvgpr_write_b32 a6, v66
v_accvgpr_write_b32 a7, v67
v_perm_b32 v64, v37, v33, s34
v_perm_b32 v65, v45, v41, s34
v_perm_b32 v66, v53, v49, s34
v_perm_b32 v67, v61, v57, s34
v_accvgpr_write_b32 a8, v64
v_accvgpr_write_b32 a9, v65
v_accvgpr_write_b32 a10, v66
v_accvgpr_write_b32 a11, v67
v_perm_b32 v64, v37, v33, s35
v_perm_b32 v65, v45, v41, s35
v_perm_b32 v66, v53, v49, s35
v_perm_b32 v67, v61, v57, s35
v_accvgpr_write_b32 a12, v64
v_accvgpr_write_b32 a13, v65
v_accvgpr_write_b32 a14, v66
v_accvgpr_write_b32 a15, v67
v_perm_b32 v64, v38, v34, s34
v_perm_b32 v65, v46, v42, s34
v_perm_b32 v66, v54, v50, s34
v_perm_b32 v67, v62, v58, s34
v_accvgpr_write_b32 a16, v64
v_accvgpr_write_b32 a17, v65
v_accvgpr_write_b32 a18, v66
v_accvgpr_write_b32 a19, v67
v_perm_b32 v64, v38, v34, s35
v_perm_b32 v65, v46, v42, s35
v_perm_b32 v66, v54, v50, s35
v_perm_b32 v67, v62, v58, s35
v_accvgpr_write_b32 a20, v64
v_accvgpr_write_b32 a21, v65
v_accvgpr_write_b32 a22, v66
v_accvgpr_write_b32 a23, v67
v_perm_b32 v64, v39, v35, s34
v_perm_b32 v65, v47, v43, s34
v_perm_b32 v66, v55, v51, s34
v_perm_b32 v67, v63, v59, s34
v_accvgpr_write_b32 a24, v64
v_accvgpr_write_b32 a25, v65
v_accvgpr_write_b32 a26, v66
v_accvgpr_write_b32 a27, v67
v_perm_b32 v64, v39, v35, s35
v_perm_b32 v65, v47, v43, s35
v_perm_b32 v66, v55, v51, s35
v_perm_b32 v67, v63, v59, s35
v_accvgpr_write_b32 a28, v64
v_accvgpr_write_b32 a29, v65
v_accvgpr_write_b32 a30, v66
v_accvgpr_write_b32 a31, v67
s_waitcnt vmcnt(48)
v_accvgpr_read_b32 v32, a32
v_accvgpr_read_b32 v33, a33
v_accvgpr_read_b32 v34, a34
v_accvgpr_read_b32 v35, a35
v_accvgpr_read_b32 v36, a36
v_accvgpr_read_b32 v37, a37
v_accvgpr_read_b32 v38, a38
v_accvgpr_read_b32 v39, a39
v_accvgpr_read_b32 v40, a40
v_accvgpr_read_b32 v41, a41
v_accvgpr_read_b32 v42, a42
v_accvgpr_read_b32 v43, a43
v_accvgpr_read_b32 v44, a44
v_accvgpr_read_b32 v45, a45
v_accvgpr_read_b32 v46, a46
v_accvgpr_read_b32 v47, a47
v_accvgpr_read_b32 v48, a48
v_accvgpr_read_b32 v49, a49
v_accvgpr_read_b32 v50, a50
v_accvgpr_read_b32 v51, a51
v_accvgpr_read_b32 v52, a52
v_accvgpr_read_b32 v53, a53
v_accvgpr_read_b32 v54, a54
v_accvgpr_read_b32 v55, a55
v_accvgpr_read_b32 v56, a56
v_accvgpr_read_b32 v57, a57
v_accvgpr_read_b32 v58, a58
v_accvgpr_read_b32 v59, a59
v_accvgpr_read_b32 v60, a60
v_accvgpr_read_b32 v61, a61
v_accvgpr_read_b32 v62, a62
v_accvgpr_read_b32 v63, a63
v_perm_b32 v64, v36, v32, s34
v_perm_b32 v65, v44, v40, s34
v_perm_b32 v66, v52, v48, s34
v_perm_b32 v67, v60, v56, s34
v_accvgpr_write_b32 a32, v64
v_accvgpr_write_b32 a33, v65
v_accvgpr_write_b32 a34, v66
v_accvgpr_write_b32 a35, v67
v_perm_b32 v64, v36, v32, s35
v_perm_b32 v65, v44, v40, s35
v_perm_b32 v66, v52, v48, s35
v_perm_b32 v67, v60, v56, s35
v_accvgpr_write_b32 a36, v64
v_accvgpr_write_b32 a37, v65
v_accvgpr_write_b32 a38, v66
v_accvgpr_write_b32 a39, v67
v_perm_b32 v64, v37, v33, s34
v_perm_b32 v65, v45, v41, s34
v_perm_b32 v66, v53, v49, s34
v_perm_b32 v67, v61, v57, s34
v_accvgpr_write_b32 a40, v64
v_accvgpr_write_b32 a41, v65
v_accvgpr_write_b32 a42, v66
v_accvgpr_write_b32 a43, v67
v_perm_b32 v64, v37, v33, s35
v_perm_b32 v65, v45, v41, s35
v_perm_b32 v66, v53, v49, s35
v_perm_b32 v67, v61, v57, s35
v_accvgpr_write_b32 a44, v64
v_accvgpr_write_b32 a45, v65
v_accvgpr_write_b32 a46, v66
v_accvgpr_write_b32 a47, v67
v_perm_b32 v64, v38, v34, s34
v_perm_b32 v65, v46, v42, s34
v_perm_b32 v66, v54, v50, s34
v_perm_b32 v67, v62, v58, s34
v_accvgpr_write_b32 a48, v64
v_accvgpr_write_b32 a49, v65
v_accvgpr_write_b32 a50, v66
v_accvgpr_write_b32 a51, v67
v_perm_b32 v64, v38, v34, s35
v_perm_b32 v65, v46, v42, s35
v_perm_b32 v66, v54, v50, s35
v_perm_b32 v67, v62, v58, s35
v_accvgpr_write_b32 a52, v64
v_accvgpr_write_b32 a53, v65
v_accvgpr_write_b32 a54, v66
v_accvgpr_write_b32 a55, v67
v_perm_b32 v64, v39, v35, s34
v_perm_b32 v65, v47, v43, s34
v_perm_b32 v66, v55, v51, s34
v_perm_b32 v67, v63, v59, s34
v_accvgpr_write_b32 a56, v64
v_accvgpr_write_b32 a57, v65
v_accvgpr_write_b32 a58, v66
v_accvgpr_write_b32 a59, v67
v_perm_b32 v64, v39, v35, s35
v_perm_b32 v65, v47, v43, s35
v_perm_b32 v66, v55, v51, s35
v_perm_b32 v67, v63, v59, s35
v_accvgpr_write_b32 a60, v64
v_accvgpr_write_b32 a61, v65
v_accvgpr_write_b32 a62, v66
v_accvgpr_write_b32 a63, v67
s_waitcnt vmcnt(40)
v_accvgpr_read_b32 v32, a64
v_accvgpr_read_b32 v33, a65
v_accvgpr_read_b32 v34, a66
v_accvgpr_read_b32 v35, a67
v_accvgpr_read_b32 v36, a68
v_accvgpr_read_b32 v37, a69
v_accvgpr_read_b32 v38, a70
v_accvgpr_read_b32 v39, a71
v_accvgpr_read_b32 v40, a72
v_accvgpr_read_b32 v41, a73
v_accvgpr_read_b32 v42, a74
v_accvgpr_read_b32 v43, a75
v_accvgpr_read_b32 v44, a76
v_accvgpr_read_b32 v45, a77
v_accvgpr_read_b32 v46, a78
v_accvgpr_read_b32 v47, a79
v_accvgpr_read_b32 v48, a80
v_accvgpr_read_b32 v49, a81
v_accvgpr_read_b32 v50, a82
v_accvgpr_read_b32 v51, a83
v_accvgpr_read_b32 v52, a84
v_accvgpr_read_b32 v53, a85
v_accvgpr_read_b32 v54, a86
v_accvgpr_read_b32 v55, a87
v_accvgpr_read_b32 v56, a88
v_accvgpr_read_b32 v57, a89
v_accvgpr_read_b32 v58, a90
v_accvgpr_read_b32 v59, a91
v_accvgpr_read_b32 v60, a92
v_accvgpr_read_b32 v61, a93
v_accvgpr_read_b32 v62, a94
v_accvgpr_read_b32 v63, a95
v_perm_b32 v64, v36, v32, s34
v_perm_b32 v65, v44, v40, s34
v_perm_b32 v66, v52, v48, s34
v_perm_b32 v67, v60, v56, s34
v_accvgpr_write_b32 a64, v64
v_accvgpr_write_b32 a65, v65
v_accvgpr_write_b32 a66, v66
v_accvgpr_write_b32 a67, v67
v_perm_b32 v64, v36, v32, s35
v_perm_b32 v65, v44, v40, s35
v_perm_b32 v66, v52, v48, s35
v_perm_b32 v67, v60, v56, s35
v_accvgpr_write_b32 a68, v64
v_accvgpr_write_b32 a69, v65
v_accvgpr_write_b32 a70, v66
v_accvgpr_write_b32 a71, v67
v_perm_b32 v64, v37, v33, s34
v_perm_b32 v65, v45, v41, s34
v_perm_b32 v66, v53, v49, s34
v_perm_b32 v67, v61, v57, s34
v_accvgpr_write_b32 a72, v64
v_accvgpr_write_b32 a73, v65
v_accvgpr_write_b32 a74, v66
v_accvgpr_write_b32 a75, v67
v_perm_b32 v64, v37, v33, s35
v_perm_b32 v65, v45, v41, s35
v_perm_b32 v66, v53, v49, s35
v_perm_b32 v67, v61, v57, s35
v_accvgpr_write_b32 a76, v64
v_accvgpr_write_b32 a77, v65
v_accvgpr_write_b32 a78, v66
v_accvgpr_write_b32 a79, v67
v_perm_b32 v64, v38, v34, s34
v_perm_b32 v65, v46, v42, s34
v_perm_b32 v66, v54, v50, s34
v_perm_b32 v67, v62, v58, s34
v_accvgpr_write_b32 a80, v64
v_accvgpr_write_b32 a81, v65
v_accvgpr_write_b32 a82, v66
v_accvgpr_write_b32 a83, v67
v_perm_b32 v64, v38, v34, s35
v_perm_b32 v65, v46, v42, s35
v_perm_b32 v66, v54, v50, s35
v_perm_b32 v67, v62, v58, s35
v_accvgpr_write_b32 a84, v64
v_accvgpr_write_b32 a85, v65
v_accvgpr_write_b32 a86, v66
v_accvgpr_write_b32 a87, v67
v_perm_b32 v64, v39, v35, s34
v_perm_b32 v65, v47, v43, s34
v_perm_b32 v66, v55, v51, s34
v_perm_b32 v67, v63, v59, s34
v_accvgpr_write_b32 a88, v64
v_accvgpr_write_b32 a89, v65
v_accvgpr_write_b32 a90, v66
v_accvgpr_write_b32 a91, v67
v_perm_b32 v64, v39, v35, s35
v_perm_b32 v65, v47, v43, s35
v_perm_b32 v66, v55, v51, s35
v_perm_b32 v67, v63, v59, s35
v_accvgpr_write_b32 a92, v64
v_accvgpr_write_b32 a93, v65
v_accvgpr_write_b32 a94, v66
v_accvgpr_write_b32 a95, v67
s_waitcnt vmcnt(32)
v_accvgpr_read_b32 v32, a96
v_accvgpr_read_b32 v33, a97
v_accvgpr_read_b32 v34, a98
v_accvgpr_read_b32 v35, a99
v_accvgpr_read_b32 v36, a100
v_accvgpr_read_b32 v37, a101
v_accvgpr_read_b32 v38, a102
v_accvgpr_read_b32 v39, a103
v_accvgpr_read_b32 v40, a104
v_accvgpr_read_b32 v41, a105
v_accvgpr_read_b32 v42, a106
v_accvgpr_read_b32 v43, a107
v_accvgpr_read_b32 v44, a108
v_accvgpr_read_b32 v45, a109
v_accvgpr_read_b32 v46, a110
v_accvgpr_read_b32 v47, a111
v_accvgpr_read_b32 v48, a112
v_accvgpr_read_b32 v49, a113
v_accvgpr_read_b32 v50, a114
v_accvgpr_read_b32 v51, a115
v_accvgpr_read_b32 v52, a116
v_accvgpr_read_b32 v53, a117
v_accvgpr_read_b32 v54, a118
v_accvgpr_read_b32 v55, a119
v_accvgpr_read_b32 v56, a120
v_accvgpr_read_b32 v57, a121
v_accvgpr_read_b32 v58, a122
v_accvgpr_read_b32 v59, a123
v_accvgpr_read_b32 v60, a124
v_accvgpr_read_b32 v61, a125
v_accvgpr_read_b32 v62, a126
v_accvgpr_read_b32 v63, a127
v_perm_b32 v64, v36, v32, s34
v_perm_b32 v65, v44, v40, s34
v_perm_b32 v66, v52, v48, s34
v_perm_b32 v67, v60, v56, s34
v_accvgpr_write_b32 a96, v64
v_accvgpr_write_b32 a97, v65
v_accvgpr_write_b32 a98, v66
v_accvgpr_write_b32 a99, v67
v_perm_b32 v64, v36, v32, s35
v_perm_b32 v65, v44, v40, s35
v_perm_b32 v66, v52, v48, s35
v_perm_b32 v67, v60, v56, s35
v_accvgpr_write_b32 a100, v64
v_accvgpr_write_b32 a101, v65
v_accvgpr_write_b32 a102, v66
v_accvgpr_write_b32 a103, v67
v_perm_b32 v64, v37, v33, s34
v_perm_b32 v65, v45, v41, s34
v_perm_b32 v66, v53, v49, s34
v_perm_b32 v67, v61, v57, s34
v_accvgpr_write_b32 a104, v64
v_accvgpr_write_b32 a105, v65
v_accvgpr_write_b32 a106, v66
v_accvgpr_write_b32 a107, v67
v_perm_b32 v64, v37, v33, s35
v_perm_b32 v65, v45, v41, s35
v_perm_b32 v66, v53, v49, s35
v_perm_b32 v67, v61, v57, s35
v_accvgpr_write_b32 a108, v64
v_accvgpr_write_b32 a109, v65
v_accvgpr_write_b32 a110, v66
v_accvgpr_write_b32 a111, v67
v_perm_b32 v64, v38, v34, s34
v_perm_b32 v65, v46, v42, s34
v_perm_b32 v66, v54, v50, s34
v_perm_b32 v67, v62, v58, s34
v_accvgpr_write_b32 a112, v64
v_accvgpr_write_b32 a113, v65
v_accvgpr_write_b32 a114, v66
v_accvgpr_write_b32 a115, v67
v_perm_b32 v64, v38, v34, s35
v_perm_b32 v65, v46, v42, s35
v_perm_b32 v66, v54, v50, s35
v_perm_b32 v67, v62, v58, s35
v_accvgpr_write_b32 a116, v64
v_accvgpr_write_b32 a117, v65
v_accvgpr_write_b32 a118, v66
v_accvgpr_write_b32 a119, v67
v_perm_b32 v64, v39, v35, s34
v_perm_b32 v65, v47, v43, s34
v_perm_b32 v66, v55, v51, s34
v_perm_b32 v67, v63, v59, s34
v_accvgpr_write_b32 a120, v64
v_accvgpr_write_b32 a121, v65
v_accvgpr_write_b32 a122, v66
v_accvgpr_write_b32 a123, v67
v_perm_b32 v64, v39, v35, s35
v_perm_b32 v65, v47, v43, s35
v_perm_b32 v66, v55, v51, s35
v_perm_b32 v67, v63, v59, s35
v_accvgpr_write_b32 a124, v64
v_accvgpr_write_b32 a125, v65
v_accvgpr_write_b32 a126, v66
v_accvgpr_write_b32 a127, v67
s_waitcnt vmcnt(24)
v_accvgpr_read_b32 v32, a128
v_accvgpr_read_b32 v33, a129
v_accvgpr_read_b32 v34, a130
v_accvgpr_read_b32 v35, a131
v_accvgpr_read_b32 v36, a132
v_accvgpr_read_b32 v37, a133
v_accvgpr_read_b32 v38, a134
v_accvgpr_read_b32 v39, a135
v_accvgpr_read_b32 v40, a136
v_accvgpr_read_b32 v41, a137
v_accvgpr_read_b32 v42, a138
v_accvgpr_read_b32 v43, a139
v_accvgpr_read_b32 v44, a140
v_accvgpr_read_b32 v45, a141
v_accvgpr_read_b32 v46, a142
v_accvgpr_read_b32 v47, a143
v_accvgpr_read_b32 v48, a144
v_accvgpr_read_b32 v49, a145
v_accvgpr_read_b32 v50, a146
v_accvgpr_read_b32 v51, a147
v_accvgpr_read_b32 v52, a148
v_accvgpr_read_b32 v53, a149
v_accvgpr_read_b32 v54, a150
v_accvgpr_read_b32 v55, a151
v_accvgpr_read_b32 v56, a152
v_accvgpr_read_b32 v57, a153
v_accvgpr_read_b32 v58, a154
v_accvgpr_read_b32 v59, a155
v_accvgpr_read_b32 v60, a156
v_accvgpr_read_b32 v61, a157
v_accvgpr_read_b32 v62, a158
v_accvgpr_read_b32 v63, a159
v_perm_b32 v64, v36, v32, s34
v_perm_b32 v65, v44, v40, s34
v_perm_b32 v66, v52, v48, s34
v_perm_b32 v67, v60, v56, s34
v_accvgpr_write_b32 a128, v64
v_accvgpr_write_b32 a129, v65
v_accvgpr_write_b32 a130, v66
v_accvgpr_write_b32 a131, v67
v_perm_b32 v64, v36, v32, s35
v_perm_b32 v65, v44, v40, s35
v_perm_b32 v66, v52, v48, s35
v_perm_b32 v67, v60, v56, s35
v_accvgpr_write_b32 a132, v64
v_accvgpr_write_b32 a133, v65
v_accvgpr_write_b32 a134, v66
v_accvgpr_write_b32 a135, v67
v_perm_b32 v64, v37, v33, s34
v_perm_b32 v65, v45, v41, s34
v_perm_b32 v66, v53, v49, s34
v_perm_b32 v67, v61, v57, s34
v_accvgpr_write_b32 a136, v64
v_accvgpr_write_b32 a137, v65
v_accvgpr_write_b32 a138, v66
v_accvgpr_write_b32 a139, v67
v_perm_b32 v64, v37, v33, s35
v_perm_b32 v65, v45, v41, s35
v_perm_b32 v66, v53, v49, s35
v_perm_b32 v67, v61, v57, s35
v_accvgpr_write_b32 a140, v64
v_accvgpr_write_b32 a141, v65
v_accvgpr_write_b32 a142, v66
v_accvgpr_write_b32 a143, v67
v_perm_b32 v64, v38, v34, s34
v_perm_b32 v65, v46, v42, s34
v_perm_b32 v66, v54, v50, s34
v_perm_b32 v67, v62, v58, s34
v_accvgpr_write_b32 a144, v64
v_accvgpr_write_b32 a145, v65
v_accvgpr_write_b32 a146, v66
v_accvgpr_write_b32 a147, v67
v_perm_b32 v64, v38, v34, s35
v_perm_b32 v65, v46, v42, s35
v_perm_b32 v66, v54, v50, s35
v_perm_b32 v67, v62, v58, s35
v_accvgpr_write_b32 a148, v64
v_accvgpr_write_b32 a149, v65
v_accvgpr_write_b32 a150, v66
v_accvgpr_write_b32 a151, v67
v_perm_b32 v64, v39, v35, s34
v_perm_b32 v65, v47, v43, s34
v_perm_b32 v66, v55, v51, s34
v_perm_b32 v67, v63, v59, s34
v_accvgpr_write_b32 a152, v64
v_accvgpr_write_b32 a153, v65
v_accvgpr_write_b32 a154, v66
v_accvgpr_write_b32 a155, v67
v_perm_b32 v64, v39, v35, s35
v_perm_b32 v65, v47, v43, s35
v_perm_b32 v66, v55, v51, s35
v_perm_b32 v67, v63, v59, s35
v_accvgpr_write_b32 a156, v64
v_accvgpr_write_b32 a157, v65
v_accvgpr_write_b32 a158, v66
v_accvgpr_write_b32 a159, v67
s_waitcnt vmcnt(16)
v_accvgpr_read_b32 v32, a160
v_accvgpr_read_b32 v33, a161
v_accvgpr_read_b32 v34, a162
v_accvgpr_read_b32 v35, a163
v_accvgpr_read_b32 v36, a164
v_accvgpr_read_b32 v37, a165
v_accvgpr_read_b32 v38, a166
v_accvgpr_read_b32 v39, a167
v_accvgpr_read_b32 v40, a168
v_accvgpr_read_b32 v41, a169
v_accvgpr_read_b32 v42, a170
v_accvgpr_read_b32 v43, a171
v_accvgpr_read_b32 v44, a172
v_accvgpr_read_b32 v45, a173
v_accvgpr_read_b32 v46, a174
v_accvgpr_read_b32 v47, a175
v_accvgpr_read_b32 v48, a176
v_accvgpr_read_b32 v49, a177
v_accvgpr_read_b32 v50, a178
v_accvgpr_read_b32 v51, a179
v_accvgpr_read_b32 v52, a180
v_accvgpr_read_b32 v53, a181
v_accvgpr_read_b32 v54, a182
v_accvgpr_read_b32 v55, a183
v_accvgpr_read_b32 v56, a184
v_accvgpr_read_b32 v57, a185
v_accvgpr_read_b32 v58, a186
v_accvgpr_read_b32 v59, a187
v_accvgpr_read_b32 v60, a188
v_accvgpr_read_b32 v61, a189
v_accvgpr_read_b32 v62, a190
v_accvgpr_read_b32 v63, a191
v_perm_b32 v64, v36, v32, s34
v_perm_b32 v65, v44, v40, s34
v_perm_b32 v66, v52, v48, s34
v_perm_b32 v67, v60, v56, s34
v_accvgpr_write_b32 a160, v64
v_accvgpr_write_b32 a161, v65
v_accvgpr_write_b32 a162, v66
v_accvgpr_write_b32 a163, v67
v_perm_b32 v64, v36, v32, s35
v_perm_b32 v65, v44, v40, s35
v_perm_b32 v66, v52, v48, s35
v_perm_b32 v67, v60, v56, s35
v_accvgpr_write_b32 a164, v64
v_accvgpr_write_b32 a165, v65
v_accvgpr_write_b32 a166, v66
v_accvgpr_write_b32 a167, v67
v_perm_b32 v64, v37, v33, s34
v_perm_b32 v65, v45, v41, s34
v_perm_b32 v66, v53, v49, s34
v_perm_b32 v67, v61, v57, s34
v_accvgpr_write_b32 a168, v64
v_accvgpr_write_b32 a169, v65
v_accvgpr_write_b32 a170, v66
v_accvgpr_write_b32 a171, v67
v_perm_b32 v64, v37, v33, s35
v_perm_b32 v65, v45, v41, s35
v_perm_b32 v66, v53, v49, s35
v_perm_b32 v67, v61, v57, s35
v_accvgpr_write_b32 a172, v64
v_accvgpr_write_b32 a173, v65
v_accvgpr_write_b32 a174, v66
v_accvgpr_write_b32 a175, v67
v_perm_b32 v64, v38, v34, s34
v_perm_b32 v65, v46, v42, s34
v_perm_b32 v66, v54, v50, s34
v_perm_b32 v67, v62, v58, s34
v_accvgpr_write_b32 a176, v64
v_accvgpr_write_b32 a177, v65
v_accvgpr_write_b32 a178, v66
v_accvgpr_write_b32 a179, v67
v_perm_b32 v64, v38, v34, s35
v_perm_b32 v65, v46, v42, s35
v_perm_b32 v66, v54, v50, s35
v_perm_b32 v67, v62, v58, s35
v_accvgpr_write_b32 a180, v64
v_accvgpr_write_b32 a181, v65
v_accvgpr_write_b32 a182, v66
v_accvgpr_write_b32 a183, v67
v_perm_b32 v64, v39, v35, s34
v_perm_b32 v65, v47, v43, s34
v_perm_b32 v66, v55, v51, s34
v_perm_b32 v67, v63, v59, s34
v_accvgpr_write_b32 a184, v64
v_accvgpr_write_b32 a185, v65
v_accvgpr_write_b32 a186, v66
v_accvgpr_write_b32 a187, v67
v_perm_b32 v64, v39, v35, s35
v_perm_b32 v65, v47, v43, s35
v_perm_b32 v66, v55, v51, s35
v_perm_b32 v67, v63, v59, s35
v_accvgpr_write_b32 a188, v64
v_accvgpr_write_b32 a189, v65
v_accvgpr_write_b32 a190, v66
v_accvgpr_write_b32 a191, v67
s_waitcnt vmcnt(8)
v_accvgpr_read_b32 v32, a192
v_accvgpr_read_b32 v33, a193
v_accvgpr_read_b32 v34, a194
v_accvgpr_read_b32 v35, a195
v_accvgpr_read_b32 v36, a196
v_accvgpr_read_b32 v37, a197
v_accvgpr_read_b32 v38, a198
v_accvgpr_read_b32 v39, a199
v_accvgpr_read_b32 v40, a200
v_accvgpr_read_b32 v41, a201
v_accvgpr_read_b32 v42, a202
v_accvgpr_read_b32 v43, a203
v_accvgpr_read_b32 v44, a204
v_accvgpr_read_b32 v45, a205
v_accvgpr_read_b32 v46, a206
v_accvgpr_read_b32 v47, a207
v_accvgpr_read_b32 v48, a208
v_accvgpr_read_b32 v49, a209
v_accvgpr_read_b32 v50, a210
v_accvgpr_read_b32 v51, a211
v_accvgpr_read_b32 v52, a212
v_accvgpr_read_b32 v53, a213
v_accvgpr_read_b32 v54, a214
v_accvgpr_read_b32 v55, a215
v_accvgpr_read_b32 v56, a216
v_accvgpr_read_b32 v57, a217
v_accvgpr_read_b32 v58, a218
v_accvgpr_read_b32 v59, a219
v_accvgpr_read_b32 v60, a220
v_accvgpr_read_b32 v61, a221
v_accvgpr_read_b32 v62, a222
v_accvgpr_read_b32 v63, a223
v_perm_b32 v64, v36, v32, s34
v_perm_b32 v65, v44, v40, s34
v_perm_b32 v66, v52, v48, s34
v_perm_b32 v67, v60, v56, s34
v_accvgpr_write_b32 a192, v64
v_accvgpr_write_b32 a193, v65
v_accvgpr_write_b32 a194, v66
v_accvgpr_write_b32 a195, v67
v_perm_b32 v64, v36, v32, s35
v_perm_b32 v65, v44, v40, s35
v_perm_b32 v66, v52, v48, s35
v_perm_b32 v67, v60, v56, s35
v_accvgpr_write_b32 a196, v64
v_accvgpr_write_b32 a197, v65
v_accvgpr_write_b32 a198, v66
v_accvgpr_write_b32 a199, v67
v_perm_b32 v64, v37, v33, s34
v_perm_b32 v65, v45, v41, s34
v_perm_b32 v66, v53, v49, s34
v_perm_b32 v67, v61, v57, s34
v_accvgpr_write_b32 a200, v64
v_accvgpr_write_b32 a201, v65
v_accvgpr_write_b32 a202, v66
v_accvgpr_write_b32 a203, v67
v_perm_b32 v64, v37, v33, s35
v_perm_b32 v65, v45, v41, s35
v_perm_b32 v66, v53, v49, s35
v_perm_b32 v67, v61, v57, s35
v_accvgpr_write_b32 a204, v64
v_accvgpr_write_b32 a205, v65
v_accvgpr_write_b32 a206, v66
v_accvgpr_write_b32 a207, v67
v_perm_b32 v64, v38, v34, s34
v_perm_b32 v65, v46, v42, s34
v_perm_b32 v66, v54, v50, s34
v_perm_b32 v67, v62, v58, s34
v_accvgpr_write_b32 a208, v64
v_accvgpr_write_b32 a209, v65
v_accvgpr_write_b32 a210, v66
v_accvgpr_write_b32 a211, v67
v_perm_b32 v64, v38, v34, s35
v_perm_b32 v65, v46, v42, s35
v_perm_b32 v66, v54, v50, s35
v_perm_b32 v67, v62, v58, s35
v_accvgpr_write_b32 a212, v64
v_accvgpr_write_b32 a213, v65
v_accvgpr_write_b32 a214, v66
v_accvgpr_write_b32 a215, v67
v_perm_b32 v64, v39, v35, s34
v_perm_b32 v65, v47, v43, s34
v_perm_b32 v66, v55, v51, s34
v_perm_b32 v67, v63, v59, s34
v_accvgpr_write_b32 a216, v64
v_accvgpr_write_b32 a217, v65
v_accvgpr_write_b32 a218, v66
v_accvgpr_write_b32 a219, v67
v_perm_b32 v64, v39, v35, s35
v_perm_b32 v65, v47, v43, s35
v_perm_b32 v66, v55, v51, s35
v_perm_b32 v67, v63, v59, s35
v_accvgpr_write_b32 a220, v64
v_accvgpr_write_b32 a221, v65
v_accvgpr_write_b32 a222, v66
v_accvgpr_write_b32 a223, v67
s_waitcnt vmcnt(0)
v_accvgpr_read_b32 v32, a224
v_accvgpr_read_b32 v33, a225
v_accvgpr_read_b32 v34, a226
v_accvgpr_read_b32 v35, a227
v_accvgpr_read_b32 v36, a228
v_accvgpr_read_b32 v37, a229
v_accvgpr_read_b32 v38, a230
v_accvgpr_read_b32 v39, a231
v_accvgpr_read_b32 v40, a232
v_accvgpr_read_b32 v41, a233
v_accvgpr_read_b32 v42, a234
v_accvgpr_read_b32 v43, a235
v_accvgpr_read_b32 v44, a236
v_accvgpr_read_b32 v45, a237
v_accvgpr_read_b32 v46, a238
v_accvgpr_read_b32 v47, a239
v_accvgpr_read_b32 v48, a240
v_accvgpr_read_b32 v49, a241
v_accvgpr_read_b32 v50, a242
v_accvgpr_read_b32 v51, a243
v_accvgpr_read_b32 v52, a244
v_accvgpr_read_b32 v53, a245
v_accvgpr_read_b32 v54, a246
v_accvgpr_read_b32 v55, a247
v_accvgpr_read_b32 v56, a248
v_accvgpr_read_b32 v57, a249
v_accvgpr_read_b32 v58, a250
v_accvgpr_read_b32 v59, a251
v_accvgpr_read_b32 v60, a252
v_accvgpr_read_b32 v61, a253
v_accvgpr_read_b32 v62, a254
v_accvgpr_read_b32 v63, a255
v_perm_b32 v64, v36, v32, s34
v_perm_b32 v65, v44, v40, s34
v_perm_b32 v66, v52, v48, s34
v_perm_b32 v67, v60, v56, s34
v_accvgpr_write_b32 a224, v64
v_accvgpr_write_b32 a225, v65
v_accvgpr_write_b32 a226, v66
v_accvgpr_write_b32 a227, v67
v_perm_b32 v64, v36, v32, s35
v_perm_b32 v65, v44, v40, s35
v_perm_b32 v66, v52, v48, s35
v_perm_b32 v67, v60, v56, s35
v_accvgpr_write_b32 a228, v64
v_accvgpr_write_b32 a229, v65
v_accvgpr_write_b32 a230, v66
v_accvgpr_write_b32 a231, v67
v_perm_b32 v64, v37, v33, s34
v_perm_b32 v65, v45, v41, s34
v_perm_b32 v66, v53, v49, s34
v_perm_b32 v67, v61, v57, s34
v_accvgpr_write_b32 a232, v64
v_accvgpr_write_b32 a233, v65
v_accvgpr_write_b32 a234, v66
v_accvgpr_write_b32 a235, v67
v_perm_b32 v64, v37, v33, s35
v_perm_b32 v65, v45, v41, s35
v_perm_b32 v66, v53, v49, s35
v_perm_b32 v67, v61, v57, s35
v_accvgpr_write_b32 a236, v64
v_accvgpr_write_b32 a237, v65
v_accvgpr_write_b32 a238, v66
v_accvgpr_write_b32 a239, v67
v_perm_b32 v64, v38, v34, s34
v_perm_b32 v65, v46, v42, s34
v_perm_b32 v66, v54, v50, s34
v_perm_b32 v67, v62, v58, s34
v_accvgpr_write_b32 a240, v64
v_accvgpr_write_b32 a241, v65
v_accvgpr_write_b32 a242, v66
v_accvgpr_write_b32 a243, v67
v_perm_b32 v64, v38, v34, s35
v_perm_b32 v65, v46, v42, s35
v_perm_b32 v66, v54, v50, s35
v_perm_b32 v67, v62, v58, s35
v_accvgpr_write_b32 a244, v64
v_accvgpr_write_b32 a245, v65
v_accvgpr_write_b32 a246, v66
v_accvgpr_write_b32 a247, v67
v_perm_b32 v64, v39, v35, s34
v_perm_b32 v65, v47, v43, s34
v_perm_b32 v66, v55, v51, s34
v_perm_b32 v67, v63, v59, s34
v_accvgpr_write_b32 a248, v64
v_accvgpr_write_b32 a249, v65
v_accvgpr_write_b32 a250, v66
v_accvgpr_write_b32 a251, v67
v_perm_b32 v64, v39, v35, s35
v_perm_b32 v65, v47, v43, s35
v_perm_b32 v66, v55, v51, s35
v_perm_b32 v67, v63, v59, s35
v_accvgpr_write_b32 a252, v64
v_accvgpr_write_b32 a253, v65
v_accvgpr_write_b32 a254, v66
v_accvgpr_write_b32 a255, v67
v_mov_b32 v136, 0x3f803f80
v_mov_b32 v137, 0x3f803f80
v_mov_b32 v138, 0x3f803f80
v_mov_b32 v139, 0x3f803f80
s_nop 1
v_mfma_f32_16x16x32_bf16 v[104:107], v[136:139], a[0:3], 0
v_mfma_f32_16x16x32_bf16 v[108:111], v[136:139], a[4:7], 0
v_mfma_f32_16x16x32_bf16 v[112:115], v[136:139], a[8:11], 0
v_mfma_f32_16x16x32_bf16 v[116:119], v[136:139], a[12:15], 0
v_mfma_f32_16x16x32_bf16 v[120:123], v[136:139], a[16:19], 0
v_mfma_f32_16x16x32_bf16 v[124:127], v[136:139], a[20:23], 0
v_mfma_f32_16x16x32_bf16 v[128:131], v[136:139], a[24:27], 0
v_mfma_f32_16x16x32_bf16 v[132:135], v[136:139], a[28:31], 0
v_mfma_f32_16x16x32_bf16 v[104:107], v[136:139], a[32:35], v[104:107]
v_mfma_f32_16x16x32_bf16 v[108:111], v[136:139], a[36:39], v[108:111]
v_mfma_f32_16x16x32_bf16 v[112:115], v[136:139], a[40:43], v[112:115]
v_mfma_f32_16x16x32_bf16 v[116:119], v[136:139], a[44:47], v[116:119]
v_mfma_f32_16x16x32_bf16 v[120:123], v[136:139], a[48:51], v[120:123]
v_mfma_f32_16x16x32_bf16 v[124:127], v[136:139], a[52:55], v[124:127]
v_mfma_f32_16x16x32_bf16 v[128:131], v[136:139], a[56:59], v[128:131]
v_mfma_f32_16x16x32_bf16 v[132:135], v[136:139], a[60:63], v[132:135]
v_mfma_f32_16x16x32_bf16 v[104:107], v[136:139], a[64:67], v[104:107]
v_mfma_f32_16x16x32_bf16 v[108:111], v[136:139], a[68:71], v[108:111]
v_mfma_f32_16x16x32_bf16 v[112:115], v[136:139], a[72:75], v[112:115]
v_mfma_f32_16x16x32_bf16 v[116:119], v[136:139], a[76:79], v[116:119]
v_mfma_f32_16x16x32_bf16 v[120:123], v[136:139], a[80:83], v[120:123]
v_mfma_f32_16x16x32_bf16 v[124:127], v[136:139], a[84:87], v[124:127]
v_mfma_f32_16x16x32_bf16 v[128:131], v[136:139], a[88:91], v[128:131]
v_mfma_f32_16x16x32_bf16 v[132:135], v[136:139], a[92:95], v[132:135]
v_mfma_f32_16x16x32_bf16 v[104:107], v[136:139], a[96:99], v[104:107]
v_mfma_f32_16x16x32_bf16 v[108:111], v[136:139], a[100:103], v[108:111]
v_mfma_f32_16x16x32_bf16 v[112:115], v[136:139], a[104:107], v[112:115]
v_mfma_f32_16x16x32_bf16 v[116:119], v[136:139], a[108:111], v[116:119]
v_mfma_f32_16x16x32_bf16 v[120:123], v[136:139], a[112:115], v[120:123]
v_mfma_f32_16x16x32_bf16 v[124:127], v[136:139], a[116:119], v[124:127]
v_mfma_f32_16x16x32_bf16 v[128:131], v[136:139], a[120:123], v[128:131]
v_mfma_f32_16x16x32_bf16 v[132:135], v[136:139], a[124:127], v[132:135]
s_nop 2
v_cndmask_b32 v168, v104, v112, s[40:41]
s_nop 2
v_cndmask_b32 v170, v120, v128, s[40:41]
v_cndmask_b32 v168, v168, v170, s[42:43]
v_cndmask_b32 v172, v108, v116, s[40:41]
v_cndmask_b32 v174, v124, v132, s[40:41]
v_cndmask_b32 v172, v172, v174, s[42:43]
v_mov_b32 v169, v172
ds_write_b64 v8, v[168:169] offset:4096
s_waitcnt lgkmcnt(0)
s_barrier
ds_read_b64 v[140:141], v9 offset:4096
ds_read_b64 v[142:143], v9 offset:4608
ds_read_b64 v[144:145], v9 offset:5120
ds_read_b64 v[146:147], v9 offset:5632
s_waitcnt lgkmcnt(0)
v_add_f32 v24, v140, v142
v_add_f32 v25, v141, v143
v_add_f32 v168, v144, v146
v_add_f32 v169, v145, v147
v_add_f32 v24, v24, v168
v_add_f32 v25, v25, v169
s_barrier
s_and_b32 s31, s28, 15
s_lshr_b64 s[28:29], s[28:29], 4
s_lshl_b32 s36, s31, 15
s_add_u32 s36, s36, 0x701000
s_and_b32 s47, s31, 7
s_lshl_b32 s37, s47, 12
s_add_u32 s37, s37, 0x801000
s_cmp_lt_u32 s31, 8
s_cselect_b32 s37, s36, s37
s_cselect_b64 s[56:57], -1, 0
v_cndmask_b32 v26, v5, v4, s[56:57]
buffer_load_dwordx4 v[32:35], v4, s[24:27], s36 offen sc1
s_add_u32 s46, s36, 0x800
buffer_load_dwordx4 v[36:39], v4, s[24:27], s46 offen sc1
s_add_u32 s46, s36, 0x1000
buffer_load_dwordx4 v[40:43], v4, s[24:27], s46 offen sc1
s_add_u32 s46, s36, 0x1800
buffer_load_dwordx4 v[44:47], v4, s[24:27], s46 offen sc1
s_add_u32 s46, s36, 0x2000
buffer_load_dwordx4 v[48:51], v4, s[24:27], s46 offen sc1
s_add_u32 s46, s36, 0x2800
buffer_load_dwordx4 v[52:55], v4, s[24:27], s46 offen sc1
s_add_u32 s46, s36, 0x3000
buffer_load_dwordx4 v[56:59], v4, s[24:27], s46 offen sc1
s_add_u32 s46, s36, 0x3800
buffer_load_dwordx4 v[60:63], v4, s[24:27], s46 offen sc1
buffer_load_dwordx4 v[64:67], v26, s[24:27], s37 offen sc1
s_waitcnt vmcnt(0)
Lmloop_1_%=:
s_mul_i32 s38, s31, 0x7000
s_add_u32 s38, s38, 0x781000
s_lshl_b32 s39, s31, 12
s_add_u32 s39, s39, 0x7f1000
s_and_b32 s49, s30, 1
s_lshl_b32 s49, s49, 11
s_cmp_lt_u32 s31, 8
s_cselect_b64 s[54:55], 0, -1
s_mov_b32 s33, 0
s_waitcnt vmcnt(1)
v_bitop3_b32 v184, v33, v35, s12 bitop3:0x7e
v_bitop3_b32 v185, v37, v39, s12 bitop3:0x7e
v_bitop3_b32 v186, v41, v43, s12 bitop3:0x7e
v_bitop3_b32 v187, v45, v47, s12 bitop3:0x7e
v_bitop3_b32 v188, v49, v51, s12 bitop3:0x7e
v_bitop3_b32 v189, v53, v55, s12 bitop3:0x7e
v_bitop3_b32 v190, v57, v59, s12 bitop3:0x7e
v_bitop3_b32 v191, v61, v63, s12 bitop3:0x7e
v_bitop3_b32 v192, v65, v67, s12 bitop3:0x7e
v_or3_b32 v184, v184, v185, v186
v_or3_b32 v187, v187, v188, v189
v_or3_b32 v190, v190, v191, v192
v_or3_b32 v184, v184, v187, v190
v_cmp_ne_u32 vcc, 0, v184
s_cbranch_vccz LmA_4_%=
s_lshr_b32 s61, s30, 2
s_mov_b32 s62, 0x1f00008f
s_cmp_eq_u32 s61, 1
s_cselect_b32 s62, 0x2f002f00, s62
s_cmp_eq_u32 s61, 2
s_cselect_b32 s62, 0x2f002f00, s62
s_cmp_eq_u32 s61, 3
s_cselect_b32 s62, 0x43352300, s62
s_and_b32 s61, s30, 3
s_lshl_b32 s61, s61, 3
s_lshr_b32 s62, s62, s61
s_and_b32 s62, s62, 0xff
s_lshl_b32 s62, s62, 3
Lmcg_8_%=:
s_memrealtime s[58:59]
s_waitcnt lgkmcnt(0)
s_sub_u32 s61, s58, s60
s_cmp_ge_u32 s61, s62
s_cbranch_scc1 Lmcx_9_%=
s_sleep 3
s_branch Lmcg_8_%=
Lmcx_9_%=:
buffer_load_dwordx4 v[32:35], v4, s[24:27], s36 offen sc1
s_add_u32 s46, s36, 0x800
buffer_load_dwordx4 v[36:39], v4, s[24:27], s46 offen sc1
s_add_u32 s46, s36, 0x1000
buffer_load_dwordx4 v[40:43], v4, s[24:27], s46 offen sc1
s_add_u32 s46, s36, 0x1800
buffer_load_dwordx4 v[44:47], v4, s[24:27], s46 offen sc1
s_add_u32 s46, s36, 0x2000
buffer_load_dwordx4 v[48:51], v4, s[24:27], s46 offen sc1
s_add_u32 s46, s36, 0x2800
buffer_load_dwordx4 v[52:55], v4, s[24:27], s46 offen sc1
s_add_u32 s46, s36, 0x3000
buffer_load_dwordx4 v[56:59], v4, s[24:27], s46 offen sc1
s_add_u32 s46, s36, 0x3800
buffer_load_dwordx4 v[60:63], v4, s[24:27], s46 offen sc1
buffer_load_dwordx4 v[64:67], v26, s[24:27], s37 offen sc1
s_sleep 8
Lml0_2_%=:
buffer_load_dwordx4 v[68:71], v4, s[24:27], s36 offen sc1
s_add_u32 s46, s36, 0x800
buffer_load_dwordx4 v[72:75], v4, s[24:27], s46 offen sc1
s_add_u32 s46, s36, 0x1000
buffer_load_dwordx4 v[76:79], v4, s[24:27], s46 offen sc1
s_add_u32 s46, s36, 0x1800
buffer_load_dwordx4 v[80:83], v4, s[24:27], s46 offen sc1
s_add_u32 s46, s36, 0x2000
buffer_load_dwordx4 v[84:87], v4, s[24:27], s46 offen sc1
s_add_u32 s46, s36, 0x2800
buffer_load_dwordx4 v[88:91], v4, s[24:27], s46 offen sc1
s_add_u32 s46, s36, 0x3000
buffer_load_dwordx4 v[92:95], v4, s[24:27], s46 offen sc1
s_add_u32 s46, s36, 0x3800
buffer_load_dwordx4 v[96:99], v4, s[24:27], s46 offen sc1
buffer_load_dwordx4 v[100:103], v26, s[24:27], s37 offen sc1
s_add_u32 s33, s33, 1
s_waitcnt vmcnt(9)
v_bitop3_b32 v184, v33, v35, s12 bitop3:0x7e
v_bitop3_b32 v185, v37, v39, s12 bitop3:0x7e
v_bitop3_b32 v186, v41, v43, s12 bitop3:0x7e
v_bitop3_b32 v187, v45, v47, s12 bitop3:0x7e
v_bitop3_b32 v188, v49, v51, s12 bitop3:0x7e
v_bitop3_b32 v189, v53, v55, s12 bitop3:0x7e
v_bitop3_b32 v190, v57, v59, s12 bitop3:0x7e
v_bitop3_b32 v191, v61, v63, s12 bitop3:0x7e
v_bitop3_b32 v192, v65, v67, s12 bitop3:0x7e
v_or3_b32 v184, v184, v185, v186
v_or3_b32 v187, v187, v188, v189
v_or3_b32 v190, v190, v191, v192
v_or3_b32 v184, v184, v187, v190
v_cmp_ne_u32 vcc, 0, v184
s_cbranch_vccz LmA_4_%=
buffer_load_dwordx4 v[32:35], v4, s[24:27], s36 offen sc1
s_add_u32 s46, s36, 0x800
buffer_load_dwordx4 v[36:39], v4, s[24:27], s46 offen sc1
s_add_u32 s46, s36, 0x1000
buffer_load_dwordx4 v[40:43], v4, s[24:27], s46 offen sc1
s_add_u32 s46, s36, 0x1800
buffer_load_dwordx4 v[44:47], v4, s[24:27], s46 offen sc1
s_add_u32 s46, s36, 0x2000
buffer_load_dwordx4 v[48:51], v4, s[24:27], s46 offen sc1
s_add_u32 s46, s36, 0x2800
buffer_load_dwordx4 v[52:55], v4, s[24:27], s46 offen sc1
s_add_u32 s46, s36, 0x3000
buffer_load_dwordx4 v[56:59], v4, s[24:27], s46 offen sc1
s_add_u32 s46, s36, 0x3800
buffer_load_dwordx4 v[60:63], v4, s[24:27], s46 offen sc1
buffer_load_dwordx4 v[64:67], v26, s[24:27], s37 offen sc1
s_waitcnt vmcnt(9)
v_bitop3_b32 v184, v69, v71, s12 bitop3:0x7e
v_bitop3_b32 v185, v73, v75, s12 bitop3:0x7e
v_bitop3_b32 v186, v77, v79, s12 bitop3:0x7e
v_bitop3_b32 v187, v81, v83, s12 bitop3:0x7e
v_bitop3_b32 v188, v85, v87, s12 bitop3:0x7e
v_bitop3_b32 v189, v89, v91, s12 bitop3:0x7e
v_bitop3_b32 v190, v93, v95, s12 bitop3:0x7e
v_bitop3_b32 v191, v97, v99, s12 bitop3:0x7e
v_bitop3_b32 v192, v101, v103, s12 bitop3:0x7e
v_or3_b32 v184, v184, v185, v186
v_or3_b32 v187, v187, v188, v189
v_or3_b32 v190, v190, v191, v192
v_or3_b32 v184, v184, v187, v190
v_cmp_ne_u32 vcc, 0, v184
s_cbranch_vccz LmB_5_%=
s_cmp_lt_u32 s33, 32768
s_cbranch_scc1 Lml0_2_%=
s_branch Lmab_7_%=
LmB_5_%=:
s_nop 7
s_nop 1
v_lshlrev_b32 v168, 16, v68
v_and_b32 v169, 0xffff0000, v68
v_lshlrev_b32 v170, 16, v70
v_and_b32 v171, 0xffff0000, v70
v_lshlrev_b32 v188, 16, v72
v_and_b32 v189, 0xffff0000, v72
v_lshlrev_b32 v190, 16, v74
v_and_b32 v191, 0xffff0000, v74
v_pk_add_f32 v[168:169], v[168:169], v[188:189]
v_pk_add_f32 v[170:171], v[170:171], v[190:191]
v_lshlrev_b32 v184, 16, v76
v_and_b32 v185, 0xffff0000, v76
v_lshlrev_b32 v186, 16, v78
v_and_b32 v187, 0xffff0000, v78
v_pk_add_f32 v[168:169], v[168:169], v[184:185]
v_pk_add_f32 v[170:171], v[170:171], v[186:187]
v_lshlrev_b32 v188, 16, v80
v_and_b32 v189, 0xffff0000, v80
v_lshlrev_b32 v190, 16, v82
v_and_b32 v191, 0xffff0000, v82
v_pk_add_f32 v[168:169], v[168:169], v[188:189]
v_pk_add_f32 v[170:171], v[170:171], v[190:191]
v_lshlrev_b32 v184, 16, v84
v_and_b32 v185, 0xffff0000, v84
v_lshlrev_b32 v186, 16, v86
v_and_b32 v187, 0xffff0000, v86
v_pk_add_f32 v[168:169], v[168:169], v[184:185]
v_pk_add_f32 v[170:171], v[170:171], v[186:187]
v_lshlrev_b32 v188, 16, v88
v_and_b32 v189, 0xffff0000, v88
v_lshlrev_b32 v190, 16, v90
v_and_b32 v191, 0xffff0000, v90
v_pk_add_f32 v[168:169], v[168:169], v[188:189]
v_pk_add_f32 v[170:171], v[170:171], v[190:191]
v_lshlrev_b32 v184, 16, v92
v_and_b32 v185, 0xffff0000, v92
v_lshlrev_b32 v186, 16, v94
v_and_b32 v187, 0xffff0000, v94
v_pk_add_f32 v[168:169], v[168:169], v[184:185]
v_pk_add_f32 v[170:171], v[170:171], v[186:187]
v_lshlrev_b32 v188, 16, v96
v_and_b32 v189, 0xffff0000, v96
v_lshlrev_b32 v190, 16, v98
v_and_b32 v191, 0xffff0000, v98
v_pk_add_f32 v[168:169], v[168:169], v[188:189]
v_pk_add_f32 v[170:171], v[170:171], v[190:191]
v_mov_b32 v27, v100
v_mov_b32 v28, v102
s_branch LmJ_6_%=
LmA_4_%=:
s_nop 1
v_lshlrev_b32 v168, 16, v32
v_and_b32 v169, 0xffff0000, v32
v_lshlrev_b32 v170, 16, v34
v_and_b32 v171, 0xffff0000, v34
v_lshlrev_b32 v188, 16, v36
v_and_b32 v189, 0xffff0000, v36
v_lshlrev_b32 v190, 16, v38
v_and_b32 v191, 0xffff0000, v38
v_pk_add_f32 v[168:169], v[168:169], v[188:189]
v_pk_add_f32 v[170:171], v[170:171], v[190:191]
v_lshlrev_b32 v184, 16, v40
v_and_b32 v185, 0xffff0000, v40
v_lshlrev_b32 v186, 16, v42
v_and_b32 v187, 0xffff0000, v42
v_pk_add_f32 v[168:169], v[168:169], v[184:185]
v_pk_add_f32 v[170:171], v[170:171], v[186:187]
v_lshlrev_b32 v188, 16, v44
v_and_b32 v189, 0xffff0000, v44
v_lshlrev_b32 v190, 16, v46
v_and_b32 v191, 0xffff0000, v46
v_pk_add_f32 v[168:169], v[168:169], v[188:189]
v_pk_add_f32 v[170:171], v[170:171], v[190:191]
v_lshlrev_b32 v184, 16, v48
v_and_b32 v185, 0xffff0000, v48
v_lshlrev_b32 v186, 16, v50
v_and_b32 v187, 0xffff0000, v50
v_pk_add_f32 v[168:169], v[168:169], v[184:185]
v_pk_add_f32 v[170:171], v[170:171], v[186:187]
v_lshlrev_b32 v188, 16, v52
v_and_b32 v189, 0xffff0000, v52
v_lshlrev_b32 v190, 16, v54
v_and_b32 v191, 0xffff0000, v54
v_pk_add_f32 v[168:169], v[168:169], v[188:189]
v_pk_add_f32 v[170:171], v[170:171], v[190:191]
v_lshlrev_b32 v184, 16, v56
v_and_b32 v185, 0xffff0000, v56
v_lshlrev_b32 v186, 16, v58
v_and_b32 v187, 0xffff0000, v58
v_pk_add_f32 v[168:169], v[168:169], v[184:185]
v_pk_add_f32 v[170:171], v[170:171], v[186:187]
v_lshlrev_b32 v188, 16, v60
v_and_b32 v189, 0xffff0000, v60
v_lshlrev_b32 v190, 16, v62
v_and_b32 v191, 0xffff0000, v62
v_pk_add_f32 v[168:169], v[168:169], v[188:189]
v_pk_add_f32 v[170:171], v[170:171], v[190:191]
v_mov_b32 v27, v64
v_mov_b32 v28, v66
LmJ_6_%=:
s_nop 1
s_cmp_ge_u32 s31, 8
s_cbranch_scc1 Lmk_10_%=
s_memrealtime s[60:61]
Lmk_10_%=:
s_and_b32 s31, s28, 15
s_lshr_b64 s[28:29], s[28:29], 4
s_lshl_b32 s36, s31, 15
s_add_u32 s36, s36, 0x701000
s_and_b32 s47, s31, 7
s_lshl_b32 s37, s47, 12
s_add_u32 s37, s37, 0x801000
s_cmp_lt_u32 s31, 8
s_cselect_b32 s37, s36, s37
s_cselect_b64 s[56:57], -1, 0
v_cndmask_b32 v26, v5, v4, s[56:57]
buffer_load_dwordx4 v[32:35], v4, s[24:27], s36 offen sc1
s_add_u32 s46, s36, 0x800
buffer_load_dwordx4 v[36:39], v4, s[24:27], s46 offen sc1
s_add_u32 s46, s36, 0x1000
buffer_load_dwordx4 v[40:43], v4, s[24:27], s46 offen sc1
s_add_u32 s46, s36, 0x1800
buffer_load_dwordx4 v[44:47], v4, s[24:27], s46 offen sc1
s_add_u32 s46, s36, 0x2000
buffer_load_dwordx4 v[48:51], v4, s[24:27], s46 offen sc1
s_add_u32 s46, s36, 0x2800
buffer_load_dwordx4 v[52:55], v4, s[24:27], s46 offen sc1
s_add_u32 s46, s36, 0x3000
buffer_load_dwordx4 v[56:59], v4, s[24:27], s46 offen sc1
s_add_u32 s46, s36, 0x3800
buffer_load_dwordx4 v[60:63], v4, s[24:27], s46 offen sc1
buffer_load_dwordx4 v[64:67], v26, s[24:27], s37 offen sc1
v_permlane32_swap_b32 v168, v170
v_permlane32_swap_b32 v169, v171
v_lshlrev_b32 v173, 16, v16
v_and_b32 v172, 0xffff0000, v16
v_add_f32 v168, v168, v170
v_add_f32 v169, v169, v171
v_cndmask_b32 v173, v173, v27, s[54:55]
v_cndmask_b32 v172, v172, v28, s[54:55]
v_add_f32 v168, v168, v173
v_add_f32 v169, v169, v172
v_cvt_pk_bf16_f32 v172, v168, v169
ds_write_b32 v6, v172
v_add_f32 v180, v168, v169
ds_read_b128 v[136:139], v7 offset:0
v_mov_b32 v208, v168
v_add_f32_dpp v180, v180, v180 quad_perm:[1,0,3,2] row_mask:0xf bank_mask:0xf bound_ctrl:1
v_mov_b32 v210, v169
ds_read_b128 v[140:143], v7 offset:64
v_add_f32_dpp v180, v180, v180 quad_perm:[2,3,0,1] row_mask:0xf bank_mask:0xf bound_ctrl:1
v_add_u32 v176, s49, v8
v_add_u32 v177, s49, v10
ds_read_b128 v[144:147], v7 offset:128
v_add_f32_dpp v180, v180, v180 row_half_mirror row_mask:0xf bank_mask:0xf bound_ctrl:1
v_mov_b32 v209, s12
v_mov_b32 v211, s12
ds_read_b128 v[148:151], v7 offset:192
v_add_f32_dpp v180, v180, v180 row_ror:8 row_mask:0xf bank_mask:0xf bound_ctrl:1
v_mov_b32 v182, v180
v_add_u32 v178, s49, v9
v_mov_b32 v179, s49
v_permlane16_swap_b32 v180, v182
v_add_f32 v180, v180, v182
v_mov_b32 v182, v180
s_nop 1
v_permlane32_swap_b32 v180, v182
v_add_f32 v180, v180, v182
v_mul_f32 v180, 0x3c000000, v180
v_sub_f32 v181, v168, v180
v_sub_f32 v183, v169, v180
v_mul_f32 v181, v181, v181
v_fma_f32 v181, v183, v183, v181
s_nop 1
v_add_f32_dpp v181, v181, v181 quad_perm:[1,0,3,2] row_mask:0xf bank_mask:0xf bound_ctrl:1
s_nop 1
v_add_f32_dpp v181, v181, v181 quad_perm:[2,3,0,1] row_mask:0xf bank_mask:0xf bound_ctrl:1
s_nop 1
v_add_f32_dpp v181, v181, v181 row_half_mirror row_mask:0xf bank_mask:0xf bound_ctrl:1
s_waitcnt lgkmcnt(3)
v_mfma_f32_16x16x32_bf16 v[108:111], v[136:139], a[4:7], 0
v_add_f32_dpp v181, v181, v181 row_ror:8 row_mask:0xf bank_mask:0xf bound_ctrl:1
v_mov_b32 v182, v181
v_mfma_f32_16x16x32_bf16 v[116:119], v[136:139], a[12:15], 0
s_nop 0
v_permlane16_swap_b32 v181, v182
v_add_f32 v181, v181, v182
v_mfma_f32_16x16x32_bf16 v[124:127], v[136:139], a[20:23], 0
v_mov_b32 v182, v181
v_mfma_f32_16x16x32_bf16 v[132:135], v[136:139], a[28:31], 0
s_nop 0
v_permlane32_swap_b32 v181, v182
v_add_f32 v181, v181, v182
v_mfma_f32_16x16x32_bf16 v[104:107], v[136:139], a[0:3], 0
v_mfma_f32_16x16x32_bf16 v[112:115], v[136:139], a[8:11], 0
v_mfma_f32_16x16x32_bf16 v[120:123], v[136:139], a[16:19], 0
v_mfma_f32_16x16x32_bf16 v[128:131], v[136:139], a[24:27], 0
s_waitcnt lgkmcnt(2)
v_mfma_f32_16x16x32_bf16 v[108:111], v[140:143], a[36:39], v[108:111]
v_mfma_f32_16x16x32_bf16 v[116:119], v[140:143], a[44:47], v[116:119]
v_mfma_f32_16x16x32_bf16 v[124:127], v[140:143], a[52:55], v[124:127]
v_mfma_f32_16x16x32_bf16 v[132:135], v[140:143], a[60:63], v[132:135]
v_mfma_f32_16x16x32_bf16 v[104:107], v[140:143], a[32:35], v[104:107]
v_mfma_f32_16x16x32_bf16 v[112:115], v[140:143], a[40:43], v[112:115]
v_mfma_f32_16x16x32_bf16 v[120:123], v[140:143], a[48:51], v[120:123]
v_mfma_f32_16x16x32_bf16 v[128:131], v[140:143], a[56:59], v[128:131]
s_waitcnt lgkmcnt(1)
v_mfma_f32_16x16x32_bf16 v[108:111], v[144:147], a[68:71], v[108:111]
v_mfma_f32_16x16x32_bf16 v[116:119], v[144:147], a[76:79], v[116:119]
v_mfma_f32_16x16x32_bf16 v[124:127], v[144:147], a[84:87], v[124:127]
v_mfma_f32_16x16x32_bf16 v[132:135], v[144:147], a[92:95], v[132:135]
v_mfma_f32_16x16x32_bf16 v[104:107], v[144:147], a[64:67], v[104:107]
v_mfma_f32_16x16x32_bf16 v[112:115], v[144:147], a[72:75], v[112:115]
v_mfma_f32_16x16x32_bf16 v[120:123], v[144:147], a[80:83], v[120:123]
v_mfma_f32_16x16x32_bf16 v[128:131], v[144:147], a[88:91], v[128:131]
s_waitcnt lgkmcnt(0)
v_mfma_f32_16x16x32_bf16 v[108:111], v[148:151], a[100:103], v[108:111]
v_mfma_f32_16x16x32_bf16 v[116:119], v[148:151], a[108:111], v[116:119]
v_mfma_f32_16x16x32_bf16 v[124:127], v[148:151], a[116:119], v[124:127]
v_mfma_f32_16x16x32_bf16 v[132:135], v[148:151], a[124:127], v[132:135]
v_mfma_f32_16x16x32_bf16 v[104:107], v[148:151], a[96:99], v[104:107]
s_nop 4
v_cndmask_b32 v172, v108, v116, s[40:41]
v_mfma_f32_16x16x32_bf16 v[112:115], v[148:151], a[104:107], v[112:115]
v_mfma_f32_16x16x32_bf16 v[120:123], v[148:151], a[112:115], v[120:123]
v_cndmask_b32 v174, v124, v132, s[40:41]
v_cndmask_b32 v172, v172, v174, s[42:43]
v_mfma_f32_16x16x32_bf16 v[128:131], v[148:151], a[120:123], v[128:131]
v_mov_b32 v169, v172
s_nop 2
v_cndmask_b32 v168, v104, v112, s[40:41]
s_nop 2
v_cndmask_b32 v170, v120, v128, s[40:41]
v_cndmask_b32 v168, v168, v170, s[42:43]
ds_write_b64 v176, v[168:169]
ds_write_b64 v177, v[180:181]
s_cmp_lg_u32 s14, 0
s_cbranch_scc1 Lmnp_11_%=
buffer_store_dwordx4 v[208:211], v5, s[24:27], s39 offen sc1
Lmnp_11_%=:
s_waitcnt lgkmcnt(0)
s_barrier
ds_read_b64 v[192:193], v179 offset:8192
v_mov_b32 v177, s12
ds_read_b64 v[194:195], v179 offset:8200
ds_read_b64 v[196:197], v179 offset:8208
ds_read_b64 v[198:199], v179 offset:8216
v_mov_b32 v179, s12
ds_read_b64 v[200:201], v178 offset:0
ds_read_b64 v[202:203], v178 offset:512
ds_read_b64 v[204:205], v178 offset:1024
ds_read_b64 v[206:207], v178 offset:1536
s_waitcnt lgkmcnt(6)
v_add_f32 v168, v192, v194
v_add_f32 v169, v193, v195
s_waitcnt lgkmcnt(4)
v_add_f32 v170, v196, v198
v_add_f32 v168, v168, v170
v_mul_f32 v168, 0x3e800000, v168
v_add_f32 v170, v197, v199
v_sub_f32 v192, v192, v168
v_add_f32 v169, v169, v170
v_sub_f32 v194, v194, v168
v_mul_f32 v170, v192, v192
v_sub_f32 v196, v196, v168
v_fma_f32 v170, v194, v194, v170
v_sub_f32 v198, v198, v168
v_fma_f32 v170, v196, v196, v170
v_fma_f32 v170, v198, v198, v170
v_mov_b32 v192, 0x43000000
v_fma_f32 v169, v170, v192, v169
v_mov_b32 v194, 0x3727c5ac
v_mov_b32 v196, 0x3b000000
v_fma_f32 v169, v169, v196, v194
v_rsq_f32 v169, v169
s_waitcnt lgkmcnt(2)
v_add_f32 v172, v200, v202
v_add_f32 v173, v201, v203
s_waitcnt lgkmcnt(0)
v_add_f32 v174, v204, v206
v_add_f32 v175, v205, v207
v_add_f32 v172, v172, v174
v_add_f32 v173, v173, v175
v_fma_f32 v172, -v168, v24, v172
v_fma_f32 v173, -v168, v25, v173
v_mul_f32 v172, v172, v169
v_mul_f32 v173, v173, v169
v_mul_f32 v174, 0xbfb8aa3b, v172
v_mul_f32 v175, 0xbfb8aa3b, v173
v_exp_f32 v174, v174
v_exp_f32 v175, v175
v_add_f32 v174, 1.0, v174
v_add_f32 v175, 1.0, v175
v_rcp_f32 v174, v174
v_rcp_f32 v175, v175
v_mul_f32 v172, v172, v174
v_mul_f32 v173, v173, v175
v_cvt_pk_bf16_f32 v172, v172, v173
ds_write_b32 v11, v172
ds_read_b128 v[136:139], v12 offset:0
ds_read_b128 v[140:143], v12 offset:64
ds_read_b128 v[144:147], v12 offset:128
ds_read_b128 v[148:151], v12 offset:192
s_waitcnt lgkmcnt(3)
v_mfma_f32_16x16x32_bf16 v[104:107], v[136:139], a[128:131], 0
v_mfma_f32_16x16x32_bf16 v[108:111], v[136:139], a[132:135], 0
v_mfma_f32_16x16x32_bf16 v[112:115], v[136:139], a[136:139], 0
v_mfma_f32_16x16x32_bf16 v[116:119], v[136:139], a[140:143], 0
v_mfma_f32_16x16x32_bf16 v[120:123], v[136:139], a[144:147], 0
v_mfma_f32_16x16x32_bf16 v[124:127], v[136:139], a[148:151], 0
v_mfma_f32_16x16x32_bf16 v[128:131], v[136:139], a[152:155], 0
v_mfma_f32_16x16x32_bf16 v[132:135], v[136:139], a[156:159], 0
s_waitcnt lgkmcnt(2)
v_mfma_f32_16x16x32_bf16 v[104:107], v[140:143], a[160:163], v[104:107]
v_mfma_f32_16x16x32_bf16 v[108:111], v[140:143], a[164:167], v[108:111]
v_mfma_f32_16x16x32_bf16 v[112:115], v[140:143], a[168:171], v[112:115]
v_mfma_f32_16x16x32_bf16 v[116:119], v[140:143], a[172:175], v[116:119]
v_mfma_f32_16x16x32_bf16 v[120:123], v[140:143], a[176:179], v[120:123]
v_mfma_f32_16x16x32_bf16 v[124:127], v[140:143], a[180:183], v[124:127]
v_mfma_f32_16x16x32_bf16 v[128:131], v[140:143], a[184:187], v[128:131]
v_mfma_f32_16x16x32_bf16 v[132:135], v[140:143], a[188:191], v[132:135]
s_waitcnt lgkmcnt(1)
v_mfma_f32_16x16x32_bf16 v[104:107], v[144:147], a[192:195], v[104:107]
v_mfma_f32_16x16x32_bf16 v[108:111], v[144:147], a[196:199], v[108:111]
v_mfma_f32_16x16x32_bf16 v[112:115], v[144:147], a[200:203], v[112:115]
v_mfma_f32_16x16x32_bf16 v[116:119], v[144:147], a[204:207], v[116:119]
v_mfma_f32_16x16x32_bf16 v[120:123], v[144:147], a[208:211], v[120:123]
v_mfma_f32_16x16x32_bf16 v[124:127], v[144:147], a[212:215], v[124:127]
v_mfma_f32_16x16x32_bf16 v[128:131], v[144:147], a[216:219], v[128:131]
v_mfma_f32_16x16x32_bf16 v[132:135], v[144:147], a[220:223], v[132:135]
s_waitcnt lgkmcnt(0)
v_mfma_f32_16x16x32_bf16 v[104:107], v[148:151], a[224:227], v[104:107]
v_mfma_f32_16x16x32_bf16 v[108:111], v[148:151], a[228:231], v[108:111]
v_mfma_f32_16x16x32_bf16 v[112:115], v[148:151], a[232:235], v[112:115]
v_mfma_f32_16x16x32_bf16 v[116:119], v[148:151], a[236:239], v[116:119]
v_mfma_f32_16x16x32_bf16 v[120:123], v[148:151], a[240:243], v[120:123]
v_mfma_f32_16x16x32_bf16 v[124:127], v[148:151], a[244:247], v[124:127]
s_nop 4
v_cndmask_b32 v168, v104, v112, s[40:41]
v_mfma_f32_16x16x32_bf16 v[128:131], v[148:151], a[248:251], v[128:131]
v_cndmask_b32 v172, v108, v116, s[40:41]
v_mfma_f32_16x16x32_bf16 v[132:135], v[148:151], a[252:255], v[132:135]
s_nop 5
v_cndmask_b32 v170, v120, v128, s[40:41]
v_cndmask_b32 v168, v168, v170, s[42:43]
v_cndmask_b32 v174, v124, v132, s[40:41]
v_cndmask_b32 v172, v172, v174, s[42:43]
v_cvt_pk_bf16_f32 v176, v168, v172
v_mov_b32 v178, v176
s_nop 1
v_permlane16_swap_b32 v176, v178
s_nop 1
s_mov_b32 exec_lo, 0xffff
s_mov_b32 exec_hi, 0xffff
buffer_store_dwordx4 v[176:179], v13, s[24:27], s38 offen sc1
s_mov_b64 exec, -1
s_cmp_lg_u32 s54, 0
s_cbranch_scc1 Lmrot_12_%=
v_mov_b32 v16, v17
v_mov_b32 v17, v18
v_mov_b32 v18, v19
v_mov_b32 v19, v20
v_mov_b32 v20, v21
v_mov_b32 v21, v22
v_mov_b32 v22, v23
Lmrot_12_%=:
s_add_u32 s30, s30, 1
s_cmp_lt_u32 s30, 16
s_nop 1
s_cbranch_scc1 Lmloop_1_%=
s_waitcnt vmcnt(0)
s_endpgm
Lrcem_%=:
Lmab_7_%=:
v_mov_b32 v252, 256
v_mov_b32 v253, 1
buffer_store_dword v253, v252, s[24:27], 0 offen sc1
s_nop 4
s_branch LmA_4_%=
)ASM" :
      CHAIN_IN((u32x4{(unsigned)lane, vSt, vW1, vW2}), (u32x4{vG, vX, vXBw, vXBr}), (u32x4{vEXw, vEXr, vSTw, vHBw}), (u32x4{vHBr, vZ, vXin, 0u}), sX, sA, sB)
      : "v16", "v17", "v18", "v19", "v20", "v21", "v22", "v23", "v24", "v25", "v26", "v27", "v28", "v29", "v30", "v31", "v32", "v33", "v34", "v35", "v36", "v37", "v38", "v39", "v40", "v41", "v42", "v43", "v44", "v45", "v46", "v47", "v48", "v49", "v50", "v51", "v52", "v53", "v54", "v55", "v56", "v57", "v58", "v59", "v60", "v61", "v62", "v63", "v64", "v65", "v66", "v67", "v68", "v69", "v70", "v71", "v72", "v73", "v74", "v75", "v76", "v77", "v78", "v79", "v80", "v81", "v82", "v83", "v84", "v85", "v86", "v87", "v88", "v89", "v90", "v91", "v92", "v93", "v94", "v95", "v96", "v97", "v98", "v99", "v100", "v101", "v102", "v103", "v104", "v105", "v106", "v107", "v108", "v109", "v110", "v111", "v112", "v113", "v114", "v115", "v116", "v117", "v118", "v119", "v120", "v121", "v122", "v123", "v124", "v125", "v126", "v127", "v128", "v129", "v130", "v131", "v132", "v133", "v134", "v135", "v136", "v137", "v138", "v139", "v140", "v141", "v142", "v143", "v144", "v145", "v146", "v147", "v148", "v149", "v150", "v151", "v152", "v153", "v154", "v155", "v156", "v157", "v158", "v159", "v160", "v161", "v162", "v163", "v164", "v165", "v166", "v167", "v168", "v169", "v170", "v171", "v172", "v173", "v174", "v175", "v176", "v177", "v178", "v179", "v180", "v181", "v182", "v183", "v184", "v185", "v186", "v187", "v188", "v189", "v190", "v191", "v192", "v193", "v194", "v195", "v196", "v197", "v198", "v199", "v200", "v201", "v202", "v203", "v204", "v205", "v206", "v207", "v208", "v209", "v210", "v211", "v212", "v213", "v214", "v215", "v216", "v217", "v218", "v219", "v220", "v221", "v222", "v223", "v224", "v225", "v226", "v227", "v228", "v229", "v230", "v231", "v232", "v233", "v234", "v235", "v236", "v237", "v238", "v239", "v240", "v241", "v242", "v243", "v244", "v245", "v246", "v247", "v248", "v249", "v250", "v251", "v252", "v253", "v254", "v255", "a0", "a1", "a2", "a3", "a4", "a5", "a6", "a7", "a8", "a9", "a10", "a11", "a12", "a13", "a14", "a15", "a16", "a17", "a18", "a19", "a20", "a21", "a22", "a23", "a24", "a25", "a26", "a27", "a28", "a29", "a30", "a31", "a32", "a33", "a34", "a35", "a36", "a37", "a38", "a39", "a40", "a41", "a42", "a43", "a44", "a45", "a46", "a47", "a48", "a49", "a50", "a51", "a52", "a53", "a54", "a55", "a56", "a57", "a58", "a59", "a60", "a61", "a62", "a63", "a64", "a65", "a66", "a67", "a68", "a69", "a70", "a71", "a72", "a73", "a74", "a75", "a76", "a77", "a78", "a79", "a80", "a81", "a82", "a83", "a84", "a85", "a86", "a87", "a88", "a89", "a90", "a91", "a92", "a93", "a94", "a95", "a96", "a97", "a98", "a99", "a100", "a101", "a102", "a103", "a104", "a105", "a106", "a107", "a108", "a109", "a110", "a111", "a112", "a113", "a114", "a115", "a116", "a117", "a118", "a119", "a120", "a121", "a122", "a123", "a124", "a125", "a126", "a127", "a128", "a129", "a130", "a131", "a132", "a133", "a134", "a135", "a136", "a137", "a138", "a139", "a140", "a141", "a142", "a143", "a144", "a145", "a146", "a147", "a148", "a149", "a150", "a151", "a152", "a153", "a154", "a155", "a156", "a157", "a158", "a159", "a160", "a161", "a162", "a163", "a164", "a165", "a166", "a167", "a168", "a169", "a170", "a171", "a172", "a173", "a174", "a175", "a176", "a177", "a178", "a179", "a180", "a181", "a182", "a183", "a184", "a185", "a186", "a187", "a188", "a189", "a190", "a191", "a192", "a193", "a194", "a195", "a196", "a197", "a198", "a199", "a200", "a201", "a202", "a203", "a204", "a205", "a206", "a207", "a208", "a209", "a210", "a211", "a212", "a213", "a214", "a215", "a216", "a217", "a218", "a219", "a220", "a221", "a222", "a223", "a224", "a225", "a226", "a227", "a228", "a229", "a230", "a231", "a232", "a233", "a234", "a235", "a236", "a237", "a238", "a239", "a240", "a241", "a242", "a243", "a244", "a245", "a246", "a247", "a248", "a249", "a250", "a251", "a252", "a253", "a254", "a255", "s28", "s29", "s30", "s31", "s33", "s34", "s35", "s36", "s37", "s38", "s39", "s40", "s41", "s42", "s43", "s44", "s45", "s46", "s47", "s50", "s51", "s52", "s53", "s54", "s55", "s56", "s57", "s58", "s59", "s60", "s61", "s62", "s63", "vcc", "memory");
  __builtin_unreachable();
}
__device__ __forceinline__ void role_q(const Params& p, int idx, unsigned tag, int w) {
  CHAIN_COMMON
  const unsigned vWq = (128 * w + 8 * g) * 8192 + c * 16;
  const unsigned n0 = 128 * w + 4 * q32 + 2 * ph;
  const unsigned vG = ph * 7 * 2048 + w * 512 + q32 * 16, vX = n0 * 8;
  const unsigned vXBw = w * 256 + (4 * q32 + 2 * ph) * 2, vXBr = w * 256 + g * 16;
  const unsigned vEXw = 1024 + w * 1024 + (8 * c + 2 * g) * 4, vEXr = 1024 + (128 * ph + 4 * q32) * 4, vSTw = 6144 + w * 8;
  const unsigned vQp = (idx >> 1) * 2048 + qrec_off(2 * (idx & 1) + ph, q32);
  const unsigned vXa = (c & 7) * 1024 + (128 * w + 8 * g) * 2, vXs = 2 * w * 1024 + lane * 16;
  const unsigned vQ1r = ph * 8192 + (q32 & 1) * 4096 + (q32 >> 1) * 16, vQ1w = w * 16384 + lane * 16;
  const u32x4 sX = mk_srd(p.x, 0, NB * DM * 2);
  const u32x4 sA = mk_srd(p.wq, (unsigned long long)idx * 512, DM * NH * DH * 2 - idx * 512);
  asm volatile(R"ASM(
s_nop 4
s_mov_b32 s34, 0x5040100
s_mov_b32 s35, 0x7060302
s_mov_b32 s44, 0
s_mov_b32 s45, -1
s_mov_b32 s28, 0x5a493201
s_mov_b32 s29, 0xf8ed7c6b
s_mov_b32 s30, 0
v_mov_b32 v253, 0
buffer_load_dword v252, v253, s[24:27], 0 offen sc1
s_getpc_b64 s[56:57]
Lrc0q_%=:
s_and_b32 s57, s57, 0xffff
s_mov_b32 s58, Lrceq_%=-Lrc0q_%=
s_mov_b32 s59, 0x20000
v_lshlrev_b32 v251, 7, v0
s_lshl_b32 s46, s13, 13
v_add_u32 v251, s46, v251
buffer_load_dword v250, v251, s[56:59], 0 offen
v_and_b32 v16, 16, v0
v_cmp_ne_u32 s[40:41], 0, v16
v_and_b32 v16, 32, v0
v_cmp_ne_u32 s[42:43], 0, v16
v_and_b32 v16, 8, v0
v_cmp_ne_u32 s[56:57], 0, v16
buffer_load_dwordx4 v[168:171], v12, s[8:11], 0 offen 
buffer_load_dwordx4 v[172:175], v12, s[8:11], 0 offen offset:64 
buffer_load_dwordx4 v[176:179], v12, s[8:11], 0 offen offset:128 
buffer_load_dwordx4 v[180:183], v12, s[8:11], 0 offen offset:192 
buffer_load_dwordx4 v[184:187], v13, s[8:11], 0 offen 
buffer_load_dwordx4 v[188:191], v13, s[8:11], 0 offen offset:1024 
s_mov_b32 s46, 0
buffer_load_dwordx4 a[0:3], v2, s[16:19], s46 offen 
s_mov_b32 s46, 0x2000
buffer_load_dwordx4 a[4:7], v2, s[16:19], s46 offen 
s_mov_b32 s46, 0x4000
buffer_load_dwordx4 a[8:11], v2, s[16:19], s46 offen 
s_mov_b32 s46, 0x6000
buffer_load_dwordx4 a[12:15], v2, s[16:19], s46 offen 
s_mov_b32 s46, 0x8000
buffer_load_dwordx4 a[16:19], v2, s[16:19], s46 offen 
s_mov_b32 s46, 0xa000
buffer_load_dwordx4 a[20:23], v2, s[16:19], s46 offen 
s_mov_b32 s46, 0xc000
buffer_load_dwordx4 a[24:27], v2, s[16:19], s46 offen 
s_mov_b32 s46, 0xe000
buffer_load_dwordx4 a[28:31], v2, s[16:19], s46 offen 
s_mov_b32 s46, 0x100
buffer_load_dwordx4 a[32:35], v2, s[16:19], s46 offen 
s_mov_b32 s46, 0x2100
buffer_load_dwordx4 a[36:39], v2, s[16:19], s46 offen 
s_mov_b32 s46, 0x4100
buffer_load_dwordx4 a[40:43], v2, s[16:19], s46 offen 
s_mov_b32 s46, 0x6100
buffer_load_dwordx4 a[44:47], v2, s[16:19], s46 offen 
s_mov_b32 s46, 0x8100
buffer_load_dwordx4 a[48:51], v2, s[16:19], s46 offen 
s_mov_b32 s46, 0xa100
buffer_load_dwordx4 a[52:55], v2, s[16:19], s46 offen 
s_mov_b32 s46, 0xc100
buffer_load_dwordx4 a[56:59], v2, s[16:19], s46 offen 
s_mov_b32 s46, 0xe100
buffer_load_dwordx4 a[60:63], v2, s[16:19], s46 offen 
s_mov_b32 s46, 0x40000
buffer_load_dwordx4 a[64:67], v2, s[16:19], s46 offen 
s_mov_b32 s46, 0x42000
buffer_load_dwordx4 a[68:71], v2, s[16:19], s46 offen 
s_mov_b32 s46, 0x44000
buffer_load_dwordx4 a[72:75], v2, s[16:19], s46 offen 
s_mov_b32 s46, 0x46000
buffer_load_dwordx4 a[76:79], v2, s[16:19], s46 offen 
s_mov_b32 s46, 0x48000
buffer_load_dwordx4 a[80:83], v2, s[16:19], s46 offen 
s_mov_b32 s46, 0x4a000
buffer_load_dwordx4 a[84:87], v2, s[16:19], s46 offen 
s_mov_b32 s46, 0x4c000
buffer_load_dwordx4 a[88:91], v2, s[16:19], s46 offen 
s_mov_b32 s46, 0x4e000
buffer_load_dwordx4 a[92:95], v2, s[16:19], s46 offen 
s_mov_b32 s46, 0x40100
buffer_load_dwordx4 a[96:99], v2, s[16:19], s46 offen 
s_mov_b32 s46, 0x42100
buffer_load_dwordx4 a[100:103], v2, s[16:19], s46 offen 
s_mov_b32 s46, 0x44100
buffer_load_dwordx4 a[104:107], v2, s[16:19], s46 offen 
s_mov_b32 s46, 0x46100
buffer_load_dwordx4 a[108:111], v2, s[16:19], s46 offen 
s_mov_b32 s46, 0x48100
buffer_load_dwordx4 a[112:115], v2, s[16:19], s46 offen 
s_mov_b32 s46, 0x4a100
buffer_load_dwordx4 a[116:119], v2, s[16:19], s46 offen 
s_mov_b32 s46, 0x4c100
buffer_load_dwordx4 a[120:123], v2, s[16:19], s46 offen 
s_mov_b32 s46, 0x4e100
buffer_load_dwordx4 a[124:127], v2, s[16:19], s46 offen 
s_mov_b32 s46, 0x80000
buffer_load_dwordx4 a[128:131], v2, s[16:19], s46 offen 
s_mov_b32 s46, 0x82000
buffer_load_dwordx4 a[132:135], v2, s[16:19], s46 offen 
s_mov_b32 s46, 0x84000
buffer_load_dwordx4 a[136:139], v2, s[16:19], s46 offen 
s_mov_b32 s46, 0x86000
buffer_load_dwordx4 a[140:143], v2, s[16:19], s46 offen 
s_mov_b32 s46, 0x88000
buffer_load_dwordx4 a[144:147], v2, s[16:19], s46 offen 
s_mov_b32 s46, 0x8a000
buffer_load_dwordx4 a[148:151], v2, s[16:19], s46 offen 
s_mov_b32 s46, 0x8c000
buffer_load_dwordx4 a[152:155], v2, s[16:19], s46 offen 
s_mov_b32 s46, 0x8e000
buffer_load_dwordx4 a[156:159], v2, s[16:19], s46 offen 
s_mov_b32 s46, 0x80100
buffer_load_dwordx4 a[160:163], v2, s[16:19], s46 offen 
s_mov_b32 s46, 0x82100
buffer_load_dwordx4 a[164:167], v2, s[16:19], s46 offen 
s_mov_b32 s46, 0x84100
buffer_load_dwordx4 a[168:171], v2, s[16:19], s46 offen 
s_mov_b32 s46, 0x86100
buffer_load_dwordx4 a[172:175], v2, s[16:19], s46 offen 
s_mov_b32 s46, 0x88100
buffer_load_dwordx4 a[176:179], v2, s[16:19], s46 offen 
s_mov_b32 s46, 0x8a100
buffer_load_dwordx4 a[180:183], v2, s[16:19], s46 offen 
s_mov_b32 s46, 0x8c100
buffer_load_dwordx4 a[184:187], v2, s[16:19], s46 offen 
s_mov_b32 s46, 0x8e100
buffer_load_dwordx4 a[188:191], v2, s[16:19], s46 offen 
s_mov_b32 s46, 0xc0000
buffer_load_dwordx4 a[192:195], v2, s[16:19], s46 offen 
s_mov_b32 s46, 0xc2000
buffer_load_dwordx4 a[196:199], v2, s[16:19], s46 offen 
s_mov_b32 s46, 0xc4000
buffer_load_dwordx4 a[200:203], v2, s[16:19], s46 offen 
s_mov_b32 s46, 0xc6000
buffer_load_dwordx4 a[204:207], v2, s[16:19], s46 offen 
s_mov_b32 s46, 0xc8000
buffer_load_dwordx4 a[208:211], v2, s[16:19], s46 offen 
s_mov_b32 s46, 0xca000
buffer_load_dwordx4 a[212:215], v2, s[16:19], s46 offen 
s_mov_b32 s46, 0xcc000
buffer_load_dwordx4 a[216:219], v2, s[16:19], s46 offen 
s_mov_b32 s46, 0xce000
buffer_load_dwordx4 a[220:223], v2, s[16:19], s46 offen 
s_mov_b32 s46, 0xc0100
buffer_load_dwordx4 a[224:227], v2, s[16:19], s46 offen 
s_mov_b32 s46, 0xc2100
buffer_load_dwordx4 a[228:231], v2, s[16:19], s46 offen 
s_mov_b32 s46, 0xc4100
buffer_load_dwordx4 a[232:235], v2, s[16:19], s46 offen 
s_mov_b32 s46, 0xc6100
buffer_load_dwordx4 a[236:239], v2, s[16:19], s46 offen 
s_mov_b32 s46, 0xc8100
buffer_load_dwordx4 a[240:243], v2, s[16:19], s46 offen 
s_mov_b32 s46, 0xca100
buffer_load_dwordx4 a[244:247], v2, s[16:19], s46 offen 
s_mov_b32 s46, 0xcc100
buffer_load_dwordx4 a[248:251], v2, s[16:19], s46 offen 
s_mov_b32 s46, 0xce100
buffer_load_dwordx4 a[252:255], v2, s[16:19], s46 offen 
s_memrealtime s[58:59]
s_waitcnt lgkmcnt(0)
s_mov_b32 s60, s58
s_waitcnt vmcnt(62)
v_readfirstlane_b32 s12, v252
s_nop 3
s_add_u32 s12, s12, 1
s_waitcnt vmcnt(62)
v_lshlrev_b32 v200, 16, v184
v_and_b32 v201, 0xffff0000, v184
v_lshlrev_b32 v202, 16, v185
v_and_b32 v203, 0xffff0000, v185
v_lshlrev_b32 v204, 16, v186
v_and_b32 v205, 0xffff0000, v186
v_lshlrev_b32 v206, 16, v187
v_and_b32 v207, 0xffff0000, v187
v_add_f32 v208, v200, v201
v_add_f32 v208, v208, v202
v_add_f32 v208, v208, v203
v_add_f32 v208, v208, v204
v_add_f32 v208, v208, v205
v_add_f32 v208, v208, v206
v_add_f32 v208, v208, v207
s_nop 1
v_add_f32_dpp v208, v208, v208 quad_perm:[1,0,3,2] row_mask:0xf bank_mask:0xf bound_ctrl:1
s_nop 1
v_add_f32_dpp v208, v208, v208 quad_perm:[2,3,0,1] row_mask:0xf bank_mask:0xf bound_ctrl:1
s_nop 1
v_add_f32_dpp v208, v208, v208 row_half_mirror row_mask:0xf bank_mask:0xf bound_ctrl:1
s_nop 1
v_add_f32_dpp v208, v208, v208 row_ror:8 row_mask:0xf bank_mask:0xf bound_ctrl:1
v_mov_b32 v209, v208
s_nop 1
v_permlane16_swap_b32 v208, v209
v_add_f32 v208, v208, v209
v_mov_b32 v209, v208
s_nop 1
v_permlane32_swap_b32 v208, v209
v_add_f32 v208, v208, v209
v_mul_f32 v192, 0x3b000000, v208
v_sub_f32 v200, v200, v192
v_sub_f32 v201, v201, v192
v_sub_f32 v202, v202, v192
v_sub_f32 v203, v203, v192
v_sub_f32 v204, v204, v192
v_sub_f32 v205, v205, v192
v_sub_f32 v206, v206, v192
v_sub_f32 v207, v207, v192
v_mul_f32 v208, v200, v200
v_fma_f32 v208, v201, v201, v208
v_fma_f32 v208, v202, v202, v208
v_fma_f32 v208, v203, v203, v208
v_fma_f32 v208, v204, v204, v208
v_fma_f32 v208, v205, v205, v208
v_fma_f32 v208, v206, v206, v208
v_fma_f32 v208, v207, v207, v208
s_nop 1
v_add_f32_dpp v208, v208, v208 quad_perm:[1,0,3,2] row_mask:0xf bank_mask:0xf bound_ctrl:1
s_nop 1
v_add_f32_dpp v208, v208, v208 quad_perm:[2,3,0,1] row_mask:0xf bank_mask:0xf bound_ctrl:1
s_nop 1
v_add_f32_dpp v208, v208, v208 row_half_mirror row_mask:0xf bank_mask:0xf bound_ctrl:1
s_nop 1
v_add_f32_dpp v208, v208, v208 row_ror:8 row_mask:0xf bank_mask:0xf bound_ctrl:1
v_mov_b32 v209, v208
s_nop 1
v_permlane16_swap_b32 v208, v209
v_add_f32 v208, v208, v209
v_mov_b32 v209, v208
s_nop 1
v_permlane32_swap_b32 v208, v209
v_add_f32 v208, v208, v209
v_mov_b32 v209, 0x3727c5ac
v_mov_b32 v210, 0x3b000000
v_fma_f32 v208, v208, v210, v209
v_rsq_f32 v193, v208
v_lshlrev_b32 v200, 16, v188
v_and_b32 v201, 0xffff0000, v188
v_lshlrev_b32 v202, 16, v189
v_and_b32 v203, 0xffff0000, v189
v_lshlrev_b32 v204, 16, v190
v_and_b32 v205, 0xffff0000, v190
v_lshlrev_b32 v206, 16, v191
v_and_b32 v207, 0xffff0000, v191
v_add_f32 v208, v200, v201
v_add_f32 v208, v208, v202
v_add_f32 v208, v208, v203
v_add_f32 v208, v208, v204
v_add_f32 v208, v208, v205
v_add_f32 v208, v208, v206
v_add_f32 v208, v208, v207
s_nop 1
v_add_f32_dpp v208, v208, v208 quad_perm:[1,0,3,2] row_mask:0xf bank_mask:0xf bound_ctrl:1
s_nop 1
v_add_f32_dpp v208, v208, v208 quad_perm:[2,3,0,1] row_mask:0xf bank_mask:0xf bound_ctrl:1
s_nop 1
v_add_f32_dpp v208, v208, v208 row_half_mirror row_mask:0xf bank_mask:0xf bound_ctrl:1
s_nop 1
v_add_f32_dpp v208, v208, v208 row_ror:8 row_mask:0xf bank_mask:0xf bound_ctrl:1
v_mov_b32 v209, v208
s_nop 1
v_permlane16_swap_b32 v208, v209
v_add_f32 v208, v208, v209
v_mov_b32 v209, v208
s_nop 1
v_permlane32_swap_b32 v208, v209
v_add_f32 v208, v208, v209
v_mul_f32 v194, 0x3b000000, v208
v_sub_f32 v200, v200, v194
v_sub_f32 v201, v201, v194
v_sub_f32 v202, v202, v194
v_sub_f32 v203, v203, v194
v_sub_f32 v204, v204, v194
v_sub_f32 v205, v205, v194
v_sub_f32 v206, v206, v194
v_sub_f32 v207, v207, v194
v_mul_f32 v208, v200, v200
v_fma_f32 v208, v201, v201, v208
v_fma_f32 v208, v202, v202, v208
v_fma_f32 v208, v203, v203, v208
v_fma_f32 v208, v204, v204, v208
v_fma_f32 v208, v205, v205, v208
v_fma_f32 v208, v206, v206, v208
v_fma_f32 v208, v207, v207, v208
s_nop 1
v_add_f32_dpp v208, v208, v208 quad_perm:[1,0,3,2] row_mask:0xf bank_mask:0xf bound_ctrl:1
s_nop 1
v_add_f32_dpp v208, v208, v208 quad_perm:[2,3,0,1] row_mask:0xf bank_mask:0xf bound_ctrl:1
s_nop 1
v_add_f32_dpp v208, v208, v208 row_half_mirror row_mask:0xf bank_mask:0xf bound_ctrl:1
s_nop 1
v_add_f32_dpp v208, v208, v208 row_ror:8 row_mask:0xf bank_mask:0xf bound_ctrl:1
v_mov_b32 v209, v208
s_nop 1
v_permlane16_swap_b32 v208, v209
v_add_f32 v208, v208, v209
v_mov_b32 v209, v208
s_nop 1
v_permlane32_swap_b32 v208, v209
v_add_f32 v208, v208, v209
v_mov_b32 v209, 0x3727c5ac
v_mov_b32 v210, 0x3b000000
v_fma_f32 v208, v208, v210, v209
v_rsq_f32 v195, v208
v_mov_b32 v200, 0x3f803f80
v_cndmask_b32 v168, v168, v200, s[56:57]
v_cndmask_b32 v169, v169, v200, s[56:57]
v_cndmask_b32 v170, v170, v200, s[56:57]
v_cndmask_b32 v171, v171, v200, s[56:57]
v_cndmask_b32 v172, v172, v200, s[56:57]
v_cndmask_b32 v173, v173, v200, s[56:57]
v_cndmask_b32 v174, v174, v200, s[56:57]
v_cndmask_b32 v175, v175, v200, s[56:57]
v_cndmask_b32 v176, v176, v200, s[56:57]
v_cndmask_b32 v177, v177, v200, s[56:57]
v_cndmask_b32 v178, v178, v200, s[56:57]
v_cndmask_b32 v179, v179, v200, s[56:57]
v_cndmask_b32 v180, v180, v200, s[56:57]
v_cndmask_b32 v181, v181, v200, s[56:57]
v_cndmask_b32 v182, v182, v200, s[56:57]
v_cndmask_b32 v183, v183, v200, s[56:57]
s_waitcnt vmcnt(56)
v_accvgpr_read_b32 v32, a0
v_accvgpr_read_b32 v33, a1
v_accvgpr_read_b32 v34, a2
v_accvgpr_read_b32 v35, a3
v_accvgpr_read_b32 v36, a4
v_accvgpr_read_b32 v37, a5
v_accvgpr_read_b32 v38, a6
v_accvgpr_read_b32 v39, a7
v_accvgpr_read_b32 v40, a8
v_accvgpr_read_b32 v41, a9
v_accvgpr_read_b32 v42, a10
v_accvgpr_read_b32 v43, a11
v_accvgpr_read_b32 v44, a12
v_accvgpr_read_b32 v45, a13
v_accvgpr_read_b32 v46, a14
v_accvgpr_read_b32 v47, a15
v_accvgpr_read_b32 v48, a16
v_accvgpr_read_b32 v49, a17
v_accvgpr_read_b32 v50, a18
v_accvgpr_read_b32 v51, a19
v_accvgpr_read_b32 v52, a20
v_accvgpr_read_b32 v53, a21
v_accvgpr_read_b32 v54, a22
v_accvgpr_read_b32 v55, a23
v_accvgpr_read_b32 v56, a24
v_accvgpr_read_b32 v57, a25
v_accvgpr_read_b32 v58, a26
v_accvgpr_read_b32 v59, a27
v_accvgpr_read_b32 v60, a28
v_accvgpr_read_b32 v61, a29
v_accvgpr_read_b32 v62, a30
v_accvgpr_read_b32 v63, a31
v_perm_b32 v64, v36, v32, s34
v_perm_b32 v65, v44, v40, s34
v_perm_b32 v66, v52, v48, s34
v_perm_b32 v67, v60, v56, s34
v_accvgpr_write_b32 a0, v64
v_accvgpr_write_b32 a1, v65
v_accvgpr_write_b32 a2, v66
v_accvgpr_write_b32 a3, v67
v_perm_b32 v64, v36, v32, s35
v_perm_b32 v65, v44, v40, s35
v_perm_b32 v66, v52, v48, s35
v_perm_b32 v67, v60, v56, s35
v_accvgpr_write_b32 a4, v64
v_accvgpr_write_b32 a5, v65
v_accvgpr_write_b32 a6, v66
v_accvgpr_write_b32 a7, v67
v_perm_b32 v64, v37, v33, s34
v_perm_b32 v65, v45, v41, s34
v_perm_b32 v66, v53, v49, s34
v_perm_b32 v67, v61, v57, s34
v_accvgpr_write_b32 a8, v64
v_accvgpr_write_b32 a9, v65
v_accvgpr_write_b32 a10, v66
v_accvgpr_write_b32 a11, v67
v_perm_b32 v64, v37, v33, s35
v_perm_b32 v65, v45, v41, s35
v_perm_b32 v66, v53, v49, s35
v_perm_b32 v67, v61, v57, s35
v_accvgpr_write_b32 a12, v64
v_accvgpr_write_b32 a13, v65
v_accvgpr_write_b32 a14, v66
v_accvgpr_write_b32 a15, v67
v_perm_b32 v64, v38, v34, s34
v_perm_b32 v65, v46, v42, s34
v_perm_b32 v66, v54, v50, s34
v_perm_b32 v67, v62, v58, s34
v_accvgpr_write_b32 a16, v64
v_accvgpr_write_b32 a17, v65
v_accvgpr_write_b32 a18, v66
v_accvgpr_write_b32 a19, v67
v_perm_b32 v64, v38, v34, s35
v_perm_b32 v65, v46, v42, s35
v_perm_b32 v66, v54, v50, s35
v_perm_b32 v67, v62, v58, s35
v_accvgpr_write_b32 a20, v64
v_accvgpr_write_b32 a21, v65
v_accvgpr_write_b32 a22, v66
v_accvgpr_write_b32 a23, v67
v_perm_b32 v64, v39, v35, s34
v_perm_b32 v65, v47, v43, s34
v_perm_b32 v66, v55, v51, s34
v_perm_b32 v67, v63, v59, s34
v_accvgpr_write_b32 a24, v64
v_accvgpr_write_b32 a25, v65
v_accvgpr_write_b32 a26, v66
v_accvgpr_write_b32 a27, v67
v_perm_b32 v64, v39, v35, s35
v_perm_b32 v65, v47, v43, s35
v_perm_b32 v66, v55, v51, s35
v_perm_b32 v67, v63, v59, s35
v_accvgpr_write_b32 a28, v64
v_accvgpr_write_b32 a29, v65
v_accvgpr_write_b32 a30, v66
v_accvgpr_write_b32 a31, v67
v_mfma_f32_16x16x32_bf16 v[96:99], v[168:171], a[0:3], 0
v_mfma_f32_16x16x32_bf16 v[100:103], v[168:171], a[4:7], 0
v_mfma_f32_16x16x32_bf16 v[104:107], v[168:171], a[8:11], 0
v_mfma_f32_16x16x32_bf16 v[108:111], v[168:171], a[12:15], 0
v_mfma_f32_16x16x32_bf16 v[112:115], v[168:171], a[16:19], 0
v_mfma_f32_16x16x32_bf16 v[116:119], v[168:171], a[20:23], 0
v_mfma_f32_16x16x32_bf16 v[120:123], v[168:171], a[24:27], 0
v_mfma_f32_16x16x32_bf16 v[124:127], v[168:171], a[28:31], 0
s_waitcnt vmcnt(48)
v_accvgpr_read_b32 v32, a32
v_accvgpr_read_b32 v33, a33
v_accvgpr_read_b32 v34, a34
v_accvgpr_read_b32 v35, a35
v_accvgpr_read_b32 v36, a36
v_accvgpr_read_b32 v37, a37
v_accvgpr_read_b32 v38, a38
v_accvgpr_read_b32 v39, a39
v_accvgpr_read_b32 v40, a40
v_accvgpr_read_b32 v41, a41
v_accvgpr_read_b32 v42, a42
v_accvgpr_read_b32 v43, a43
v_accvgpr_read_b32 v44, a44
v_accvgpr_read_b32 v45, a45
v_accvgpr_read_b32 v46, a46
v_accvgpr_read_b32 v47, a47
v_accvgpr_read_b32 v48, a48
v_accvgpr_read_b32 v49, a49
v_accvgpr_read_b32 v50, a50
v_accvgpr_read_b32 v51, a51
v_accvgpr_read_b32 v52, a52
v_accvgpr_read_b32 v53, a53
v_accvgpr_read_b32 v54, a54
v_accvgpr_read_b32 v55, a55
v_accvgpr_read_b32 v56, a56
v_accvgpr_read_b32 v57, a57
v_accvgpr_read_b32 v58, a58
v_accvgpr_read_b32 v59, a59
v_accvgpr_read_b32 v60, a60
v_accvgpr_read_b32 v61, a61
v_accvgpr_read_b32 v62, a62
v_accvgpr_read_b32 v63, a63
v_perm_b32 v64, v36, v32, s34
v_perm_b32 v65, v44, v40, s34
v_perm_b32 v66, v52, v48, s34
v_perm_b32 v67, v60, v56, s34
v_accvgpr_write_b32 a32, v64
v_accvgpr_write_b32 a33, v65
v_accvgpr_write_b32 a34, v66
v_accvgpr_write_b32 a35, v67
v_perm_b32 v64, v36, v32, s35
v_perm_b32 v65, v44, v40, s35
v_perm_b32 v66, v52, v48, s35
v_perm_b32 v67, v60, v56, s35
v_accvgpr_write_b32 a36, v64
v_accvgpr_write_b32 a37, v65
v_accvgpr_write_b32 a38, v66
v_accvgpr_write_b32 a39, v67
v_perm_b32 v64, v37, v33, s34
v_perm_b32 v65, v45, v41, s34
v_perm_b32 v66, v53, v49, s34
v_perm_b32 v67, v61, v57, s34
v_accvgpr_write_b32 a40, v64
v_accvgpr_write_b32 a41, v65
v_accvgpr_write_b32 a42, v66
v_accvgpr_write_b32 a43, v67
v_perm_b32 v64, v37, v33, s35
v_perm_b32 v65, v45, v41, s35
v_perm_b32 v66, v53, v49, s35
v_perm_b32 v67, v61, v57, s35
v_accvgpr_write_b32 a44, v64
v_accvgpr_write_b32 a45, v65
v_accvgpr_write_b32 a46, v66
v_accvgpr_write_b32 a47, v67
v_perm_b32 v64, v38, v34, s34
v_perm_b32 v65, v46, v42, s34
v_perm_b32 v66, v54, v50, s34
v_perm_b32 v67, v62, v58, s34
v_accvgpr_write_b32 a48, v64
v_accvgpr_write_b32 a49, v65
v_accvgpr_write_b32 a50, v66
v_accvgpr_write_b32 a51, v67
v_perm_b32 v64, v38, v34, s35
v_perm_b32 v65, v46, v42, s35
v_perm_b32 v66, v54, v50, s35
v_perm_b32 v67, v62, v58, s35
v_accvgpr_write_b32 a52, v64
v_accvgpr_write_b32 a53, v65
v_accvgpr_write_b32 a54, v66
v_accvgpr_write_b32 a55, v67
v_perm_b32 v64, v39, v35, s34
v_perm_b32 v65, v47, v43, s34
v_perm_b32 v66, v55, v51, s34
v_perm_b32 v67, v63, v59, s34
v_accvgpr_write_b32 a56, v64
v_accvgpr_write_b32 a57, v65
v_accvgpr_write_b32 a58, v66
v_accvgpr_write_b32 a59, v67
v_perm_b32 v64, v39, v35, s35
v_perm_b32 v65, v47, v43, s35
v_perm_b32 v66, v55, v51, s35
v_perm_b32 v67, v63, v59, s35
v_accvgpr_write_b32 a60, v64
v_accvgpr_write_b32 a61, v65
v_accvgpr_write_b32 a62, v66
v_accvgpr_write_b32 a63, v67
v_mfma_f32_16x16x32_bf16 v[128:131], v[168:171], a[32:35], 0
v_mfma_f32_16x16x32_bf16 v[132:135], v[168:171], a[36:39], 0
v_mfma_f32_16x16x32_bf16 v[136:139], v[168:171], a[40:43], 0
v_mfma_f32_16x16x32_bf16 v[140:143], v[168:171], a[44:47], 0
v_mfma_f32_16x16x32_bf16 v[144:147], v[168:171], a[48:51], 0
v_mfma_f32_16x16x32_bf16 v[148:151], v[168:171], a[52:55], 0
v_mfma_f32_16x16x32_bf16 v[152:155], v[168:171], a[56:59], 0
v_mfma_f32_16x16x32_bf16 v[156:159], v[168:171], a[60:63], 0
s_waitcnt vmcnt(40)
v_accvgpr_read_b32 v32, a64
v_accvgpr_read_b32 v33, a65
v_accvgpr_read_b32 v34, a66
v_accvgpr_read_b32 v35, a67
v_accvgpr_read_b32 v36, a68
v_accvgpr_read_b32 v37, a69
v_accvgpr_read_b32 v38, a70
v_accvgpr_read_b32 v39, a71
v_accvgpr_read_b32 v40, a72
v_accvgpr_read_b32 v41, a73
v_accvgpr_read_b32 v42, a74
v_accvgpr_read_b32 v43, a75
v_accvgpr_read_b32 v44, a76
v_accvgpr_read_b32 v45, a77
v_accvgpr_read_b32 v46, a78
v_accvgpr_read_b32 v47, a79
v_accvgpr_read_b32 v48, a80
v_accvgpr_read_b32 v49, a81
v_accvgpr_read_b32 v50, a82
v_accvgpr_read_b32 v51, a83
v_accvgpr_read_b32 v52, a84
v_accvgpr_read_b32 v53, a85
v_accvgpr_read_b32 v54, a86
v_accvgpr_read_b32 v55, a87
v_accvgpr_read_b32 v56, a88
v_accvgpr_read_b32 v57, a89
v_accvgpr_read_b32 v58, a90
v_accvgpr_read_b32 v59, a91
v_accvgpr_read_b32 v60, a92
v_accvgpr_read_b32 v61, a93
v_accvgpr_read_b32 v62, a94
v_accvgpr_read_b32 v63, a95
v_perm_b32 v64, v36, v32, s34
v_perm_b32 v65, v44, v40, s34
v_perm_b32 v66, v52, v48, s34
v_perm_b32 v67, v60, v56, s34
v_accvgpr_write_b32 a64, v64
v_accvgpr_write_b32 a65, v65
v_accvgpr_write_b32 a66, v66
v_accvgpr_write_b32 a67, v67
v_perm_b32 v64, v36, v32, s35
v_perm_b32 v65, v44, v40, s35
v_perm_b32 v66, v52, v48, s35
v_perm_b32 v67, v60, v56, s35
v_accvgpr_write_b32 a68, v64
v_accvgpr_write_b32 a69, v65
v_accvgpr_write_b32 a70, v66
v_accvgpr_write_b32 a71, v67
v_perm_b32 v64, v37, v33, s34
v_perm_b32 v65, v45, v41, s34
v_perm_b32 v66, v53, v49, s34
v_perm_b32 v67, v61, v57, s34
v_accvgpr_write_b32 a72, v64
v_accvgpr_write_b32 a73, v65
v_accvgpr_write_b32 a74, v66
v_accvgpr_write_b32 a75, v67
v_perm_b32 v64, v37, v33, s35
v_perm_b32 v65, v45, v41, s35
v_perm_b32 v66, v53, v49, s35
v_perm_b32 v67, v61, v57, s35
v_accvgpr_write_b32 a76, v64
v_accvgpr_write_b32 a77, v65
v_accvgpr_write_b32 a78, v66
v_accvgpr_write_b32 a79, v67
v_perm_b32 v64, v38, v34, s34
v_perm_b32 v65, v46, v42, s34
v_perm_b32 v66, v54, v50, s34
v_perm_b32 v67, v62, v58, s34
v_accvgpr_write_b32 a80, v64
v_accvgpr_write_b32 a81, v65
v_accvgpr_write_b32 a82, v66
v_accvgpr_write_b32 a83, v67
v_perm_b32 v64, v38, v34, s35
v_perm_b32 v65, v46, v42, s35
v_perm_b32 v66, v54, v50, s35
v_perm_b32 v67, v62, v58, s35
v_accvgpr_write_b32 a84, v64
v_accvgpr_write_b32 a85, v65
v_accvgpr_write_b32 a86, v66
v_accvgpr_write_b32 a87, v67
v_perm_b32 v64, v39, v35, s34
v_perm_b32 v65, v47, v43, s34
v_perm_b32 v66, v55, v51, s34
v_perm_b32 v67, v63, v59, s34
v_accvgpr_write_b32 a88, v64
v_accvgpr_write_b32 a89, v65
v_accvgpr_write_b32 a90, v66
v_accvgpr_write_b32 a91, v67
v_perm_b32 v64, v39, v35, s35
v_perm_b32 v65, v47, v43, s35
v_perm_b32 v66, v55, v51, s35
v_perm_b32 v67, v63, v59, s35
v_accvgpr_write_b32 a92, v64
v_accvgpr_write_b32 a93, v65
v_accvgpr_write_b32 a94, v66
v_accvgpr_write_b32 a95, v67
v_mfma_f32_16x16x32_bf16 v[96:99], v[172:175], a[64:67], v[96:99]
v_mfma_f32_16x16x32_bf16 v[100:103], v[172:175], a[68:71], v[100:103]
v_mfma_f32_16x16x32_bf16 v[104:107], v[172:175], a[72:75], v[104:107]
v_mfma_f32_16x16x32_bf16 v[108:111], v[172:175], a[76:79], v[108:111]
v_mfma_f32_16x16x32_bf16 v[112:115], v[172:175], a[80:83], v[112:115]
v_mfma_f32_16x16x32_bf16 v[116:119], v[172:175], a[84:87], v[116:119]
v_mfma_f32_16x16x32_bf16 v[120:123], v[172:175], a[88:91], v[120:123]
v_mfma_f32_16x16x32_bf16 v[124:127], v[172:175], a[92:95], v[124:127]
s_waitcnt vmcnt(32)
v_accvgpr_read_b32 v32, a96
v_accvgpr_read_b32 v33, a97
v_accvgpr_read_b32 v34, a98
v_accvgpr_read_b32 v35, a99
v_accvgpr_read_b32 v36, a100
v_accvgpr_read_b32 v37, a101
v_accvgpr_read_b32 v38, a102
v_accvgpr_read_b32 v39, a103
v_accvgpr_read_b32 v40, a104
v_accvgpr_read_b32 v41, a105
v_accvgpr_read_b32 v42, a106
v_accvgpr_read_b32 v43, a107
v_accvgpr_read_b32 v44, a108
v_accvgpr_read_b32 v45, a109
v_accvgpr_read_b32 v46, a110
v_accvgpr_read_b32 v47, a111
v_accvgpr_read_b32 v48, a112
v_accvgpr_read_b32 v49, a113
v_accvgpr_read_b32 v50, a114
v_accvgpr_read_b32 v51, a115
v_accvgpr_read_b32 v52, a116
v_accvgpr_read_b32 v53, a117
v_accvgpr_read_b32 v54, a118
v_accvgpr_read_b32 v55, a119
v_accvgpr_read_b32 v56, a120
v_accvgpr_read_b32 v57, a121
v_accvgpr_read_b32 v58, a122
v_accvgpr_read_b32 v59, a123
v_accvgpr_read_b32 v60, a124
v_accvgpr_read_b32 v61, a125
v_accvgpr_read_b32 v62, a126
v_accvgpr_read_b32 v63, a127
v_perm_b32 v64, v36, v32, s34
v_perm_b32 v65, v44, v40, s34
v_perm_b32 v66, v52, v48, s34
v_perm_b32 v67, v60, v56, s34
v_accvgpr_write_b32 a96, v64
v_accvgpr_write_b32 a97, v65
v_accvgpr_write_b32 a98, v66
v_accvgpr_write_b32 a99, v67
v_perm_b32 v64, v36, v32, s35
v_perm_b32 v65, v44, v40, s35
v_perm_b32 v66, v52, v48, s35
v_perm_b32 v67, v60, v56, s35
v_accvgpr_write_b32 a100, v64
v_accvgpr_write_b32 a101, v65
v_accvgpr_write_b32 a102, v66
v_accvgpr_write_b32 a103, v67
v_perm_b32 v64, v37, v33, s34
v_perm_b32 v65, v45, v41, s34
v_perm_b32 v66, v53, v49, s34
v_perm_b32 v67, v61, v57, s34
v_accvgpr_write_b32 a104, v64
v_accvgpr_write_b32 a105, v65
v_accvgpr_write_b32 a106, v66
v_accvgpr_write_b32 a107, v67
v_perm_b32 v64, v37, v33, s35
v_perm_b32 v65, v45, v41, s35
v_perm_b32 v66, v53, v49, s35
v_perm_b32 v67, v61, v57, s35
v_accvgpr_write_b32 a108, v64
v_accvgpr_write_b32 a109, v65
v_accvgpr_write_b32 a110, v66
v_accvgpr_write_b32 a111, v67
v_perm_b32 v64, v38, v34, s34
v_perm_b32 v65, v46, v42, s34
v_perm_b32 v66, v54, v50, s34
v_perm_b32 v67, v62, v58, s34
v_accvgpr_write_b32 a112, v64
v_accvgpr_write_b32 a113, v65
v_accvgpr_write_b32 a114, v66
v_accvgpr_write_b32 a115, v67
v_perm_b32 v64, v38, v34, s35
v_perm_b32 v65, v46, v42, s35
v_perm_b32 v66, v54, v50, s35
v_perm_b32 v67, v62, v58, s35
v_accvgpr_write_b32 a116, v64
v_accvgpr_write_b32 a117, v65
v_accvgpr_write_b32 a118, v66
v_accvgpr_write_b32 a119, v67
v_perm_b32 v64, v39, v35, s34
v_perm_b32 v65, v47, v43, s34
v_perm_b32 v66, v55, v51, s34
v_perm_b32 v67, v63, v59, s34
v_accvgpr_write_b32 a120, v64
v_accvgpr_write_b32 a121, v65
v_accvgpr_write_b32 a122, v66
v_accvgpr_write_b32 a123, v67
v_perm_b32 v64, v39, v35, s35
v_perm_b32 v65, v47, v43, s35
v_perm_b32 v66, v55, v51, s35
v_perm_b32 v67, v63, v59, s35
v_accvgpr_write_b32 a124, v64
v_accvgpr_write_b32 a125, v65
v_accvgpr_write_b32 a126, v66
v_accvgpr_write_b32 a127, v67
v_mfma_f32_16x16x32_bf16 v[128:131], v[172:175], a[96:99], v[128:131]
v_mfma_f32_16x16x32_bf16 v[132:135], v[172:175], a[100:103], v[132:135]
v_mfma_f32_16x16x32_bf16 v[136:139], v[172:175], a[104:107], v[136:139]
v_mfma_f32_16x16x32_bf16 v[140:143], v[172:175], a[108:111], v[140:143]
v_mfma_f32_16x16x32_bf16 v[144:147], v[172:175], a[112:115], v[144:147]
v_mfma_f32_16x16x32_bf16 v[148:151], v[172:175], a[116:119], v[148:151]
v_mfma_f32_16x16x32_bf16 v[152:155], v[172:175], a[120:123], v[152:155]
v_mfma_f32_16x16x32_bf16 v[156:159], v[172:175], a[124:127], v[156:159]
s_waitcnt vmcnt(24)
v_accvgpr_read_b32 v32, a128
v_accvgpr_read_b32 v33, a129
v_accvgpr_read_b32 v34, a130
v_accvgpr_read_b32 v35, a131
v_accvgpr_read_b32 v36, a132
v_accvgpr_read_b32 v37, a133
v_accvgpr_read_b32 v38, a134
v_accvgpr_read_b32 v39, a135
v_accvgpr_read_b32 v40, a136
v_accvgpr_read_b32 v41, a137
v_accvgpr_read_b32 v42, a138
v_accvgpr_read_b32 v43, a139
v_accvgpr_read_b32 v44, a140
v_accvgpr_read_b32 v45, a141
v_accvgpr_read_b32 v46, a142
v_accvgpr_read_b32 v47, a143
v_accvgpr_read_b32 v48, a144
v_accvgpr_read_b32 v49, a145
v_accvgpr_read_b32 v50, a146
v_accvgpr_read_b32 v51, a147
v_accvgpr_read_b32 v52, a148
v_accvgpr_read_b32 v53, a149
v_accvgpr_read_b32 v54, a150
v_accvgpr_read_b32 v55, a151
v_accvgpr_read_b32 v56, a152
v_accvgpr_read_b32 v57, a153
v_accvgpr_read_b32 v58, a154
v_accvgpr_read_b32 v59, a155
v_accvgpr_read_b32 v60, a156
v_accvgpr_read_b32 v61, a157
v_accvgpr_read_b32 v62, a158
v_accvgpr_read_b32 v63, a159
v_perm_b32 v64, v36, v32, s34
v_perm_b32 v65, v44, v40, s34
v_perm_b32 v66, v52, v48, s34
v_perm_b32 v67, v60, v56, s34
v_accvgpr_write_b32 a128, v64
v_accvgpr_write_b32 a129, v65
v_accvgpr_write_b32 a130, v66
v_accvgpr_write_b32 a131, v67
v_perm_b32 v64, v36, v32, s35
v_perm_b32 v65, v44, v40, s35
v_perm_b32 v66, v52, v48, s35
v_perm_b32 v67, v60, v56, s35
v_accvgpr_write_b32 a132, v64
v_accvgpr_write_b32 a133, v65
v_accvgpr_write_b32 a134, v66
v_accvgpr_write_b32 a135, v67
v_perm_b32 v64, v37, v33, s34
v_perm_b32 v65, v45, v41, s34
v_perm_b32 v66, v53, v49, s34
v_perm_b32 v67, v61, v57, s34
v_accvgpr_write_b32 a136, v64
v_accvgpr_write_b32 a137, v65
v_accvgpr_write_b32 a138, v66
v_accvgpr_write_b32 a139, v67
v_perm_b32 v64, v37, v33, s35
v_perm_b32 v65, v45, v41, s35
v_perm_b32 v66, v53, v49, s35
v_perm_b32 v67, v61, v57, s35
v_accvgpr_write_b32 a140, v64
v_accvgpr_write_b32 a141, v65
v_accvgpr_write_b32 a142, v66
v_accvgpr_write_b32 a143, v67
v_perm_b32 v64, v38, v34, s34
v_perm_b32 v65, v46, v42, s34
v_perm_b32 v66, v54, v50, s34
v_perm_b32 v67, v62, v58, s34
v_accvgpr_write_b32 a144, v64
v_accvgpr_write_b32 a145, v65
v_accvgpr_write_b32 a146, v66
v_accvgpr_write_b32 a147, v67
v_perm_b32 v64, v38, v34, s35
v_perm_b32 v65, v46, v42, s35
v_perm_b32 v66, v54, v50, s35
v_perm_b32 v67, v62, v58, s35
v_accvgpr_write_b32 a148, v64
v_accvgpr_write_b32 a149, v65
v_accvgpr_write_b32 a150, v66
v_accvgpr_write_b32 a151, v67
v_perm_b32 v64, v39, v35, s34
v_perm_b32 v65, v47, v43, s34
v_perm_b32 v66, v55, v51, s34
v_perm_b32 v67, v63, v59, s34
v_accvgpr_write_b32 a152, v64
v_accvgpr_write_b32 a153, v65
v_accvgpr_write_b32 a154, v66
v_accvgpr_write_b32 a155, v67
v_perm_b32 v64, v39, v35, s35
v_perm_b32 v65, v47, v43, s35
v_perm_b32 v66, v55, v51, s35
v_perm_b32 v67, v63, v59, s35
v_accvgpr_write_b32 a156, v64
v_accvgpr_write_b32 a157, v65
v_accvgpr_write_b32 a158, v66
v_accvgpr_write_b32 a159, v67
v_mfma_f32_16x16x32_bf16 v[96:99], v[176:179], a[128:131], v[96:99]
v_mfma_f32_16x16x32_bf16 v[100:103], v[176:179], a[132:135], v[100:103]
v_mfma_f32_16x16x32_bf16 v[104:107], v[176:179], a[136:139], v[104:107]
v_mfma_f32_16x16x32_bf16 v[108:111], v[176:179], a[140:143], v[108:111]
v_mfma_f32_16x16x32_bf16 v[112:115], v[176:179], a[144:147], v[112:115]
v_mfma_f32_16x16x32_bf16 v[116:119], v[176:179], a[148:151], v[116:119]
v_mfma_f32_16x16x32_bf16 v[120:123], v[176:179], a[152:155], v[120:123]
v_mfma_f32_16x16x32_bf16 v[124:127], v[176:179], a[156:159], v[124:127]
s_waitcnt vmcnt(16)
v_accvgpr_read_b32 v32, a160
v_accvgpr_read_b32 v33, a161
v_accvgpr_read_b32 v34, a162
v_accvgpr_read_b32 v35, a163
v_accvgpr_read_b32 v36, a164
v_accvgpr_read_b32 v37, a165
v_accvgpr_read_b32 v38, a166
v_accvgpr_read_b32 v39, a167
v_accvgpr_read_b32 v40, a168
v_accvgpr_read_b32 v41, a169
v_accvgpr_read_b32 v42, a170
v_accvgpr_read_b32 v43, a171
v_accvgpr_read_b32 v44, a172
v_accvgpr_read_b32 v45, a173
v_accvgpr_read_b32 v46, a174
v_accvgpr_read_b32 v47, a175
v_accvgpr_read_b32 v48, a176
v_accvgpr_read_b32 v49, a177
v_accvgpr_read_b32 v50, a178
v_accvgpr_read_b32 v51, a179
v_accvgpr_read_b32 v52, a180
v_accvgpr_read_b32 v53, a181
v_accvgpr_read_b32 v54, a182
v_accvgpr_read_b32 v55, a183
v_accvgpr_read_b32 v56, a184
v_accvgpr_read_b32 v57, a185
v_accvgpr_read_b32 v58, a186
v_accvgpr_read_b32 v59, a187
v_accvgpr_read_b32 v60, a188
v_accvgpr_read_b32 v61, a189
v_accvgpr_read_b32 v62, a190
v_accvgpr_read_b32 v63, a191
v_perm_b32 v64, v36, v32, s34
v_perm_b32 v65, v44, v40, s34
v_perm_b32 v66, v52, v48, s34
v_perm_b32 v67, v60, v56, s34
v_accvgpr_write_b32 a160, v64
v_accvgpr_write_b32 a161, v65
v_accvgpr_write_b32 a162, v66
v_accvgpr_write_b32 a163, v67
v_perm_b32 v64, v36, v32, s35
v_perm_b32 v65, v44, v40, s35
v_perm_b32 v66, v52, v48, s35
v_perm_b32 v67, v60, v56, s35
v_accvgpr_write_b32 a164, v64
v_accvgpr_write_b32 a165, v65
v_accvgpr_write_b32 a166, v66
v_accvgpr_write_b32 a167, v67
v_perm_b32 v64, v37, v33, s34
v_perm_b32 v65, v45, v41, s34
v_perm_b32 v66, v53, v49, s34
v_perm_b32 v67, v61, v57, s34
v_accvgpr_write_b32 a168, v64
v_accvgpr_write_b32 a169, v65
v_accvgpr_write_b32 a170, v66
v_accvgpr_write_b32 a171, v67
v_perm_b32 v64, v37, v33, s35
v_perm_b32 v65, v45, v41, s35
v_perm_b32 v66, v53, v49, s35
v_perm_b32 v67, v61, v57, s35
v_accvgpr_write_b32 a172, v64
v_accvgpr_write_b32 a173, v65
v_accvgpr_write_b32 a174, v66
v_accvgpr_write_b32 a175, v67
v_perm_b32 v64, v38, v34, s34
v_perm_b32 v65, v46, v42, s34
v_perm_b32 v66, v54, v50, s34
v_perm_b32 v67, v62, v58, s34
v_accvgpr_write_b32 a176, v64
v_accvgpr_write_b32 a177, v65
v_accvgpr_write_b32 a178, v66
v_accvgpr_write_b32 a179, v67
v_perm_b32 v64, v38, v34, s35
v_perm_b32 v65, v46, v42, s35
v_perm_b32 v66, v54, v50, s35
v_perm_b32 v67, v62, v58, s35
v_accvgpr_write_b32 a180, v64
v_accvgpr_write_b32 a181, v65
v_accvgpr_write_b32 a182, v66
v_accvgpr_write_b32 a183, v67
v_perm_b32 v64, v39, v35, s34
v_perm_b32 v65, v47, v43, s34
v_perm_b32 v66, v55, v51, s34
v_perm_b32 v67, v63, v59, s34
v_accvgpr_write_b32 a184, v64
v_accvgpr_write_b32 a185, v65
v_accvgpr_write_b32 a186, v66
v_accvgpr_write_b32 a187, v67
v_perm_b32 v64, v39, v35, s35
v_perm_b32 v65, v47, v43, s35
v_perm_b32 v66, v55, v51, s35
v_perm_b32 v67, v63, v59, s35
v_accvgpr_write_b32 a188, v64
v_accvgpr_write_b32 a189, v65
v_accvgpr_write_b32 a190, v66
v_accvgpr_write_b32 a191, v67
v_mfma_f32_16x16x32_bf16 v[128:131], v[176:179], a[160:163], v[128:131]
v_mfma_f32_16x16x32_bf16 v[132:135], v[176:179], a[164:167], v[132:135]
v_mfma_f32_16x16x32_bf16 v[136:139], v[176:179], a[168:171], v[136:139]
v_mfma_f32_16x16x32_bf16 v[140:143], v[176:179], a[172:175], v[140:143]
v_mfma_f32_16x16x32_bf16 v[144:147], v[176:179], a[176:179], v[144:147]
v_mfma_f32_16x16x32_bf16 v[148:151], v[176:179], a[180:183], v[148:151]
v_mfma_f32_16x16x32_bf16 v[152:155], v[176:179], a[184:187], v[152:155]
v_mfma_f32_16x16x32_bf16 v[156:159], v[176:179], a[188:191], v[156:159]
s_waitcnt vmcnt(8)
v_accvgpr_read_b32 v32, a192
v_accvgpr_read_b32 v33, a193
v_accvgpr_read_b32 v34, a194
v_accvgpr_read_b32 v35, a195
v_accvgpr_read_b32 v36, a196
v_accvgpr_read_b32 v37, a197
v_accvgpr_read_b32 v38, a198
v_accvgpr_read_b32 v39, a199
v_accvgpr_read_b32 v40, a200
v_accvgpr_read_b32 v41, a201
v_accvgpr_read_b32 v42, a202
v_accvgpr_read_b32 v43, a203
v_accvgpr_read_b32 v44, a204
v_accvgpr_read_b32 v45, a205
v_accvgpr_read_b32 v46, a206
v_accvgpr_read_b32 v47, a207
v_accvgpr_read_b32 v48, a208
v_accvgpr_read_b32 v49, a209
v_accvgpr_read_b32 v50, a210
v_accvgpr_read_b32 v51, a211
v_accvgpr_read_b32 v52, a212
v_accvgpr_read_b32 v53, a213
v_accvgpr_read_b32 v54, a214
v_accvgpr_read_b32 v55, a215
v_accvgpr_read_b32 v56, a216
v_accvgpr_read_b32 v57, a217
v_accvgpr_read_b32 v58, a218
v_accvgpr_read_b32 v59, a219
v_accvgpr_read_b32 v60, a220
v_accvgpr_read_b32 v61, a221
v_accvgpr_read_b32 v62, a222
v_accvgpr_read_b32 v63, a223
v_perm_b32 v64, v36, v32, s34
v_perm_b32 v65, v44, v40, s34
v_perm_b32 v66, v52, v48, s34
v_perm_b32 v67, v60, v56, s34
v_accvgpr_write_b32 a192, v64
v_accvgpr_write_b32 a193, v65
v_accvgpr_write_b32 a194, v66
v_accvgpr_write_b32 a195, v67
v_perm_b32 v64, v36, v32, s35
v_perm_b32 v65, v44, v40, s35
v_perm_b32 v66, v52, v48, s35
v_perm_b32 v67, v60, v56, s35
v_accvgpr_write_b32 a196, v64
v_accvgpr_write_b32 a197, v65
v_accvgpr_write_b32 a198, v66
v_accvgpr_write_b32 a199, v67
v_perm_b32 v64, v37, v33, s34
v_perm_b32 v65, v45, v41, s34
v_perm_b32 v66, v53, v49, s34
v_perm_b32 v67, v61, v57, s34
v_accvgpr_write_b32 a200, v64
v_accvgpr_write_b32 a201, v65
v_accvgpr_write_b32 a202, v66
v_accvgpr_write_b32 a203, v67
v_perm_b32 v64, v37, v33, s35
v_perm_b32 v65, v45, v41, s35
v_perm_b32 v66, v53, v49, s35
v_perm_b32 v67, v61, v57, s35
v_accvgpr_write_b32 a204, v64
v_accvgpr_write_b32 a205, v65
v_accvgpr_write_b32 a206, v66
v_accvgpr_write_b32 a207, v67
v_perm_b32 v64, v38, v34, s34
v_perm_b32 v65, v46, v42, s34
v_perm_b32 v66, v54, v50, s34
v_perm_b32 v67, v62, v58, s34
v_accvgpr_write_b32 a208, v64
v_accvgpr_write_b32 a209, v65
v_accvgpr_write_b32 a210, v66
v_accvgpr_write_b32 a211, v67
v_perm_b32 v64, v38, v34, s35
v_perm_b32 v65, v46, v42, s35
v_perm_b32 v66, v54, v50, s35
v_perm_b32 v67, v62, v58, s35
v_accvgpr_write_b32 a212, v64
v_accvgpr_write_b32 a213, v65
v_accvgpr_write_b32 a214, v66
v_accvgpr_write_b32 a215, v67
v_perm_b32 v64, v39, v35, s34
v_perm_b32 v65, v47, v43, s34
v_perm_b32 v66, v55, v51, s34
v_perm_b32 v67, v63, v59, s34
v_accvgpr_write_b32 a216, v64
v_accvgpr_write_b32 a217, v65
v_accvgpr_write_b32 a218, v66
v_accvgpr_write_b32 a219, v67
v_perm_b32 v64, v39, v35, s35
v_perm_b32 v65, v47, v43, s35
v_perm_b32 v66, v55, v51, s35
v_perm_b32 v67, v63, v59, s35
v_accvgpr_write_b32 a220, v64
v_accvgpr_write_b32 a221, v65
v_accvgpr_write_b32 a222, v66
v_accvgpr_write_b32 a223, v67
v_mfma_f32_16x16x32_bf16 v[96:99], v[180:183], a[192:195], v[96:99]
v_mfma_f32_16x16x32_bf16 v[100:103], v[180:183], a[196:199], v[100:103]
v_mfma_f32_16x16x32_bf16 v[104:107], v[180:183], a[200:203], v[104:107]
v_mfma_f32_16x16x32_bf16 v[108:111], v[180:183], a[204:207], v[108:111]
v_mfma_f32_16x16x32_bf16 v[112:115], v[180:183], a[208:211], v[112:115]
v_mfma_f32_16x16x32_bf16 v[116:119], v[180:183], a[212:215], v[116:119]
v_mfma_f32_16x16x32_bf16 v[120:123], v[180:183], a[216:219], v[120:123]
v_mfma_f32_16x16x32_bf16 v[124:127], v[180:183], a[220:223], v[124:127]
s_waitcnt vmcnt(0)
v_accvgpr_read_b32 v32, a224
v_accvgpr_read_b32 v33, a225
v_accvgpr_read_b32 v34, a226
v_accvgpr_read_b32 v35, a227
v_accvgpr_read_b32 v36, a228
v_accvgpr_read_b32 v37, a229
v_accvgpr_read_b32 v38, a230
v_accvgpr_read_b32 v39, a231
v_accvgpr_read_b32 v40, a232
v_accvgpr_read_b32 v41, a233
v_accvgpr_read_b32 v42, a234
v_accvgpr_read_b32 v43, a235
v_accvgpr_read_b32 v44, a236
v_accvgpr_read_b32 v45, a237
v_accvgpr_read_b32 v46, a238
v_accvgpr_read_b32 v47, a239
v_accvgpr_read_b32 v48, a240
v_accvgpr_read_b32 v49, a241
v_accvgpr_read_b32 v50, a242
v_accvgpr_read_b32 v51, a243
v_accvgpr_read_b32 v52, a244
v_accvgpr_read_b32 v53, a245
v_accvgpr_read_b32 v54, a246
v_accvgpr_read_b32 v55, a247
v_accvgpr_read_b32 v56, a248
v_accvgpr_read_b32 v57, a249
v_accvgpr_read_b32 v58, a250
v_accvgpr_read_b32 v59, a251
v_accvgpr_read_b32 v60, a252
v_accvgpr_read_b32 v61, a253
v_accvgpr_read_b32 v62, a254
v_accvgpr_read_b32 v63, a255
v_perm_b32 v64, v36, v32, s34
v_perm_b32 v65, v44, v40, s34
v_perm_b32 v66, v52, v48, s34
v_perm_b32 v67, v60, v56, s34
v_accvgpr_write_b32 a224, v64
v_accvgpr_write_b32 a225, v65
v_accvgpr_write_b32 a226, v66
v_accvgpr_write_b32 a227, v67
v_perm_b32 v64, v36, v32, s35
v_perm_b32 v65, v44, v40, s35
v_perm_b32 v66, v52, v48, s35
v_perm_b32 v67, v60, v56, s35
v_accvgpr_write_b32 a228, v64
v_accvgpr_write_b32 a229, v65
v_accvgpr_write_b32 a230, v66
v_accvgpr_write_b32 a231, v67
v_perm_b32 v64, v37, v33, s34
v_perm_b32 v65, v45, v41, s34
v_perm_b32 v66, v53, v49, s34
v_perm_b32 v67, v61, v57, s34
v_accvgpr_write_b32 a232, v64
v_accvgpr_write_b32 a233, v65
v_accvgpr_write_b32 a234, v66
v_accvgpr_write_b32 a235, v67
v_perm_b32 v64, v37, v33, s35
v_perm_b32 v65, v45, v41, s35
v_perm_b32 v66, v53, v49, s35
v_perm_b32 v67, v61, v57, s35
v_accvgpr_write_b32 a236, v64
v_accvgpr_write_b32 a237, v65
v_accvgpr_write_b32 a238, v66
v_accvgpr_write_b32 a239, v67
v_perm_b32 v64, v38, v34, s34
v_perm_b32 v65, v46, v42, s34
v_perm_b32 v66, v54, v50, s34
v_perm_b32 v67, v62, v58, s34
v_accvgpr_write_b32 a240, v64
v_accvgpr_write_b32 a241, v65
v_accvgpr_write_b32 a242, v66
v_accvgpr_write_b32 a243, v67
v_perm_b32 v64, v38, v34, s35
v_perm_b32 v65, v46, v42, s35
v_perm_b32 v66, v54, v50, s35
v_perm_b32 v67, v62, v58, s35
v_accvgpr_write_b32 a244, v64
v_accvgpr_write_b32 a245, v65
v_accvgpr_write_b32 a246, v66
v_accvgpr_write_b32 a247, v67
v_perm_b32 v64, v39, v35, s34
v_perm_b32 v65, v47, v43, s34
v_perm_b32 v66, v55, v51, s34
v_perm_b32 v67, v63, v59, s34
v_accvgpr_write_b32 a248, v64
v_accvgpr_write_b32 a249, v65
v_accvgpr_write_b32 a250, v66
v_accvgpr_write_b32 a251, v67
v_perm_b32 v64, v39, v35, s35
v_perm_b32 v65, v47, v43, s35
v_perm_b32 v66, v55, v51, s35
v_perm_b32 v67, v63, v59, s35
v_accvgpr_write_b32 a252, v64
v_accvgpr_write_b32 a253, v65
v_accvgpr_write_b32 a254, v66
v_accvgpr_write_b32 a255, v67
v_mfma_f32_16x16x32_bf16 v[128:131], v[180:183], a[224:227], v[128:131]
v_mfma_f32_16x16x32_bf16 v[132:135], v[180:183], a[228:231], v[132:135]
v_mfma_f32_16x16x32_bf16 v[136:139], v[180:183], a[232:235], v[136:139]
v_mfma_f32_16x16x32_bf16 v[140:143], v[180:183], a[236:239], v[140:143]
v_mfma_f32_16x16x32_bf16 v[144:147], v[180:183], a[240:243], v[144:147]
v_mfma_f32_16x16x32_bf16 v[148:151], v[180:183], a[244:247], v[148:151]
v_mfma_f32_16x16x32_bf16 v[152:155], v[180:183], a[248:251], v[152:155]
v_mfma_f32_16x16x32_bf16 v[156:159], v[180:183], a[252:255], v[156:159]
ds_write_b128 v15, v[96:99] offset:0
ds_write_b128 v15, v[100:103] offset:1024
ds_write_b128 v15, v[104:107] offset:2048
ds_write_b128 v15, v[108:111] offset:3072
ds_write_b128 v15, v[112:115] offset:4096
ds_write_b128 v15, v[116:119] offset:5120
ds_write_b128 v15, v[120:123] offset:6144
ds_write_b128 v15, v[124:127] offset:7168
ds_write_b128 v15, v[128:131] offset:8192
ds_write_b128 v15, v[132:135] offset:9216
ds_write_b128 v15, v[136:139] offset:10240
ds_write_b128 v15, v[140:143] offset:11264
ds_write_b128 v15, v[144:147] offset:12288
ds_write_b128 v15, v[148:151] offset:13312
ds_write_b128 v15, v[152:155] offset:14336
ds_write_b128 v15, v[156:159] offset:15360
s_waitcnt lgkmcnt(0)
s_barrier
v_mov_b32 v212, v14
ds_read_b32 v32, v212 offset:512
ds_read_b32 v33, v212 offset:1536
ds_read_b32 v34, v212 offset:2560
ds_read_b32 v35, v212 offset:3584
ds_read_b32 v36, v212 offset:16896
ds_read_b32 v37, v212 offset:17920
ds_read_b32 v38, v212 offset:18944
ds_read_b32 v39, v212 offset:19968
ds_read_b32 v40, v212 offset:33280
ds_read_b32 v41, v212 offset:34304
ds_read_b32 v42, v212 offset:35328
ds_read_b32 v43, v212 offset:36352
ds_read_b32 v44, v212 offset:49664
ds_read_b32 v45, v212 offset:50688
ds_read_b32 v46, v212 offset:51712
ds_read_b32 v47, v212 offset:52736
s_waitcnt lgkmcnt(0)
v_add_f32 v32, v32, v36
v_add_f32 v40, v40, v44
v_add_f32 v16, v32, v40
v_add_f32 v33, v33, v37
v_add_f32 v41, v41, v45
v_add_f32 v17, v33, v41
v_add_f32 v34, v34, v38
v_add_f32 v42, v42, v46
v_add_f32 v18, v34, v42
v_add_f32 v35, v35, v39
v_add_f32 v43, v43, v47
v_add_f32 v19, v35, v43
s_lshl_b32 s46, s13, 1
s_add_u32 s46, s46, 0
s_lshr_b32 s47, s46, 2
s_lshl_b32 s47, s47, 8
s_and_b32 s36, s46, 3
s_lshl_b32 s36, s36, 2
s_add_u32 s47, s47, s36
v_add_u32 v212, s47, v14
ds_read_b32 v32, v212 offset:0
ds_read_b32 v33, v212 offset:1024
ds_read_b32 v34, v212 offset:2048
ds_read_b32 v35, v212 offset:3072
ds_read_b32 v36, v212 offset:16384
ds_read_b32 v37, v212 offset:17408
ds_read_b32 v38, v212 offset:18432
ds_read_b32 v39, v212 offset:19456
ds_read_b32 v40, v212 offset:32768
ds_read_b32 v41, v212 offset:33792
ds_read_b32 v42, v212 offset:34816
ds_read_b32 v43, v212 offset:35840
ds_read_b32 v44, v212 offset:49152
ds_read_b32 v45, v212 offset:50176
ds_read_b32 v46, v212 offset:51200
ds_read_b32 v47, v212 offset:52224
s_waitcnt lgkmcnt(0)
v_add_f32 v32, v32, v36
v_add_f32 v40, v40, v44
v_add_f32 v200, v32, v40
v_add_f32 v33, v33, v37
v_add_f32 v41, v41, v45
v_add_f32 v201, v33, v41
v_add_f32 v34, v34, v38
v_add_f32 v42, v42, v46
v_add_f32 v202, v34, v42
v_add_f32 v35, v35, v39
v_add_f32 v43, v43, v47
v_add_f32 v203, v35, v43
v_fma_f32 v200, -v192, v16, v200
v_mul_f32 v200, v200, v193
v_fma_f32 v201, -v192, v17, v201
v_mul_f32 v201, v201, v193
v_fma_f32 v202, -v192, v18, v202
v_mul_f32 v202, v202, v193
v_fma_f32 v203, -v192, v19, v203
v_mul_f32 v203, v203, v193
v_cvt_pk_bf16_f32 v204, v200, v201
v_mov_b32 v205, s12
v_cvt_pk_bf16_f32 v206, v202, v203
v_mov_b32 v207, s12
s_lshl_b32 s36, s46, 14
s_add_u32 s36, s36, 0x1000
buffer_store_dwordx4 v[204:207], v11, s[24:27], s36 offen sc1
s_nop 1
s_lshl_b32 s46, s13, 1
s_add_u32 s46, s46, 1
s_lshr_b32 s47, s46, 2
s_lshl_b32 s47, s47, 8
s_and_b32 s36, s46, 3
s_lshl_b32 s36, s36, 2
s_add_u32 s47, s47, s36
v_add_u32 v212, s47, v14
ds_read_b32 v32, v212 offset:0
ds_read_b32 v33, v212 offset:1024
ds_read_b32 v34, v212 offset:2048
ds_read_b32 v35, v212 offset:3072
ds_read_b32 v36, v212 offset:16384
ds_read_b32 v37, v212 offset:17408
ds_read_b32 v38, v212 offset:18432
ds_read_b32 v39, v212 offset:19456
ds_read_b32 v40, v212 offset:32768
ds_read_b32 v41, v212 offset:33792
ds_read_b32 v42, v212 offset:34816
ds_read_b32 v43, v212 offset:35840
ds_read_b32 v44, v212 offset:49152
ds_read_b32 v45, v212 offset:50176
ds_read_b32 v46, v212 offset:51200
ds_read_b32 v47, v212 offset:52224
s_waitcnt lgkmcnt(0)
v_add_f32 v32, v32, v36
v_add_f32 v40, v40, v44
v_add_f32 v200, v32, v40
v_add_f32 v33, v33, v37
v_add_f32 v41, v41, v45
v_add_f32 v201, v33, v41
v_add_f32 v34, v34, v38
v_add_f32 v42, v42, v46
v_add_f32 v202, v34, v42
v_add_f32 v35, v35, v39
v_add_f32 v43, v43, v47
v_add_f32 v203, v35, v43
v_fma_f32 v200, -v194, v16, v200
v_mul_f32 v200, v200, v195
v_fma_f32 v201, -v194, v17, v201
v_mul_f32 v201, v201, v195
v_fma_f32 v202, -v194, v18, v202
v_mul_f32 v202, v202, v195
v_fma_f32 v203, -v194, v19, v203
v_mul_f32 v203, v203, v195
v_cvt_pk_bf16_f32 v204, v200, v201
v_mov_b32 v205, s12
v_cvt_pk_bf16_f32 v206, v202, v203
v_mov_b32 v207, s12
s_lshl_b32 s36, s46, 14
s_add_u32 s36, s36, 0x1000
buffer_store_dwordx4 v[204:207], v11, s[24:27], s36 offen sc1
s_nop 1
s_barrier
s_mov_b32 s28, 0x76543201
s_mov_b32 s29, 0xb9753210
s_mov_b32 s53, 0
s_and_b32 s31, s28, 15
s_lshr_b32 s28, s28, 4
s_mul_i32 s36, s31, 0x7000
s_add_u32 s36, s36, 0x781000
s_lshl_b32 s37, s31, 12
s_add_u32 s37, s37, 0x7f1000
buffer_load_dwordx4 v[32:35], v4, s[24:27], s36 offen sc1
s_add_u32 s46, s36, 0x800
buffer_load_dwordx4 v[36:39], v4, s[24:27], s46 offen sc1
s_add_u32 s46, s36, 0x1000
buffer_load_dwordx4 v[40:43], v4, s[24:27], s46 offen sc1
s_add_u32 s46, s36, 0x1800
buffer_load_dwordx4 v[44:47], v4, s[24:27], s46 offen sc1
s_add_u32 s46, s36, 0x2000
buffer_load_dwordx4 v[48:51], v4, s[24:27], s46 offen sc1
s_add_u32 s46, s36, 0x2800
buffer_load_dwordx4 v[52:55], v4, s[24:27], s46 offen sc1
s_add_u32 s46, s36, 0x3000
buffer_load_dwordx4 v[56:59], v4, s[24:27], s46 offen sc1
buffer_load_dwordx4 v[60:63], v5, s[24:27], s37 offen sc1
s_waitcnt vmcnt(0)
Lqloop_1_%=:
s_and_b32 s30, s29, 15
s_lshr_b32 s29, s29, 4
s_lshl_b32 s39, s31, 12
s_add_u32 s39, s39, 0x801000
s_lshl_b32 s38, s31, 14
s_add_u32 s38, s38, 0x21000
s_and_b32 s49, s53, 1
s_lshl_b32 s49, s49, 13
s_mov_b32 s33, 0
s_waitcnt vmcnt(0)
v_bitop3_b32 v224, v33, v35, s12 bitop3:0x7e
v_bitop3_b32 v225, v37, v39, s12 bitop3:0x7e
v_bitop3_b32 v226, v41, v43, s12 bitop3:0x7e
v_bitop3_b32 v227, v45, v47, s12 bitop3:0x7e
v_bitop3_b32 v228, v49, v51, s12 bitop3:0x7e
v_bitop3_b32 v229, v53, v55, s12 bitop3:0x7e
v_bitop3_b32 v230, v57, v59, s12 bitop3:0x7e
v_bitop3_b32 v231, v61, v63, s12 bitop3:0x7e
v_or3_b32 v224, v224, v225, v226
v_or3_b32 v227, v227, v228, v229
v_or_b32 v230, v230, v231
v_or3_b32 v224, v224, v227, v230
v_cmp_ne_u32 vcc, 0, v224
s_cbranch_vccz LqA_4_%=
s_lshr_b32 s61, s53, 2
s_mov_b32 s62, 0x1f0000a8
s_cmp_eq_u32 s61, 1
s_cselect_b32 s62, 0x2f2f2f2f, s62
s_cmp_eq_u32 s61, 2
s_cselect_b32 s62, 0x0, s62
s_cmp_eq_u32 s61, 3
s_cselect_b32 s62, 0x0, s62
s_and_b32 s61, s53, 3
s_lshl_b32 s61, s61, 3
s_lshr_b32 s62, s62, s61
s_and_b32 s62, s62, 0xff
s_lshl_b32 s62, s62, 3
Lqcg_8_%=:
s_memrealtime s[58:59]
s_waitcnt lgkmcnt(0)
s_sub_u32 s61, s58, s60
s_cmp_ge_u32 s61, s62
s_cbranch_scc1 Lqcx_9_%=
s_sleep 3
s_branch Lqcg_8_%=
Lqcx_9_%=:
buffer_load_dwordx4 v[32:35], v4, s[24:27], s36 offen sc1
s_add_u32 s46, s36, 0x800
buffer_load_dwordx4 v[36:39], v4, s[24:27], s46 offen sc1
s_add_u32 s46, s36, 0x1000
buffer_load_dwordx4 v[40:43], v4, s[24:27], s46 offen sc1
s_add_u32 s46, s36, 0x1800
buffer_load_dwordx4 v[44:47], v4, s[24:27], s46 offen sc1
s_add_u32 s46, s36, 0x2000
buffer_load_dwordx4 v[48:51], v4, s[24:27], s46 offen sc1
s_add_u32 s46, s36, 0x2800
buffer_load_dwordx4 v[52:55], v4, s[24:27], s46 offen sc1
s_add_u32 s46, s36, 0x3000
buffer_load_dwordx4 v[56:59], v4, s[24:27], s46 offen sc1
buffer_load_dwordx4 v[60:63], v5, s[24:27], s37 offen sc1
s_sleep 8
Lql0_2_%=:
buffer_load_dwordx4 v[64:67], v4, s[24:27], s36 offen sc1
s_add_u32 s46, s36, 0x800
buffer_load_dwordx4 v[68:71], v4, s[24:27], s46 offen sc1
s_add_u32 s46, s36, 0x1000
buffer_load_dwordx4 v[72:75], v4, s[24:27], s46 offen sc1
s_add_u32 s46, s36, 0x1800
buffer_load_dwordx4 v[76:79], v4, s[24:27], s46 offen sc1
s_add_u32 s46, s36, 0x2000
buffer_load_dwordx4 v[80:83], v4, s[24:27], s46 offen sc1
s_add_u32 s46, s36, 0x2800
buffer_load_dwordx4 v[84:87], v4, s[24:27], s46 offen sc1
s_add_u32 s46, s36, 0x3000
buffer_load_dwordx4 v[88:91], v4, s[24:27], s46 offen sc1
buffer_load_dwordx4 v[92:95], v5, s[24:27], s37 offen sc1
s_add_u32 s33, s33, 1
s_waitcnt vmcnt(8)
v_bitop3_b32 v224, v33, v35, s12 bitop3:0x7e
v_bitop3_b32 v225, v37, v39, s12 bitop3:0x7e
v_bitop3_b32 v226, v41, v43, s12 bitop3:0x7e
v_bitop3_b32 v227, v45, v47, s12 bitop3:0x7e
v_bitop3_b32 v228, v49, v51, s12 bitop3:0x7e
v_bitop3_b32 v229, v53, v55, s12 bitop3:0x7e
v_bitop3_b32 v230, v57, v59, s12 bitop3:0x7e
v_bitop3_b32 v231, v61, v63, s12 bitop3:0x7e
v_or3_b32 v224, v224, v225, v226
v_or3_b32 v227, v227, v228, v229
v_or_b32 v230, v230, v231
v_or3_b32 v224, v224, v227, v230
v_cmp_ne_u32 vcc, 0, v224
s_cbranch_vccz LqA_4_%=
buffer_load_dwordx4 v[32:35], v4, s[24:27], s36 offen sc1
s_add_u32 s46, s36, 0x800
buffer_load_dwordx4 v[36:39], v4, s[24:27], s46 offen sc1
s_add_u32 s46, s36, 0x1000
buffer_load_dwordx4 v[40:43], v4, s[24:27], s46 offen sc1
s_add_u32 s46, s36, 0x1800
buffer_load_dwordx4 v[44:47], v4, s[24:27], s46 offen sc1
s_add_u32 s46, s36, 0x2000
buffer_load_dwordx4 v[48:51], v4, s[24:27], s46 offen sc1
s_add_u32 s46, s36, 0x2800
buffer_load_dwordx4 v[52:55], v4, s[24:27], s46 offen sc1
s_add_u32 s46, s36, 0x3000
buffer_load_dwordx4 v[56:59], v4, s[24:27], s46 offen sc1
buffer_load_dwordx4 v[60:63], v5, s[24:27], s37 offen sc1
s_waitcnt vmcnt(8)
v_bitop3_b32 v224, v65, v67, s12 bitop3:0x7e
v_bitop3_b32 v225, v69, v71, s12 bitop3:0x7e
v_bitop3_b32 v226, v73, v75, s12 bitop3:0x7e
v_bitop3_b32 v227, v77, v79, s12 bitop3:0x7e
v_bitop3_b32 v228, v81, v83, s12 bitop3:0x7e
v_bitop3_b32 v229, v85, v87, s12 bitop3:0x7e
v_bitop3_b32 v230, v89, v91, s12 bitop3:0x7e
v_bitop3_b32 v231, v93, v95, s12 bitop3:0x7e
v_or3_b32 v224, v224, v225, v226
v_or3_b32 v227, v227, v228, v229
v_or_b32 v230, v230, v231
v_or3_b32 v224, v224, v227, v230
v_cmp_ne_u32 vcc, 0, v224
s_cbranch_vccz LqB_5_%=
s_cmp_lt_u32 s33, 32768
s_cbranch_scc1 Lql0_2_%=
s_branch Lqab_7_%=
LqB_5_%=:
s_nop 7
s_nop 1
v_lshlrev_b32 v200, 16, v64
v_and_b32 v201, 0xffff0000, v64
v_lshlrev_b32 v202, 16, v66
v_and_b32 v203, 0xffff0000, v66
v_lshlrev_b32 v228, 16, v68
v_and_b32 v229, 0xffff0000, v68
v_lshlrev_b32 v230, 16, v70
v_and_b32 v231, 0xffff0000, v70
v_pk_add_f32 v[200:201], v[200:201], v[228:229]
v_pk_add_f32 v[202:203], v[202:203], v[230:231]
v_lshlrev_b32 v224, 16, v72
v_and_b32 v225, 0xffff0000, v72
v_lshlrev_b32 v226, 16, v74
v_and_b32 v227, 0xffff0000, v74
v_pk_add_f32 v[200:201], v[200:201], v[224:225]
v_pk_add_f32 v[202:203], v[202:203], v[226:227]
v_lshlrev_b32 v228, 16, v76
v_and_b32 v229, 0xffff0000, v76
v_lshlrev_b32 v230, 16, v78
v_and_b32 v231, 0xffff0000, v78
v_pk_add_f32 v[200:201], v[200:201], v[228:229]
v_pk_add_f32 v[202:203], v[202:203], v[230:231]
v_lshlrev_b32 v224, 16, v80
v_and_b32 v225, 0xffff0000, v80
v_lshlrev_b32 v226, 16, v82
v_and_b32 v227, 0xffff0000, v82
v_pk_add_f32 v[200:201], v[200:201], v[224:225]
v_pk_add_f32 v[202:203], v[202:203], v[226:227]
v_lshlrev_b32 v228, 16, v84
v_and_b32 v229, 0xffff0000, v84
v_lshlrev_b32 v230, 16, v86
v_and_b32 v231, 0xffff0000, v86
v_pk_add_f32 v[200:201], v[200:201], v[228:229]
v_pk_add_f32 v[202:203], v[202:203], v[230:231]
v_lshlrev_b32 v224, 16, v88
v_and_b32 v225, 0xffff0000, v88
v_lshlrev_b32 v226, 16, v90
v_and_b32 v227, 0xffff0000, v90
v_pk_add_f32 v[200:201], v[200:201], v[224:225]
v_pk_add_f32 v[202:203], v[202:203], v[226:227]
v_mov_b32 v20, v92
v_mov_b32 v21, v94
s_branch LqJ_6_%=
LqA_4_%=:
s_nop 1
v_lshlrev_b32 v200, 16, v32
v_and_b32 v201, 0xffff0000, v32
v_lshlrev_b32 v202, 16, v34
v_and_b32 v203, 0xffff0000, v34
v_lshlrev_b32 v228, 16, v36
v_and_b32 v229, 0xffff0000, v36
v_lshlrev_b32 v230, 16, v38
v_and_b32 v231, 0xffff0000, v38
v_pk_add_f32 v[200:201], v[200:201], v[228:229]
v_pk_add_f32 v[202:203], v[202:203], v[230:231]
v_lshlrev_b32 v224, 16, v40
v_and_b32 v225, 0xffff0000, v40
v_lshlrev_b32 v226, 16, v42
v_and_b32 v227, 0xffff0000, v42
v_pk_add_f32 v[200:201], v[200:201], v[224:225]
v_pk_add_f32 v[202:203], v[202:203], v[226:227]
v_lshlrev_b32 v228, 16, v44
v_and_b32 v229, 0xffff0000, v44
v_lshlrev_b32 v230, 16, v46
v_and_b32 v231, 0xffff0000, v46
v_pk_add_f32 v[200:201], v[200:201], v[228:229]
v_pk_add_f32 v[202:203], v[202:203], v[230:231]
v_lshlrev_b32 v224, 16, v48
v_and_b32 v225, 0xffff0000, v48
v_lshlrev_b32 v226, 16, v50
v_and_b32 v227, 0xffff0000, v50
v_pk_add_f32 v[200:201], v[200:201], v[224:225]
v_pk_add_f32 v[202:203], v[202:203], v[226:227]
v_lshlrev_b32 v228, 16, v52
v_and_b32 v229, 0xffff0000, v52
v_lshlrev_b32 v230, 16, v54
v_and_b32 v231, 0xffff0000, v54
v_pk_add_f32 v[200:201], v[200:201], v[228:229]
v_pk_add_f32 v[202:203], v[202:203], v[230:231]
v_lshlrev_b32 v224, 16, v56
v_and_b32 v225, 0xffff0000, v56
v_lshlrev_b32 v226, 16, v58
v_and_b32 v227, 0xffff0000, v58
v_pk_add_f32 v[200:201], v[200:201], v[224:225]
v_pk_add_f32 v[202:203], v[202:203], v[226:227]
v_mov_b32 v20, v60
v_mov_b32 v21, v62
LqJ_6_%=:
s_nop 1
s_memrealtime s[60:61]
Lmk_10_%=:
s_and_b32 s31, s28, 15
s_lshr_b32 s28, s28, 4
s_mul_i32 s36, s31, 0x7000
s_add_u32 s36, s36, 0x781000
s_lshl_b32 s37, s31, 12
s_add_u32 s37, s37, 0x7f1000
buffer_load_dwordx4 v[32:35], v4, s[24:27], s36 offen sc1
s_add_u32 s46, s36, 0x800
buffer_load_dwordx4 v[36:39], v4, s[24:27], s46 offen sc1
s_add_u32 s46, s36, 0x1000
buffer_load_dwordx4 v[40:43], v4, s[24:27], s46 offen sc1
s_add_u32 s46, s36, 0x1800
buffer_load_dwordx4 v[44:47], v4, s[24:27], s46 offen sc1
s_add_u32 s46, s36, 0x2000
buffer_load_dwordx4 v[48:51], v4, s[24:27], s46 offen sc1
s_add_u32 s46, s36, 0x2800
buffer_load_dwordx4 v[52:55], v4, s[24:27], s46 offen sc1
s_add_u32 s46, s36, 0x3000
buffer_load_dwordx4 v[56:59], v4, s[24:27], s46 offen sc1
buffer_load_dwordx4 v[60:63], v5, s[24:27], s37 offen sc1
v_permlane32_swap_b32 v200, v202
v_permlane32_swap_b32 v201, v203
v_add_f32 v200, v200, v202
v_add_f32 v201, v201, v203
v_add_f32 v200, v200, v20
v_add_f32 v201, v201, v21
v_cvt_pk_bf16_f32 v204, v200, v201
ds_write_b32 v6, v204
v_add_f32 v212, v200, v201
ds_read_b128 v[168:171], v7 offset:0
v_mov_b32 v248, v200
v_add_f32_dpp v212, v212, v212 quad_perm:[1,0,3,2] row_mask:0xf bank_mask:0xf bound_ctrl:1
v_mov_b32 v250, v201
ds_read_b128 v[172:175], v7 offset:64
v_add_f32_dpp v212, v212, v212 quad_perm:[2,3,0,1] row_mask:0xf bank_mask:0xf bound_ctrl:1
v_add_u32 v216, s49, v8
v_add_u32 v217, s49, v10
ds_read_b128 v[176:179], v7 offset:128
v_add_f32_dpp v212, v212, v212 row_half_mirror row_mask:0xf bank_mask:0xf bound_ctrl:1
v_mov_b32 v249, s12
v_mov_b32 v251, s12
ds_read_b128 v[180:183], v7 offset:192
v_add_f32_dpp v212, v212, v212 row_ror:8 row_mask:0xf bank_mask:0xf bound_ctrl:1
v_mov_b32 v214, v212
v_add_u32 v218, s49, v9
v_mov_b32 v219, s49
v_permlane16_swap_b32 v212, v214
v_add_f32 v212, v212, v214
v_mov_b32 v214, v212
s_nop 1
v_permlane32_swap_b32 v212, v214
v_add_f32 v212, v212, v214
v_mul_f32 v212, 0x3c000000, v212
v_sub_f32 v213, v200, v212
v_sub_f32 v215, v201, v212
v_mul_f32 v213, v213, v213
v_fma_f32 v213, v215, v215, v213
s_nop 1
v_add_f32_dpp v213, v213, v213 quad_perm:[1,0,3,2] row_mask:0xf bank_mask:0xf bound_ctrl:1
s_nop 1
v_add_f32_dpp v213, v213, v213 quad_perm:[2,3,0,1] row_mask:0xf bank_mask:0xf bound_ctrl:1
s_nop 1
v_add_f32_dpp v213, v213, v213 row_half_mirror row_mask:0xf bank_mask:0xf bound_ctrl:1
s_waitcnt lgkmcnt(3)
v_mfma_f32_16x16x32_bf16 v[100:103], v[168:171], a[4:7], 0
v_add_f32_dpp v213, v213, v213 row_ror:8 row_mask:0xf bank_mask:0xf bound_ctrl:1
v_mov_b32 v214, v213
v_mfma_f32_16x16x32_bf16 v[108:111], v[168:171], a[12:15], 0
s_nop 0
v_permlane16_swap_b32 v213, v214
v_add_f32 v213, v213, v214
v_mfma_f32_16x16x32_bf16 v[116:119], v[168:171], a[20:23], 0
v_mov_b32 v214, v213
v_mfma_f32_16x16x32_bf16 v[124:127], v[168:171], a[28:31], 0
s_nop 0
v_permlane32_swap_b32 v213, v214
v_add_f32 v213, v213, v214
v_mfma_f32_16x16x32_bf16 v[96:99], v[168:171], a[0:3], 0
v_mfma_f32_16x16x32_bf16 v[104:107], v[168:171], a[8:11], 0
v_mfma_f32_16x16x32_bf16 v[112:115], v[168:171], a[16:19], 0
v_mfma_f32_16x16x32_bf16 v[120:123], v[168:171], a[24:27], 0
v_mfma_f32_16x16x32_bf16 v[132:135], v[168:171], a[36:39], 0
v_mfma_f32_16x16x32_bf16 v[140:143], v[168:171], a[44:47], 0
v_mfma_f32_16x16x32_bf16 v[148:151], v[168:171], a[52:55], 0
v_mfma_f32_16x16x32_bf16 v[156:159], v[168:171], a[60:63], 0
v_mfma_f32_16x16x32_bf16 v[128:131], v[168:171], a[32:35], 0
v_mfma_f32_16x16x32_bf16 v[136:139], v[168:171], a[40:43], 0
v_mfma_f32_16x16x32_bf16 v[144:147], v[168:171], a[48:51], 0
v_mfma_f32_16x16x32_bf16 v[152:155], v[168:171], a[56:59], 0
s_waitcnt lgkmcnt(2)
v_mfma_f32_16x16x32_bf16 v[100:103], v[172:175], a[68:71], v[100:103]
v_mfma_f32_16x16x32_bf16 v[108:111], v[172:175], a[76:79], v[108:111]
v_mfma_f32_16x16x32_bf16 v[116:119], v[172:175], a[84:87], v[116:119]
v_mfma_f32_16x16x32_bf16 v[124:127], v[172:175], a[92:95], v[124:127]
v_mfma_f32_16x16x32_bf16 v[96:99], v[172:175], a[64:67], v[96:99]
v_mfma_f32_16x16x32_bf16 v[104:107], v[172:175], a[72:75], v[104:107]
v_mfma_f32_16x16x32_bf16 v[112:115], v[172:175], a[80:83], v[112:115]
v_mfma_f32_16x16x32_bf16 v[120:123], v[172:175], a[88:91], v[120:123]
v_mfma_f32_16x16x32_bf16 v[132:135], v[172:175], a[100:103], v[132:135]
v_mfma_f32_16x16x32_bf16 v[140:143], v[172:175], a[108:111], v[140:143]
v_mfma_f32_16x16x32_bf16 v[148:151], v[172:175], a[116:119], v[148:151]
v_mfma_f32_16x16x32_bf16 v[156:159], v[172:175], a[124:127], v[156:159]
v_mfma_f32_16x16x32_bf16 v[128:131], v[172:175], a[96:99], v[128:131]
v_mfma_f32_16x16x32_bf16 v[136:139], v[172:175], a[104:107], v[136:139]
v_mfma_f32_16x16x32_bf16 v[144:147], v[172:175], a[112:115], v[144:147]
v_mfma_f32_16x16x32_bf16 v[152:155], v[172:175], a[120:123], v[152:155]
s_waitcnt lgkmcnt(1)
v_mfma_f32_16x16x32_bf16 v[100:103], v[176:179], a[132:135], v[100:103]
v_mfma_f32_16x16x32_bf16 v[108:111], v[176:179], a[140:143], v[108:111]
v_mfma_f32_16x16x32_bf16 v[116:119], v[176:179], a[148:151], v[116:119]
v_mfma_f32_16x16x32_bf16 v[124:127], v[176:179], a[156:159], v[124:127]
v_mfma_f32_16x16x32_bf16 v[96:99], v[176:179], a[128:131], v[96:99]
v_mfma_f32_16x16x32_bf16 v[104:107], v[176:179], a[136:139], v[104:107]
v_mfma_f32_16x16x32_bf16 v[112:115], v[176:179], a[144:147], v[112:115]
v_mfma_f32_16x16x32_bf16 v[120:123], v[176:179], a[152:155], v[120:123]
v_mfma_f32_16x16x32_bf16 v[132:135], v[176:179], a[164:167], v[132:135]
v_mfma_f32_16x16x32_bf16 v[140:143], v[176:179], a[172:175], v[140:143]
v_mfma_f32_16x16x32_bf16 v[148:151], v[176:179], a[180:183], v[148:151]
v_mfma_f32_16x16x32_bf16 v[156:159], v[176:179], a[188:191], v[156:159]
v_mfma_f32_16x16x32_bf16 v[128:131], v[176:179], a[160:163], v[128:131]
v_mfma_f32_16x16x32_bf16 v[136:139], v[176:179], a[168:171], v[136:139]
v_mfma_f32_16x16x32_bf16 v[144:147], v[176:179], a[176:179], v[144:147]
v_mfma_f32_16x16x32_bf16 v[152:155], v[176:179], a[184:187], v[152:155]
s_waitcnt lgkmcnt(0)
v_mfma_f32_16x16x32_bf16 v[100:103], v[180:183], a[196:199], v[100:103]
v_mfma_f32_16x16x32_bf16 v[108:111], v[180:183], a[204:207], v[108:111]
v_mfma_f32_16x16x32_bf16 v[116:119], v[180:183], a[212:215], v[116:119]
v_mfma_f32_16x16x32_bf16 v[124:127], v[180:183], a[220:223], v[124:127]
v_mfma_f32_16x16x32_bf16 v[96:99], v[180:183], a[192:195], v[96:99]
s_nop 4
v_cndmask_b32 v204, v100, v108, s[40:41]
v_mfma_f32_16x16x32_bf16 v[104:107], v[180:183], a[200:203], v[104:107]
v_mfma_f32_16x16x32_bf16 v[112:115], v[180:183], a[208:211], v[112:115]
v_cndmask_b32 v206, v116, v124, s[40:41]
v_cndmask_b32 v204, v204, v206, s[42:43]
v_mfma_f32_16x16x32_bf16 v[120:123], v[180:183], a[216:219], v[120:123]
v_mov_b32 v201, v204
v_mfma_f32_16x16x32_bf16 v[132:135], v[180:183], a[228:231], v[132:135]
s_nop 1
v_cndmask_b32 v200, v96, v104, s[40:41]
v_mfma_f32_16x16x32_bf16 v[140:143], v[180:183], a[236:239], v[140:143]
v_mfma_f32_16x16x32_bf16 v[148:151], v[180:183], a[244:247], v[148:151]
s_nop 0
v_cndmask_b32 v202, v112, v120, s[40:41]
v_cndmask_b32 v200, v200, v202, s[42:43]
v_mfma_f32_16x16x32_bf16 v[156:159], v[180:183], a[252:255], v[156:159]
ds_write_b64 v216, v[200:201] offset:0
v_mfma_f32_16x16x32_bf16 v[128:131], v[180:183], a[224:227], v[128:131]
s_nop 0
v_cndmask_b32 v204, v132, v140, s[40:41]
v_mfma_f32_16x16x32_bf16 v[136:139], v[180:183], a[232:235], v[136:139]
v_mfma_f32_16x16x32_bf16 v[144:147], v[180:183], a[240:243], v[144:147]
s_nop 1
v_cndmask_b32 v206, v148, v156, s[40:41]
v_cndmask_b32 v204, v204, v206, s[42:43]
v_mfma_f32_16x16x32_bf16 v[152:155], v[180:183], a[248:251], v[152:155]
v_mov_b32 v201, v204
s_nop 0
v_cndmask_b32 v200, v128, v136, s[40:41]
s_nop 4
v_cndmask_b32 v202, v144, v152, s[40:41]
v_cndmask_b32 v200, v200, v202, s[42:43]
ds_write_b64 v216, v[200:201] offset:512
ds_write_b64 v217, v[212:213]
s_cmp_lg_u32 s14, 0
s_cbranch_scc1 Lqnp_11_%=
buffer_store_dwordx4 v[248:251], v5, s[24:27], s39 offen sc1
Lqnp_11_%=:
s_waitcnt lgkmcnt(0)
s_barrier
s_cmp_lg_u32 s13, 0
s_cbranch_scc1 Lqskip_12_%=
ds_read_b64 v[184:185], v219 offset:6144
v_mov_b32 v209, s12
ds_read_b64 v[186:187], v219 offset:6152
v_mov_b32 v211, s12
ds_read_b64 v[188:189], v219 offset:6160
ds_read_b64 v[190:191], v219 offset:6168
ds_read_b128 v[232:235], v218 offset:0
ds_read_b128 v[236:239], v218 offset:1024
ds_read_b128 v[240:243], v218 offset:2048
ds_read_b128 v[244:247], v218 offset:3072
s_waitcnt lgkmcnt(6)
v_add_f32 v200, v184, v186
v_add_f32 v201, v185, v187
s_waitcnt lgkmcnt(4)
v_add_f32 v202, v188, v190
v_add_f32 v200, v200, v202
v_mul_f32 v200, 0x3e800000, v200
v_add_f32 v202, v189, v191
v_sub_f32 v184, v184, v200
v_add_f32 v201, v201, v202
v_sub_f32 v186, v186, v200
v_mul_f32 v202, v184, v184
v_sub_f32 v188, v188, v200
v_fma_f32 v202, v186, v186, v202
v_sub_f32 v190, v190, v200
v_fma_f32 v202, v188, v188, v202
v_fma_f32 v202, v190, v190, v202
v_mov_b32 v184, 0x43000000
v_fma_f32 v201, v202, v184, v201
v_mov_b32 v186, 0x3727c5ac
v_mov_b32 v188, 0x3b000000
v_fma_f32 v201, v201, v188, v186
v_rsq_f32 v201, v201
s_waitcnt lgkmcnt(2)
v_add_f32 v232, v232, v236
v_add_f32 v233, v233, v237
s_waitcnt lgkmcnt(0)
v_add_f32 v240, v240, v244
v_add_f32 v241, v241, v245
v_add_f32 v234, v234, v238
v_add_f32 v242, v242, v246
v_add_f32 v235, v235, v239
v_add_f32 v243, v243, v247
v_add_f32 v204, v232, v240
v_add_f32 v205, v233, v241
v_add_f32 v206, v234, v242
v_add_f32 v207, v235, v243
v_fma_f32 v204, -v200, v16, v204
v_fma_f32 v205, -v200, v17, v205
v_fma_f32 v206, -v200, v18, v206
v_fma_f32 v207, -v200, v19, v207
v_mul_f32 v204, v204, v201
v_mul_f32 v205, v205, v201
v_mul_f32 v206, v206, v201
v_mul_f32 v207, v207, v201
v_cvt_pk_bf16_f32 v208, v204, v205
v_cvt_pk_bf16_f32 v210, v206, v207
buffer_store_dwordx4 v[208:211], v11, s[24:27], s38 offen sc1
Lqskip_12_%=:
s_nop 1
s_add_u32 s53, s53, 1
s_cmp_lt_u32 s53, 8
s_cbranch_scc1 Lqloop_1_%=
s_waitcnt vmcnt(0)
s_endpgm
Lrceq_%=:
Lqab_7_%=:
v_mov_b32 v252, 256
v_mov_b32 v253, 1
buffer_store_dword v253, v252, s[24:27], 0 offen sc1
s_nop 4
s_branch LqA_4_%=
)ASM" :
      CHAIN_IN((u32x4{(unsigned)lane, vSt, vWq, 0u}), (u32x4{vG, vX, vXBw, vXBr}), (u32x4{vEXw, vEXr, vSTw, vQp}), (u32x4{vXa, vXs, vQ1r, vQ1w}), sX, sA, sA)
      : "v16", "v17", "v18", "v19", "v20", "v21", "v22", "v23", "v24", "v25", "v26", "v27", "v28", "v29", "v30", "v31", "v32", "v33", "v34", "v35", "v36", "v37", "v38", "v39", "v40", "v41", "v42", "v43", "v44", "v45", "v46", "v47", "v48", "v49", "v50", "v51", "v52", "v53", "v54", "v55", "v56", "v57", "v58", "v59", "v60", "v61", "v62", "v63", "v64", "v65", "v66", "v67", "v68", "v69", "v70", "v71", "v72", "v73", "v74", "v75", "v76", "v77", "v78", "v79", "v80", "v81", "v82", "v83", "v84", "v85", "v86", "v87", "v88", "v89", "v90", "v91", "v92", "v93", "v94", "v95", "v96", "v97", "v98", "v99", "v100", "v101", "v102", "v103", "v104", "v105", "v106", "v107", "v108", "v109", "v110", "v111", "v112", "v113", "v114", "v115", "v116", "v117", "v118", "v119", "v120", "v121", "v122", "v123", "v124", "v125", "v126", "v127", "v128", "v129", "v130", "v131", "v132", "v133", "v134", "v135", "v136", "v137", "v138", "v139", "v140", "v141", "v142", "v143", "v144", "v145", "v146", "v147", "v148", "v149", "v150", "v151", "v152", "v153", "v154", "v155", "v156", "v157", "v158", "v159", "v160", "v161", "v162", "v163", "v164", "v165", "v166", "v167", "v168", "v169", "v170", "v171", "v172", "v173", "v174", "v175", "v176", "v177", "v178", "v179", "v180", "v181", "v182", "v183", "v184", "v185", "v186", "v187", "v188", "v189", "v190", "v191", "v192", "v193", "v194", "v195", "v196", "v197", "v198", "v199", "v200", "v201", "v202", "v203", "v204", "v205", "v206", "v207", "v208", "v209", "v210", "v211", "v212", "v213", "v214", "v215", "v216", "v217", "v218", "v219", "v220", "v221", "v222", "v223", "v224", "v225", "v226", "v227", "v228", "v229", "v230", "v231", "v232", "v233", "v234", "v235", "v236", "v237", "v238", "v239", "v240", "v241", "v242", "v243", "v244", "v245", "v246", "v247", "v248", "v249", "v250", "v251", "v252", "v253", "v254", "v255", "a0", "a1", "a2", "a3", "a4", "a5", "a6", "a7", "a8", "a9", "a10", "a11", "a12", "a13", "a14", "a15", "a16", "a17", "a18", "a19", "a20", "a21", "a22", "a23", "a24", "a25", "a26", "a27", "a28", "a29", "a30", "a31", "a32", "a33", "a34", "a35", "a36", "a37", "a38", "a39", "a40", "a41", "a42", "a43", "a44", "a45", "a46", "a47", "a48", "a49", "a50", "a51", "a52", "a53", "a54", "a55", "a56", "a57", "a58", "a59", "a60", "a61", "a62", "a63", "a64", "a65", "a66", "a67", "a68", "a69", "a70", "a71", "a72", "a73", "a74", "a75", "a76", "a77", "a78", "a79", "a80", "a81", "a82", "a83", "a84", "a85", "a86", "a87", "a88", "a89", "a90", "a91", "a92", "a93", "a94", "a95", "a96", "a97", "a98", "a99", "a100", "a101", "a102", "a103", "a104", "a105", "a106", "a107", "a108", "a109", "a110", "a111", "a112", "a113", "a114", "a115", "a116", "a117", "a118", "a119", "a120", "a121", "a122", "a123", "a124", "a125", "a126", "a127", "a128", "a129", "a130", "a131", "a132", "a133", "a134", "a135", "a136", "a137", "a138", "a139", "a140", "a141", "a142", "a143", "a144", "a145", "a146", "a147", "a148", "a149", "a150", "a151", "a152", "a153", "a154", "a155", "a156", "a157", "a158", "a159", "a160", "a161", "a162", "a163", "a164", "a165", "a166", "a167", "a168", "a169", "a170", "a171", "a172", "a173", "a174", "a175", "a176", "a177", "a178", "a179", "a180", "a181", "a182", "a183", "a184", "a185", "a186", "a187", "a188", "a189", "a190", "a191", "a192", "a193", "a194", "a195", "a196", "a197", "a198", "a199", "a200", "a201", "a202", "a203", "a204", "a205", "a206", "a207", "a208", "a209", "a210", "a211", "a212", "a213", "a214", "a215", "a216", "a217", "a218", "a219", "a220", "a221", "a222", "a223", "a224", "a225", "a226", "a227", "a228", "a229", "a230", "a231", "a232", "a233", "a234", "a235", "a236", "a237", "a238", "a239", "a240", "a241", "a242", "a243", "a244", "a245", "a246", "a247", "a248", "a249", "a250", "a251", "a252", "a253", "a254", "a255", "s28", "s29", "s30", "s31", "s33", "s34", "s35", "s36", "s37", "s38", "s39", "s40", "s41", "s42", "s43", "s44", "s45", "s46", "s47", "s50", "s51", "s52", "s53", "s54", "s55", "s56", "s57", "s58", "s59", "s60", "s61", "s62", "s63", "vcc", "memory");
  __builtin_unreachable();
}
__device__ __forceinline__ void role_f(const Params& p, int idx, unsigned tag, int w) {
  CHAIN_COMMON
  const unsigned n0 = 128 * w + 4 * q32 + 2 * ph;
  const unsigned vG = ph * 7 * 2048 + w * 512 + q32 * 16, vX = n0 * 8, vOut = n0 * 2;
  const u32x4 sA = mk_srd(p.out, 0, NB * DM * 2);
  asm volatile(R"ASM(
s_nop 4
s_mov_b32 s34, 0x5040100
s_mov_b32 s35, 0x7060302
s_mov_b32 s44, 0
s_mov_b32 s45, -1
s_mov_b32 s28, 0x5a493201
s_mov_b32 s29, 0xf8ed7c6b
s_mov_b32 s30, 0
v_mov_b32 v253, 0
buffer_load_dword v252, v253, s[24:27], 0 offen sc1
s_getpc_b64 s[56:57]
Lrc0f_%=:
s_and_b32 s57, s57, 0xffff
s_mov_b32 s58, Lrcef_%=-Lrc0f_%=
s_mov_b32 s59, 0x20000
v_lshlrev_b32 v251, 7, v0
s_lshl_b32 s46, s13, 13
v_add_u32 v251, s46, v251
buffer_load_dword v250, v251, s[56:59], 0 offen
s_memrealtime s[58:59]
s_waitcnt lgkmcnt(0)
s_mov_b32 s60, s58
s_waitcnt vmcnt(1)
v_readfirstlane_b32 s12, v252
s_nop 3
s_add_u32 s12, s12, 1
s_mov_b32 s28, 0x70654321
s_mov_b32 s29, 0xfedca864
s_mov_b32 s53, 0
s_and_b32 s31, s28, 15
s_lshr_b32 s28, s28, 4
s_add_u32 s47, s31, 8
s_mul_i32 s36, s47, 0x7000
s_add_u32 s36, s36, 0x781000
s_lshl_b32 s37, s47, 12
s_add_u32 s37, s37, 0x7f1000
buffer_load_dwordx4 v[32:35], v4, s[24:27], s36 offen sc1
s_add_u32 s46, s36, 0x800
buffer_load_dwordx4 v[36:39], v4, s[24:27], s46 offen sc1
s_add_u32 s46, s36, 0x1000
buffer_load_dwordx4 v[40:43], v4, s[24:27], s46 offen sc1
s_add_u32 s46, s36, 0x1800
buffer_load_dwordx4 v[44:47], v4, s[24:27], s46 offen sc1
s_add_u32 s46, s36, 0x2000
buffer_load_dwordx4 v[48:51], v4, s[24:27], s46 offen sc1
s_add_u32 s46, s36, 0x2800
buffer_load_dwordx4 v[52:55], v4, s[24:27], s46 offen sc1
s_add_u32 s46, s36, 0x3000
buffer_load_dwordx4 v[56:59], v4, s[24:27], s46 offen sc1
buffer_load_dwordx4 v[60:63], v5, s[24:27], s37 offen sc1
s_waitcnt vmcnt(0)
Lfloop_1_%=:
s_and_b32 s30, s29, 15
s_lshr_b32 s29, s29, 4
s_lshl_b32 s38, s31, 10
s_mov_b32 s33, 0
s_waitcnt vmcnt(0)
v_bitop3_b32 v96, v33, v35, s12 bitop3:0x7e
v_bitop3_b32 v97, v37, v39, s12 bitop3:0x7e
v_bitop3_b32 v98, v41, v43, s12 bitop3:0x7e
v_bitop3_b32 v99, v45, v47, s12 bitop3:0x7e
v_bitop3_b32 v100, v49, v51, s12 bitop3:0x7e
v_bitop3_b32 v101, v53, v55, s12 bitop3:0x7e
v_bitop3_b32 v102, v57, v59, s12 bitop3:0x7e
v_bitop3_b32 v103, v61, v63, s12 bitop3:0x7e
v_or3_b32 v96, v96, v97, v98
v_or3_b32 v99, v99, v100, v101
v_or_b32 v102, v102, v103
v_or3_b32 v96, v96, v99, v102
v_cmp_ne_u32 vcc, 0, v96
s_cbranch_vccz LfA_4_%=
s_lshr_b32 s61, s53, 2
s_mov_b32 s62, 0x2f2f2ffa
s_cmp_eq_u32 s61, 1
s_cselect_b32 s62, 0x252f, s62
s_cmp_eq_u32 s61, 2
s_cselect_b32 s62, 0x0, s62
s_cmp_eq_u32 s61, 3
s_cselect_b32 s62, 0x0, s62
s_and_b32 s61, s53, 3
s_lshl_b32 s61, s61, 3
s_lshr_b32 s62, s62, s61
s_and_b32 s62, s62, 0xff
s_lshl_b32 s62, s62, 3
Lfcg_8_%=:
s_memrealtime s[58:59]
s_waitcnt lgkmcnt(0)
s_sub_u32 s61, s58, s60
s_cmp_ge_u32 s61, s62
s_cbranch_scc1 Lfcx_9_%=
s_sleep 3
s_branch Lfcg_8_%=
Lfcx_9_%=:
buffer_load_dwordx4 v[32:35], v4, s[24:27], s36 offen sc1
s_add_u32 s46, s36, 0x800
buffer_load_dwordx4 v[36:39], v4, s[24:27], s46 offen sc1
s_add_u32 s46, s36, 0x1000
buffer_load_dwordx4 v[40:43], v4, s[24:27], s46 offen sc1
s_add_u32 s46, s36, 0x1800
buffer_load_dwordx4 v[44:47], v4, s[24:27], s46 offen sc1
s_add_u32 s46, s36, 0x2000
buffer_load_dwordx4 v[48:51], v4, s[24:27], s46 offen sc1
s_add_u32 s46, s36, 0x2800
buffer_load_dwordx4 v[52:55], v4, s[24:27], s46 offen sc1
s_add_u32 s46, s36, 0x3000
buffer_load_dwordx4 v[56:59], v4, s[24:27], s46 offen sc1
buffer_load_dwordx4 v[60:63], v5, s[24:27], s37 offen sc1
s_sleep 8
Lfl0_2_%=:
buffer_load_dwordx4 v[64:67], v4, s[24:27], s36 offen sc1
s_add_u32 s46, s36, 0x800
buffer_load_dwordx4 v[68:71], v4, s[24:27], s46 offen sc1
s_add_u32 s46, s36, 0x1000
buffer_load_dwordx4 v[72:75], v4, s[24:27], s46 offen sc1
s_add_u32 s46, s36, 0x1800
buffer_load_dwordx4 v[76:79], v4, s[24:27], s46 offen sc1
s_add_u32 s46, s36, 0x2000
buffer_load_dwordx4 v[80:83], v4, s[24:27], s46 offen sc1
s_add_u32 s46, s36, 0x2800
buffer_load_dwordx4 v[84:87], v4, s[24:27], s46 offen sc1
s_add_u32 s46, s36, 0x3000
buffer_load_dwordx4 v[88:91], v4, s[24:27], s46 offen sc1
buffer_load_dwordx4 v[92:95], v5, s[24:27], s37 offen sc1
s_add_u32 s33, s33, 1
s_waitcnt vmcnt(8)
v_bitop3_b32 v96, v33, v35, s12 bitop3:0x7e
v_bitop3_b32 v97, v37, v39, s12 bitop3:0x7e
v_bitop3_b32 v98, v41, v43, s12 bitop3:0x7e
v_bitop3_b32 v99, v45, v47, s12 bitop3:0x7e
v_bitop3_b32 v100, v49, v51, s12 bitop3:0x7e
v_bitop3_b32 v101, v53, v55, s12 bitop3:0x7e
v_bitop3_b32 v102, v57, v59, s12 bitop3:0x7e
v_bitop3_b32 v103, v61, v63, s12 bitop3:0x7e
v_or3_b32 v96, v96, v97, v98
v_or3_b32 v99, v99, v100, v101
v_or_b32 v102, v102, v103
v_or3_b32 v96, v96, v99, v102
v_cmp_ne_u32 vcc, 0, v96
s_cbranch_vccz LfA_4_%=
buffer_load_dwordx4 v[32:35], v4, s[24:27], s36 offen sc1
s_add_u32 s46, s36, 0x800
buffer_load_dwordx4 v[36:39], v4, s[24:27], s46 offen sc1
s_add_u32 s46, s36, 0x1000
buffer_load_dwordx4 v[40:43], v4, s[24:27], s46 offen sc1
s_add_u32 s46, s36, 0x1800
buffer_load_dwordx4 v[44:47], v4, s[24:27], s46 offen sc1
s_add_u32 s46, s36, 0x2000
buffer_load_dwordx4 v[48:51], v4, s[24:27], s46 offen sc1
s_add_u32 s46, s36, 0x2800
buffer_load_dwordx4 v[52:55], v4, s[24:27], s46 offen sc1
s_add_u32 s46, s36, 0x3000
buffer_load_dwordx4 v[56:59], v4, s[24:27], s46 offen sc1
buffer_load_dwordx4 v[60:63], v5, s[24:27], s37 offen sc1
s_waitcnt vmcnt(8)
v_bitop3_b32 v96, v65, v67, s12 bitop3:0x7e
v_bitop3_b32 v97, v69, v71, s12 bitop3:0x7e
v_bitop3_b32 v98, v73, v75, s12 bitop3:0x7e
v_bitop3_b32 v99, v77, v79, s12 bitop3:0x7e
v_bitop3_b32 v100, v81, v83, s12 bitop3:0x7e
v_bitop3_b32 v101, v85, v87, s12 bitop3:0x7e
v_bitop3_b32 v102, v89, v91, s12 bitop3:0x7e
v_bitop3_b32 v103, v93, v95, s12 bitop3:0x7e
v_or3_b32 v96, v96, v97, v98
v_or3_b32 v99, v99, v100, v101
v_or_b32 v102, v102, v103
v_or3_b32 v96, v96, v99, v102
v_cmp_ne_u32 vcc, 0, v96
s_cbranch_vccz LfB_5_%=
s_cmp_lt_u32 s33, 32768
s_cbranch_scc1 Lfl0_2_%=
s_branch Lfab_7_%=
LfB_5_%=:
s_nop 7
s_nop 1
v_lshlrev_b32 v200, 16, v64
v_and_b32 v201, 0xffff0000, v64
v_lshlrev_b32 v202, 16, v66
v_and_b32 v203, 0xffff0000, v66
v_lshlrev_b32 v100, 16, v68
v_and_b32 v101, 0xffff0000, v68
v_lshlrev_b32 v102, 16, v70
v_and_b32 v103, 0xffff0000, v70
v_pk_add_f32 v[200:201], v[200:201], v[100:101]
v_pk_add_f32 v[202:203], v[202:203], v[102:103]
v_lshlrev_b32 v96, 16, v72
v_and_b32 v97, 0xffff0000, v72
v_lshlrev_b32 v98, 16, v74
v_and_b32 v99, 0xffff0000, v74
v_pk_add_f32 v[200:201], v[200:201], v[96:97]
v_pk_add_f32 v[202:203], v[202:203], v[98:99]
v_lshlrev_b32 v100, 16, v76
v_and_b32 v101, 0xffff0000, v76
v_lshlrev_b32 v102, 16, v78
v_and_b32 v103, 0xffff0000, v78
v_pk_add_f32 v[200:201], v[200:201], v[100:101]
v_pk_add_f32 v[202:203], v[202:203], v[102:103]
v_lshlrev_b32 v96, 16, v80
v_and_b32 v97, 0xffff0000, v80
v_lshlrev_b32 v98, 16, v82
v_and_b32 v99, 0xffff0000, v82
v_pk_add_f32 v[200:201], v[200:201], v[96:97]
v_pk_add_f32 v[202:203], v[202:203], v[98:99]
v_lshlrev_b32 v100, 16, v84
v_and_b32 v101, 0xffff0000, v84
v_lshlrev_b32 v102, 16, v86
v_and_b32 v103, 0xffff0000, v86
v_pk_add_f32 v[200:201], v[200:201], v[100:101]
v_pk_add_f32 v[202:203], v[202:203], v[102:103]
v_lshlrev_b32 v96, 16, v88
v_and_b32 v97, 0xffff0000, v88
v_lshlrev_b32 v98, 16, v90
v_and_b32 v99, 0xffff0000, v90
v_pk_add_f32 v[200:201], v[200:201], v[96:97]
v_pk_add_f32 v[202:203], v[202:203], v[98:99]
v_mov_b32 v20, v92
v_mov_b32 v21, v94
s_branch LfJ_6_%=
LfA_4_%=:
s_nop 1
v_lshlrev_b32 v200, 16, v32
v_and_b32 v201, 0xffff0000, v32
v_lshlrev_b32 v202, 16, v34
v_and_b32 v203, 0xffff0000, v34
v_lshlrev_b32 v100, 16, v36
v_and_b32 v101, 0xffff0000, v36
v_lshlrev_b32 v102, 16, v38
v_and_b32 v103, 0xffff0000, v38
v_pk_add_f32 v[200:201], v[200:201], v[100:101]
v_pk_add_f32 v[202:203], v[202:203], v[102:103]
v_lshlrev_b32 v96, 16, v40
v_and_b32 v97, 0xffff0000, v40
v_lshlrev_b32 v98, 16, v42
v_and_b32 v99, 0xffff0000, v42
v_pk_add_f32 v[200:201], v[200:201], v[96:97]
v_pk_add_f32 v[202:203], v[202:203], v[98:99]
v_lshlrev_b32 v100, 16, v44
v_and_b32 v101, 0xffff0000, v44
v_lshlrev_b32 v102, 16, v46
v_and_b32 v103, 0xffff0000, v46
v_pk_add_f32 v[200:201], v[200:201], v[100:101]
v_pk_add_f32 v[202:203], v[202:203], v[102:103]
v_lshlrev_b32 v96, 16, v48
v_and_b32 v97, 0xffff0000, v48
v_lshlrev_b32 v98, 16, v50
v_and_b32 v99, 0xffff0000, v50
v_pk_add_f32 v[200:201], v[200:201], v[96:97]
v_pk_add_f32 v[202:203], v[202:203], v[98:99]
v_lshlrev_b32 v100, 16, v52
v_and_b32 v101, 0xffff0000, v52
v_lshlrev_b32 v102, 16, v54
v_and_b32 v103, 0xffff0000, v54
v_pk_add_f32 v[200:201], v[200:201], v[100:101]
v_pk_add_f32 v[202:203], v[202:203], v[102:103]
v_lshlrev_b32 v96, 16, v56
v_and_b32 v97, 0xffff0000, v56
v_lshlrev_b32 v98, 16, v58
v_and_b32 v99, 0xffff0000, v58
v_pk_add_f32 v[200:201], v[200:201], v[96:97]
v_pk_add_f32 v[202:203], v[202:203], v[98:99]
v_mov_b32 v20, v60
v_mov_b32 v21, v62
LfJ_6_%=:
s_nop 1
s_memrealtime s[60:61]
Lmk_10_%=:
s_and_b32 s31, s28, 15
s_lshr_b32 s28, s28, 4
s_add_u32 s47, s31, 8
s_mul_i32 s36, s47, 0x7000
s_add_u32 s36, s36, 0x781000
s_lshl_b32 s37, s47, 12
s_add_u32 s37, s37, 0x7f1000
buffer_load_dwordx4 v[32:35], v4, s[24:27], s36 offen sc1
s_add_u32 s46, s36, 0x800
buffer_load_dwordx4 v[36:39], v4, s[24:27], s46 offen sc1
s_add_u32 s46, s36, 0x1000
buffer_load_dwordx4 v[40:43], v4, s[24:27], s46 offen sc1
s_add_u32 s46, s36, 0x1800
buffer_load_dwordx4 v[44:47], v4, s[24:27], s46 offen sc1
s_add_u32 s46, s36, 0x2000
buffer_load_dwordx4 v[48:51], v4, s[24:27], s46 offen sc1
s_add_u32 s46, s36, 0x2800
buffer_load_dwordx4 v[52:55], v4, s[24:27], s46 offen sc1
s_add_u32 s46, s36, 0x3000
buffer_load_dwordx4 v[56:59], v4, s[24:27], s46 offen sc1
buffer_load_dwordx4 v[60:63], v5, s[24:27], s37 offen sc1
v_permlane32_swap_b32 v200, v202
v_permlane32_swap_b32 v201, v203
v_add_f32 v200, v200, v202
v_add_f32 v201, v201, v203
v_add_f32 v200, v200, v20
v_add_f32 v201, v201, v21
v_cvt_pk_bf16_f32 v204, v200, v201
buffer_store_dword v204, v6, s[16:19], s38 offen 
s_add_u32 s53, s53, 1
s_cmp_lt_u32 s53, 8
s_cbranch_scc1 Lfloop_1_%=
s_barrier
v_mov_b32 v200, 0
v_mov_b32 v201, s12
s_mov_b32 exec_lo, 1
s_mov_b32 exec_hi, 0
s_cmp_lg_u32 s13, 0
s_cbranch_scc1 Lfw_11_%=
buffer_store_dword v201, v200, s[24:27], 0 offen sc1
Lfw_11_%=:
s_waitcnt vmcnt(0)
s_endpgm
Lrcef_%=:
Lfab_7_%=:
v_mov_b32 v252, 256
v_mov_b32 v253, 1
buffer_store_dword v253, v252, s[24:27], 0 offen sc1
s_nop 4
s_branch LfA_4_%=
)ASM" :
      CHAIN_IN((u32x4{(unsigned)lane, vSt, 0u, 0u}), (u32x4{vG, vX, vOut, 0u}), (u32x4{0u, 0u, 0u, 0u}), (u32x4{0u, 0u, 0u, 0u}), sA, sA, sA)
      : "v16", "v17", "v18", "v19", "v20", "v21", "v22", "v23", "v24", "v25", "v26", "v27", "v28", "v29", "v30", "v31", "v32", "v33", "v34", "v35", "v36", "v37", "v38", "v39", "v40", "v41", "v42", "v43", "v44", "v45", "v46", "v47", "v48", "v49", "v50", "v51", "v52", "v53", "v54", "v55", "v56", "v57", "v58", "v59", "v60", "v61", "v62", "v63", "v64", "v65", "v66", "v67", "v68", "v69", "v70", "v71", "v72", "v73", "v74", "v75", "v76", "v77", "v78", "v79", "v80", "v81", "v82", "v83", "v84", "v85", "v86", "v87", "v88", "v89", "v90", "v91", "v92", "v93", "v94", "v95", "v96", "v97", "v98", "v99", "v100", "v101", "v102", "v103", "v104", "v105", "v106", "v107", "v108", "v109", "v110", "v111", "v112", "v113", "v114", "v115", "v116", "v117", "v118", "v119", "v120", "v121", "v122", "v123", "v124", "v125", "v126", "v127", "v128", "v129", "v130", "v131", "v132", "v133", "v134", "v135", "v136", "v137", "v138", "v139", "v140", "v141", "v142", "v143", "v144", "v145", "v146", "v147", "v148", "v149", "v150", "v151", "v152", "v153", "v154", "v155", "v156", "v157", "v158", "v159", "v160", "v161", "v162", "v163", "v164", "v165", "v166", "v167", "v168", "v169", "v170", "v171", "v172", "v173", "v174", "v175", "v176", "v177", "v178", "v179", "v180", "v181", "v182", "v183", "v184", "v185", "v186", "v187", "v188", "v189", "v190", "v191", "v192", "v193", "v194", "v195", "v196", "v197", "v198", "v199", "v200", "v201", "v202", "v203", "v204", "v205", "v206", "v207", "v208", "v209", "v210", "v211", "v212", "v213", "v214", "v215", "v216", "v217", "v218", "v219", "v220", "v221", "v222", "v223", "v224", "v225", "v226", "v227", "v228", "v229", "v230", "v231", "v232", "v233", "v234", "v235", "v236", "v237", "v238", "v239", "v240", "v241", "v242", "v243", "v244", "v245", "v246", "v247", "v248", "v249", "v250", "v251", "v252", "v253", "v254", "v255", "a0", "a1", "a2", "a3", "a4", "a5", "a6", "a7", "a8", "a9", "a10", "a11", "a12", "a13", "a14", "a15", "a16", "a17", "a18", "a19", "a20", "a21", "a22", "a23", "a24", "a25", "a26", "a27", "a28", "a29", "a30", "a31", "a32", "a33", "a34", "a35", "a36", "a37", "a38", "a39", "a40", "a41", "a42", "a43", "a44", "a45", "a46", "a47", "a48", "a49", "a50", "a51", "a52", "a53", "a54", "a55", "a56", "a57", "a58", "a59", "a60", "a61", "a62", "a63", "a64", "a65", "a66", "a67", "a68", "a69", "a70", "a71", "a72", "a73", "a74", "a75", "a76", "a77", "a78", "a79", "a80", "a81", "a82", "a83", "a84", "a85", "a86", "a87", "a88", "a89", "a90", "a91", "a92", "a93", "a94", "a95", "a96", "a97", "a98", "a99", "a100", "a101", "a102", "a103", "a104", "a105", "a106", "a107", "a108", "a109", "a110", "a111", "a112", "a113", "a114", "a115", "a116", "a117", "a118", "a119", "a120", "a121", "a122", "a123", "a124", "a125", "a126", "a127", "a128", "a129", "a130", "a131", "a132", "a133", "a134", "a135", "a136", "a137", "a138", "a139", "a140", "a141", "a142", "a143", "a144", "a145", "a146", "a147", "a148", "a149", "a150", "a151", "a152", "a153", "a154", "a155", "a156", "a157", "a158", "a159", "a160", "a161", "a162", "a163", "a164", "a165", "a166", "a167", "a168", "a169", "a170", "a171", "a172", "a173", "a174", "a175", "a176", "a177", "a178", "a179", "a180", "a181", "a182", "a183", "a184", "a185", "a186", "a187", "a188", "a189", "a190", "a191", "a192", "a193", "a194", "a195", "a196", "a197", "a198", "a199", "a200", "a201", "a202", "a203", "a204", "a205", "a206", "a207", "a208", "a209", "a210", "a211", "a212", "a213", "a214", "a215", "a216", "a217", "a218", "a219", "a220", "a221", "a222", "a223", "a224", "a225", "a226", "a227", "a228", "a229", "a230", "a231", "a232", "a233", "a234", "a235", "a236", "a237", "a238", "a239", "a240", "a241", "a242", "a243", "a244", "a245", "a246", "a247", "a248", "a249", "a250", "a251", "a252", "a253", "a254", "a255", "s28", "s29", "s30", "s31", "s33", "s34", "s35", "s36", "s37", "s38", "s39", "s40", "s41", "s42", "s43", "s44", "s45", "s46", "s47", "s50", "s51", "s52", "s53", "s54", "s55", "s56", "s57", "s58", "s59", "s60", "s61", "s62", "s63", "vcc", "memory");
  __builtin_unreachable();
}

__device__ __forceinline__ void role_s(const Params& p, int kvh, int j, unsigned tag, int w) {
  const int lane = lane_id(), n = lane & 15, g = lane >> 4;
  const unsigned vK = (n & 7) * 256 + (4 * (n >> 3) + g) * 16;
  const unsigned vV = g * 1024 + n * 16;
  const unsigned vVL0 = g * 256 + ((n ^ (g << 1)) * 16), vVL1 = g * 256 + ((n ^ ((4 + g) << 1)) * 16);
  const unsigned vQ = (g * 8 + (n & 7)) * 16;
  const unsigned head = kvh * 4 + w;
  const unsigned pub1 = lane != 1 ? WS_PO + (head * CPH + j) * 512 + (lane >> 1) * 16 : WS_PML + (head * 32 + j) * 16;
  const unsigned inc = lane != 1 ? 32 * CPH * 512 : 32 * 32 * 16;
  const unsigned pub2 = pub1 + 9 * inc;
  const unsigned ldsw = w * 36864;
  const unsigned vWA = ldsw + lane * 4, vWB = 147456 + 6144 + w * 2176 + lane * 4, vWML = ldsw + n * 8;
  const unsigned vRD = w * 256 + lane * 4, vRDML = 2048 + w * 8, vRDB = 1024 + w * 256 + ((lane & 3) * 16 + (lane >> 3)) * 4;
  const unsigned vSEL = ((lane >> 2) & 1) ? 0x03020c0cu : 0x01000c0cu;
  unsigned tr[8];
  { const int q = n >> 2, pp = n & 3, row = 4 * g + q, r7 = row & 7;
#pragma unroll
    for (int db = 0; db < 8; ++db) tr[db] = ldsw + row * 256 + ((db ^ r7) * 32) + (pp >> 1) * 16 + (pp & 1) * 8; }
  const unsigned vBias = (20 * j + w + 12 < 512) ? 0u : 0xff800000u;
  const unsigned vSt = w == 0 ? 0u : 0x7ffffff0u;
  const unsigned toff0 = (unsigned)(20 * j + w) * 4096;
  const unsigned long long kb = (unsigned long long)p.kc + (unsigned long long)kvh * (SK * DH * 2), vb = (unsigned long long)p.vc + (unsigned long long)kvh * (SK * DH * 2), wb = (unsigned long long)p.ws;
  const u32x4 sK = uni4(u32x4{(unsigned)kb, (unsigned)(kb >> 32) & 0xffffu, SK * DH * 2, 0x00020000u});
  const u32x4 sV = uni4(u32x4{(unsigned)vb, (unsigned)(vb >> 32) & 0xffffu, SK * DH * 2, 0x00020000u});
  const u32x4 sWS = uni4(u32x4{(unsigned)wb, (unsigned)(wb >> 32) & 0xffffu, WS_END, 0x00020000u});
  const u32x4 s28 = uni4(u32x4{toff0, toff0 + 16384, toff0 + 32768, toff0 + 49152});
  const unsigned b3 = (20 * j + w + 12 < 512) ? 0u : 0xff800000u, b4 = (20 * j + w + 16 < 512) ? 0u : 0xff800000u;
  const int xc = (blockIdx.x & 7), u = j * 4 + w, ch = u >> 4, wv = u & 15;
  unsigned long long pb = (unsigned long long)p.wq; unsigned pn = 0, pstep = 1024, pv = lane * 16;
  if (XMAP == 0 && ch < 6) {
    const int ci = xc + 8 * ch;
    const int sw = 2 * ((xc + 4) & 7) + (ch & 1);
    if (ci < 16) { pb = (unsigned long long)p.wq + sw * 512 + (unsigned long long)wv * 32 * 8192; pn = 32 * 8192 - 0; pstep = 2 * 8192; pv = (lane >> 5) * 8192 + (lane & 31) * 16; if (ci == 15 && false) pn = 0; pn = 31 * 8192 + 512; }
    else if (ci < 32) { pb = (unsigned long long)p.wo + (unsigned long long)sw * 262144 + wv * 16384; pn = 16384; }
    else if (ci < 46) {
      if (wv < 8) { pb = (unsigned long long)p.w1 + (ci - 32) * 256 + (unsigned long long)wv * 64 * 3584; pn = 63 * 3584 + 256; pstep = 4 * 3584; pv = (lane >> 4) * 3584 + (lane & 15) * 16; }
      else { pb = (unsigned long long)p.w2 + (unsigned long long)(ci - 32) * 131072 + (wv - 8) * 16384; pn = 16384; }
    }
  }
  const u32x4 sPF = uni4(u32x4{(unsigned)pb, (unsigned)(pb >> 32) & 0xffffu, pn, 0x00020000u});
  const u32x4 s32 = uni4(u32x4{toff0 + 65536, pstep, 0u, b3});
  const u32x4 s36 = uni4(u32x4{b4, ldsw, 0u, 0u});
  const u32x4 s40 = uni4(u32x4{0u, 0u, WS_Q + (unsigned)kvh * 2048, WS_Q + (unsigned)(72 + kvh) * 2048});
  const u32x2 s12 = u32x2{(unsigned)__builtin_amdgcn_readfirstlane(tag), (unsigned)w};
  const u32x2 s48 = u32x2{(unsigned)__builtin_amdgcn_readfirstlane(WS_STAMP + tag * 512), 0u};
  asm volatile(R"ASM(
s_nop 4
v_mov_b32 v27, 0
buffer_load_dword v26, v27, s[24:27], 0 offen sc1
s_mov_b32 s34, 0x3e0293ee
s_mov_b32 s40, 0x5040100
s_mov_b32 s41, 0x7060302
s_mov_b32 s46, 0
buffer_load_dwordx4 v[96:99], v61, s[56:59], s46 offen 
s_add_u32 s46, s46, s33
buffer_load_dwordx4 v[96:99], v61, s[56:59], s46 offen 
s_add_u32 s46, s46, s33
buffer_load_dwordx4 v[96:99], v61, s[56:59], s46 offen 
s_add_u32 s46, s46, s33
buffer_load_dwordx4 v[96:99], v61, s[56:59], s46 offen 
s_add_u32 s46, s46, s33
buffer_load_dwordx4 v[96:99], v61, s[56:59], s46 offen 
s_add_u32 s46, s46, s33
buffer_load_dwordx4 v[96:99], v61, s[56:59], s46 offen 
s_add_u32 s46, s46, s33
buffer_load_dwordx4 v[96:99], v61, s[56:59], s46 offen 
s_add_u32 s46, s46, s33
buffer_load_dwordx4 v[96:99], v61, s[56:59], s46 offen 
s_add_u32 s46, s46, s33
buffer_load_dwordx4 v[96:99], v61, s[56:59], s46 offen 
s_add_u32 s46, s46, s33
buffer_load_dwordx4 v[96:99], v61, s[56:59], s46 offen 
s_add_u32 s46, s46, s33
buffer_load_dwordx4 v[96:99], v61, s[56:59], s46 offen 
s_add_u32 s46, s46, s33
buffer_load_dwordx4 v[96:99], v61, s[56:59], s46 offen 
s_add_u32 s46, s46, s33
buffer_load_dwordx4 v[96:99], v61, s[56:59], s46 offen 
s_add_u32 s46, s46, s33
buffer_load_dwordx4 v[96:99], v61, s[56:59], s46 offen 
s_add_u32 s46, s46, s33
buffer_load_dwordx4 v[96:99], v61, s[56:59], s46 offen 
s_add_u32 s46, s46, s33
buffer_load_dwordx4 v[96:99], v61, s[56:59], s46 offen 
buffer_load_dwordx4 a[0:3], v1, s[16:19], s28 offen nt
buffer_load_dwordx4 a[4:7], v1, s[16:19], s28 offen offset:128 nt
buffer_load_dwordx4 a[8:11], v1, s[16:19], s28 offen offset:2048 nt
buffer_load_dwordx4 a[12:15], v1, s[16:19], s28 offen offset:2176 nt
buffer_load_dwordx4 a[16:19], v1, s[16:19], s29 offen nt
buffer_load_dwordx4 a[20:23], v1, s[16:19], s29 offen offset:128 nt
buffer_load_dwordx4 a[24:27], v1, s[16:19], s29 offen offset:2048 nt
buffer_load_dwordx4 a[28:31], v1, s[16:19], s29 offen offset:2176 nt
buffer_load_dwordx4 a[32:35], v1, s[16:19], s30 offen nt
buffer_load_dwordx4 a[36:39], v1, s[16:19], s30 offen offset:128 nt
buffer_load_dwordx4 a[40:43], v1, s[16:19], s30 offen offset:2048 nt
buffer_load_dwordx4 a[44:47], v1, s[16:19], s30 offen offset:2176 nt
s_mov_b32 s56, 2
s_mov_b32 s57, 0
s_mul_i32 s35, s13, 0xffff7880
s_add_u32 s35, s35, 0x25800
v_mov_b32 v56, v62
v_mov_b32 v57, v62
v_mov_b32 v58, v62
v_mov_b32 v59, v62
v_mov_b32 v62, v60
v_add_u32 v60, 0x24000, v5
v_lshlrev_b32 v52, 4, v0
v_mov_b32 v0, 0x7fff0000
s_getpc_b64 s[64:65]
Lcode0_%=:
s_add_u32 s68, s64, Lit2_%=-Lcode0_%=
s_addc_u32 s69, s65, 0
s_and_b32 s69, s69, 0xffff
s_mov_b32 s70, Lit3_%=-Lit2_%=
s_mov_b32 s71, 0x20000
buffer_load_dword v61, v62, s[68:71], 0 offen
s_add_u32 s68, s64, Lit3_%=-Lcode0_%=
s_addc_u32 s69, s65, 0
s_and_b32 s69, s69, 0xffff
s_mov_b32 s70, Lit4_%=-Lit3_%=
s_mov_b32 s71, 0x20000
buffer_load_dword v61, v62, s[68:71], 0 offen
v_mov_b32 v80, 0
v_mov_b32 v81, 0
v_mov_b32 v82, 0
v_mov_b32 v83, 0
v_mov_b32 v84, 0
v_mov_b32 v85, 0
v_mov_b32 v86, 0
v_mov_b32 v87, 0
v_mov_b32 v88, 0
v_mov_b32 v89, 0
v_mov_b32 v90, 0
v_mov_b32 v91, 0
v_mov_b32 v92, 0
v_mov_b32 v93, 0
v_mov_b32 v94, 0
v_mov_b32 v95, 0
v_mov_b32 v44, 0xff800000
v_mov_b32 v45, 0xff800000
v_mov_b32 v46, 0xff800000
v_mov_b32 v47, 0xff800000
s_waitcnt vmcnt(10)
v_readfirstlane_b32 s12, v26
s_nop 3
s_add_u32 s12, s12, 1
buffer_load_dwordx4 a[48:51], v1, s[16:19], s31 offen nt
buffer_load_dwordx4 a[52:55], v1, s[16:19], s31 offen offset:128 nt
buffer_load_dwordx4 a[56:59], v1, s[16:19], s31 offen offset:2048 nt
buffer_load_dwordx4 a[60:63], v1, s[16:19], s31 offen offset:2176 nt
s_waitcnt vmcnt(10)
buffer_load_dwordx4 a[64:67], v1, s[16:19], s32 offen nt
buffer_load_dwordx4 a[68:71], v1, s[16:19], s32 offen offset:128 nt
buffer_load_dwordx4 a[72:75], v1, s[16:19], s32 offen offset:2048 nt
buffer_load_dwordx4 a[76:79], v1, s[16:19], s32 offen offset:2176 nt
s_waitcnt vmcnt(10)
s_add_u32 s38, s37, 0
s_mov_b32 m0, s38
s_nop 0
buffer_load_dwordx4 v3, s[20:23], s28 offen  lds
buffer_load_dwordx4 v4, s[20:23], s28 offen offset:1024  lds
buffer_load_dwordx4 v3, s[20:23], s28 offen offset:2048  lds
buffer_load_dwordx4 v4, s[20:23], s28 offen offset:3072  lds
s_waitcnt vmcnt(8)
s_add_u32 s38, s37, 0x1000
s_mov_b32 m0, s38
s_nop 0
buffer_load_dwordx4 v3, s[20:23], s29 offen  lds
buffer_load_dwordx4 v4, s[20:23], s29 offen offset:1024  lds
buffer_load_dwordx4 v3, s[20:23], s29 offen offset:2048  lds
buffer_load_dwordx4 v4, s[20:23], s29 offen offset:3072  lds
s_waitcnt vmcnt(8)
s_add_u32 s38, s37, 0x2000
s_mov_b32 m0, s38
s_nop 0
buffer_load_dwordx4 v3, s[20:23], s30 offen  lds
buffer_load_dwordx4 v4, s[20:23], s30 offen offset:1024  lds
buffer_load_dwordx4 v3, s[20:23], s30 offen offset:2048  lds
buffer_load_dwordx4 v4, s[20:23], s30 offen offset:3072  lds
s_waitcnt vmcnt(8)
buffer_load_dwordx4 v[160:163], v2, s[20:23], s31 offen 
buffer_load_dwordx4 v[164:167], v2, s[20:23], s31 offen offset:256 
buffer_load_dwordx4 v[168:171], v2, s[20:23], s31 offen offset:512 
buffer_load_dwordx4 v[172:175], v2, s[20:23], s31 offen offset:768 
s_waitcnt vmcnt(8)
buffer_load_dwordx4 v[176:179], v2, s[20:23], s32 offen 
buffer_load_dwordx4 v[180:183], v2, s[20:23], s32 offen offset:256 
buffer_load_dwordx4 v[184:187], v2, s[20:23], s32 offen offset:512 
buffer_load_dwordx4 v[188:191], v2, s[20:23], s32 offen offset:768 
s_waitcnt vmcnt(8)
s_add_u32 s16, s16, 0x1000000
s_addc_u32 s17, s17, 0
buffer_load_dwordx4 a[80:83], v1, s[16:19], s28 offen nt
buffer_load_dwordx4 a[84:87], v1, s[16:19], s28 offen offset:128 nt
buffer_load_dwordx4 a[88:91], v1, s[16:19], s28 offen offset:2048 nt
buffer_load_dwordx4 a[92:95], v1, s[16:19], s28 offen offset:2176 nt
s_waitcnt vmcnt(8)
buffer_load_dwordx4 a[96:99], v1, s[16:19], s29 offen nt
buffer_load_dwordx4 a[100:103], v1, s[16:19], s29 offen offset:128 nt
buffer_load_dwordx4 a[104:107], v1, s[16:19], s29 offen offset:2048 nt
buffer_load_dwordx4 a[108:111], v1, s[16:19], s29 offen offset:2176 nt
s_waitcnt vmcnt(8)
buffer_load_dwordx4 a[112:115], v1, s[16:19], s30 offen nt
buffer_load_dwordx4 a[116:119], v1, s[16:19], s30 offen offset:128 nt
buffer_load_dwordx4 a[120:123], v1, s[16:19], s30 offen offset:2048 nt
buffer_load_dwordx4 a[124:127], v1, s[16:19], s30 offen offset:2176 nt
s_waitcnt vmcnt(8)
buffer_load_dwordx4 a[128:131], v1, s[16:19], s31 offen nt
buffer_load_dwordx4 a[132:135], v1, s[16:19], s31 offen offset:128 nt
buffer_load_dwordx4 a[136:139], v1, s[16:19], s31 offen offset:2048 nt
buffer_load_dwordx4 a[140:143], v1, s[16:19], s31 offen offset:2176 nt
s_waitcnt vmcnt(8)
buffer_load_dwordx4 a[144:147], v1, s[16:19], s32 offen nt
buffer_load_dwordx4 a[148:151], v1, s[16:19], s32 offen offset:128 nt
buffer_load_dwordx4 a[152:155], v1, s[16:19], s32 offen offset:2048 nt
buffer_load_dwordx4 a[156:159], v1, s[16:19], s32 offen offset:2176 nt
s_waitcnt vmcnt(8)
s_add_u32 s20, s20, 0x1000000
s_addc_u32 s21, s21, 0
s_add_u32 s38, s37, 0x3000
s_mov_b32 m0, s38
s_nop 0
buffer_load_dwordx4 v3, s[20:23], s28 offen  lds
buffer_load_dwordx4 v4, s[20:23], s28 offen offset:1024  lds
buffer_load_dwordx4 v3, s[20:23], s28 offen offset:2048  lds
buffer_load_dwordx4 v4, s[20:23], s28 offen offset:3072  lds
s_waitcnt vmcnt(8)
s_add_u32 s38, s37, 0x4000
s_mov_b32 m0, s38
s_nop 0
buffer_load_dwordx4 v3, s[20:23], s29 offen  lds
buffer_load_dwordx4 v4, s[20:23], s29 offen offset:1024  lds
buffer_load_dwordx4 v3, s[20:23], s29 offen offset:2048  lds
buffer_load_dwordx4 v4, s[20:23], s29 offen offset:3072  lds
s_waitcnt vmcnt(8)
s_add_u32 s38, s37, 0x5000
s_mov_b32 m0, s38
s_nop 0
buffer_load_dwordx4 v3, s[20:23], s30 offen  lds
buffer_load_dwordx4 v4, s[20:23], s30 offen offset:1024  lds
buffer_load_dwordx4 v3, s[20:23], s30 offen offset:2048  lds
buffer_load_dwordx4 v4, s[20:23], s30 offen offset:3072  lds
s_waitcnt vmcnt(8)
buffer_load_dwordx4 v[192:195], v2, s[20:23], s31 offen 
buffer_load_dwordx4 v[196:199], v2, s[20:23], s31 offen offset:256 
buffer_load_dwordx4 v[200:203], v2, s[20:23], s31 offen offset:512 
buffer_load_dwordx4 v[204:207], v2, s[20:23], s31 offen offset:768 
s_waitcnt vmcnt(8)
buffer_load_dwordx4 v[208:211], v2, s[20:23], s32 offen 
buffer_load_dwordx4 v[212:215], v2, s[20:23], s32 offen offset:256 
buffer_load_dwordx4 v[216:219], v2, s[20:23], s32 offen offset:512 
buffer_load_dwordx4 v[220:223], v2, s[20:23], s32 offen offset:768 
s_waitcnt vmcnt(8)
s_add_u32 s16, s16, 0x1000000
s_addc_u32 s17, s17, 0
buffer_load_dwordx4 a[160:163], v1, s[16:19], s28 offen nt
buffer_load_dwordx4 a[164:167], v1, s[16:19], s28 offen offset:128 nt
buffer_load_dwordx4 a[168:171], v1, s[16:19], s28 offen offset:2048 nt
buffer_load_dwordx4 a[172:175], v1, s[16:19], s28 offen offset:2176 nt
s_waitcnt vmcnt(8)
s_add_u32 s49, s42, 0x4000
buffer_load_dwordx4 v[112:115], v5, s[24:27], s49 offen 
buffer_load_dwordx4 v[116:119], v5, s[24:27], s49 offen offset:512 
buffer_load_dwordx4 v[120:123], v5, s[24:27], s49 offen offset:1024 
buffer_load_dwordx4 v[124:127], v5, s[24:27], s49 offen offset:1536 
buffer_load_dwordx4 v[96:99], v5, s[24:27], s42 offen 
buffer_load_dwordx4 v[100:103], v5, s[24:27], s42 offen offset:512 
buffer_load_dwordx4 v[104:107], v5, s[24:27], s42 offen offset:1024 
buffer_load_dwordx4 v[108:111], v5, s[24:27], s42 offen offset:1536 
buffer_load_dwordx4 a[176:179], v1, s[16:19], s29 offen nt
buffer_load_dwordx4 a[180:183], v1, s[16:19], s29 offen offset:128 nt
buffer_load_dwordx4 a[184:187], v1, s[16:19], s29 offen offset:2048 nt
buffer_load_dwordx4 a[188:191], v1, s[16:19], s29 offen offset:2176 nt
s_waitcnt vmcnt(16)
buffer_load_dwordx4 a[192:195], v1, s[16:19], s30 offen nt
buffer_load_dwordx4 a[196:199], v1, s[16:19], s30 offen offset:128 nt
buffer_load_dwordx4 a[200:203], v1, s[16:19], s30 offen offset:2048 nt
buffer_load_dwordx4 a[204:207], v1, s[16:19], s30 offen offset:2176 nt
s_waitcnt vmcnt(16)
buffer_load_dwordx4 a[208:211], v1, s[16:19], s31 offen nt
buffer_load_dwordx4 a[212:215], v1, s[16:19], s31 offen offset:128 nt
buffer_load_dwordx4 a[216:219], v1, s[16:19], s31 offen offset:2048 nt
buffer_load_dwordx4 a[220:223], v1, s[16:19], s31 offen offset:2176 nt
Lit2_%=:
buffer_load_dwordx4 a[224:227], v1, s[16:19], s32 offen nt
buffer_load_dwordx4 a[228:231], v1, s[16:19], s32 offen offset:128 nt
buffer_load_dwordx4 a[232:235], v1, s[16:19], s32 offen offset:2048 nt
buffer_load_dwordx4 a[236:239], v1, s[16:19], s32 offen offset:2176 nt
s_add_u32 s20, s20, 0x1000000
s_addc_u32 s21, s21, 0
s_add_u32 s38, s37, 0x6000
s_mov_b32 m0, s38
s_nop 0
buffer_load_dwordx4 v3, s[20:23], s28 offen  lds
buffer_load_dwordx4 v4, s[20:23], s28 offen offset:1024  lds
buffer_load_dwordx4 v3, s[20:23], s28 offen offset:2048  lds
buffer_load_dwordx4 v4, s[20:23], s28 offen offset:3072  lds
s_waitcnt vmcnt(24)
s_mov_b32 s39, 0
Lchk_1_%=:
v_bitop3_b32 v26, v113, v115, s12 bitop3:0x7e
v_bitop3_b32 v27, v117, v119, s12 bitop3:0x7e
v_bitop3_b32 v28, v121, v123, s12 bitop3:0x7e
v_bitop3_b32 v29, v125, v127, s12 bitop3:0x7e
v_or3_b32 v26, v26, v27, v28
v_or_b32 v26, v26, v29
v_cmp_ne_u32 vcc, 0, v26
v_mov_b32 v32, v112
v_mov_b32 v33, v114
v_mov_b32 v34, v116
v_mov_b32 v35, v118
v_mov_b32 v36, v120
v_mov_b32 v37, v122
v_mov_b32 v38, v124
v_mov_b32 v39, v126
s_cbranch_vccnz Lslow_2_%=
Lcont_3_%=:
s_waitcnt vmcnt(20)
s_mov_b32 s39, 0
Lchk_5_%=:
v_bitop3_b32 v26, v97, v99, s12 bitop3:0x7e
v_bitop3_b32 v27, v101, v103, s12 bitop3:0x7e
v_bitop3_b32 v28, v105, v107, s12 bitop3:0x7e
v_bitop3_b32 v29, v109, v111, s12 bitop3:0x7e
v_or3_b32 v26, v26, v27, v28
v_or_b32 v26, v26, v29
v_cmp_ne_u32 vcc, 0, v26
v_mov_b32 v144, v96
v_mov_b32 v145, v98
v_mov_b32 v146, v100
v_mov_b32 v147, v102
v_mov_b32 v148, v104
v_mov_b32 v149, v106
v_mov_b32 v150, v108
v_mov_b32 v151, v110
s_cbranch_vccnz Lslow_6_%=
Lcont_7_%=:
s_add_u32 s42, s42, 0x8000
v_add_u32 v6, v6, v8
v_mfma_f32_16x16x32_bf16 v[128:131], a[80:83], v[32:35], 0
v_mfma_f32_16x16x32_bf16 v[132:135], a[88:91], v[32:35], 0
v_mfma_f32_16x16x32_bf16 v[136:139], a[96:99], v[32:35], 0
v_mfma_f32_16x16x32_bf16 v[140:143], a[104:107], v[32:35], 0
v_mfma_f32_16x16x32_bf16 v[128:131], a[84:87], v[36:39], v[128:131]
v_mfma_f32_16x16x32_bf16 v[132:135], a[92:95], v[36:39], v[132:135]
v_mfma_f32_16x16x32_bf16 v[136:139], a[100:103], v[36:39], v[136:139]
v_mfma_f32_16x16x32_bf16 v[140:143], a[108:111], v[36:39], v[140:143]
v_mfma_f32_16x16x32_bf16 v[96:99], a[112:115], v[32:35], 0
s_nop 4
v_permlane32_swap_b32 v128, v132
v_permlane32_swap_b32 v129, v133
v_permlane32_swap_b32 v130, v134
v_permlane32_swap_b32 v131, v135
v_mfma_f32_16x16x32_bf16 v[100:103], a[120:123], v[32:35], 0
v_add_f32_dpp v40, v132, v128 row_shl:4 row_mask:0xf bank_mask:0x1
v_add_f32_dpp v41, v133, v129 row_shl:4 row_mask:0xf bank_mask:0x1
v_add_f32_dpp v42, v134, v130 row_shl:4 row_mask:0xf bank_mask:0x1
v_add_f32_dpp v43, v135, v131 row_shl:4 row_mask:0xf bank_mask:0x1
v_permlane32_swap_b32 v136, v140
v_permlane32_swap_b32 v137, v141
v_permlane32_swap_b32 v138, v142
v_permlane32_swap_b32 v139, v143
v_mfma_f32_16x16x32_bf16 v[128:131], a[128:131], v[32:35], v[56:59]
v_add_f32_dpp v40, v136, v140 row_shr:4 row_mask:0xf bank_mask:0x2
v_add_f32_dpp v41, v137, v141 row_shr:4 row_mask:0xf bank_mask:0x2
v_mfma_f32_16x16x32_bf16 v[132:135], a[136:139], v[32:35], v[56:59]
v_add_f32_dpp v42, v138, v142 row_shr:4 row_mask:0xf bank_mask:0x2
v_add_f32_dpp v43, v139, v143 row_shr:4 row_mask:0xf bank_mask:0x2
v_mfma_f32_16x16x32_bf16 v[96:99], a[116:119], v[36:39], v[96:99]
v_mfma_f32_16x16x32_bf16 v[100:103], a[124:127], v[36:39], v[100:103]
v_mfma_f32_16x16x32_bf16 v[136:139], a[144:147], v[32:35], v[56:59]
v_mfma_f32_16x16x32_bf16 v[140:143], a[152:155], v[32:35], v[56:59]
v_mfma_f32_16x16x32_bf16 v[128:131], a[132:135], v[36:39], v[128:131]
s_nop 4
v_permlane32_swap_b32 v96, v100
v_permlane32_swap_b32 v97, v101
v_mfma_f32_16x16x32_bf16 v[132:135], a[140:143], v[36:39], v[132:135]
v_permlane32_swap_b32 v98, v102
v_permlane32_swap_b32 v99, v103
v_mfma_f32_16x16x32_bf16 v[136:139], a[148:151], v[36:39], v[136:139]
v_add_f32_dpp v40, v100, v96 row_shl:4 row_mask:0xf bank_mask:0x4
v_add_f32_dpp v41, v101, v97 row_shl:4 row_mask:0xf bank_mask:0x4
v_mfma_f32_16x16x32_bf16 v[140:143], a[156:159], v[36:39], v[140:143]
v_add_f32_dpp v42, v102, v98 row_shl:4 row_mask:0xf bank_mask:0x4
v_add_f32_dpp v43, v103, v99 row_shl:4 row_mask:0xf bank_mask:0x4
ds_read_b64_tr_b16 v[96:97], v14 offset:12288
v_permlane32_swap_b32 v128, v132
v_permlane32_swap_b32 v129, v133
v_permlane32_swap_b32 v130, v134
v_permlane32_swap_b32 v131, v135
v_add_f32_dpp v40, v128, v132 row_shr:4 row_mask:0xf bank_mask:0x8
v_add_f32_dpp v41, v129, v133 row_shr:4 row_mask:0xf bank_mask:0x8
v_add_f32_dpp v42, v130, v134 row_shr:4 row_mask:0xf bank_mask:0x8
v_add_f32_dpp v43, v131, v135 row_shr:4 row_mask:0xf bank_mask:0x8
v_permlane32_swap_b32 v136, v140
v_permlane32_swap_b32 v137, v141
v_permlane32_swap_b32 v138, v142
v_permlane32_swap_b32 v139, v143
v_add_f32_dpp v44, v140, v136 row_shl:4 row_mask:0xf bank_mask:0x1
v_add_f32_dpp v45, v141, v137 row_shl:4 row_mask:0xf bank_mask:0x1
v_add_f32_dpp v46, v142, v138 row_shl:4 row_mask:0xf bank_mask:0x1
v_add_f32_dpp v47, v143, v139 row_shl:4 row_mask:0xf bank_mask:0x1
v_max3_f32 v26, v40, v41, v42
v_max3_f32 v27, v43, v44, v45
v_max3_f32 v26, v26, v46, v47
v_max_f32 v26, v26, v27
ds_read_b64_tr_b16 v[98:99], v14 offset:16384
s_nop 0
v_max_f32_dpp v26, v26, v26 row_ror:4 row_mask:0xf bank_mask:0xf bound_ctrl:1
ds_read_b64_tr_b16 v[100:101], v15 offset:12288
s_nop 0
v_max_f32_dpp v26, v26, v26 row_ror:8 row_mask:0xf bank_mask:0xf bound_ctrl:1
ds_read_b64_tr_b16 v[102:103], v15 offset:16384
v_mov_b32 v27, v26
ds_read_b64_tr_b16 v[104:105], v16 offset:12288
ds_read_b64_tr_b16 v[106:107], v16 offset:16384
v_permlane16_swap_b32 v26, v27
ds_read_b64_tr_b16 v[108:109], v17 offset:12288
v_max_f32 v26, v26, v27
ds_read_b64_tr_b16 v[110:111], v17 offset:16384
v_mov_b32 v27, v26
ds_read_b64_tr_b16 v[112:113], v18 offset:12288
ds_read_b64_tr_b16 v[114:115], v18 offset:16384
v_permlane32_swap_b32 v26, v27
ds_read_b64_tr_b16 v[116:117], v19 offset:12288
v_max_f32 v26, v26, v27
ds_read_b64_tr_b16 v[118:119], v19 offset:16384
v_mul_f32 v22, s34, v26
ds_read_b64_tr_b16 v[120:121], v20 offset:12288
v_fma_f32 v128, v40, s34, -v22
ds_read_b64_tr_b16 v[122:123], v20 offset:16384
v_fma_f32 v129, v41, s34, -v22
v_fma_f32 v130, v42, s34, -v22
v_fma_f32 v131, v43, s34, -v22
v_fma_f32 v132, v44, s34, -v22
v_fma_f32 v133, v45, s34, -v22
v_fma_f32 v134, v46, s34, -v22
v_fma_f32 v135, v47, s34, -v22
ds_read_b64_tr_b16 v[124:125], v21 offset:12288
v_exp_f32 v128, v128
ds_read_b64_tr_b16 v[126:127], v21 offset:16384
v_exp_f32 v129, v129
v_exp_f32 v130, v130
v_exp_f32 v131, v131
v_exp_f32 v132, v132
v_exp_f32 v133, v133
v_exp_f32 v134, v134
v_exp_f32 v135, v135
v_add_f32 v26, v128, v129
v_add_f32 v27, v130, v131
v_cvt_pk_bf16_f32 v48, v128, v129
v_cvt_pk_bf16_f32 v49, v130, v131
v_add_f32 v28, v132, v133
v_add_f32 v29, v134, v135
v_cvt_pk_bf16_f32 v50, v132, v133
v_cvt_pk_bf16_f32 v51, v134, v135
ds_read_b64_tr_b16 v[128:129], v14 offset:20480
v_mov_b32_dpp v80, v48 quad_perm:[0,1,2,3] row_mask:0xf bank_mask:0x1
ds_read_b64_tr_b16 v[130:131], v15 offset:20480
v_mov_b32_dpp v81, v49 quad_perm:[0,1,2,3] row_mask:0xf bank_mask:0x1
ds_read_b64_tr_b16 v[132:133], v16 offset:20480
v_mov_b32_dpp v82, v48 row_shl:4 row_mask:0xf bank_mask:0x1
ds_read_b64_tr_b16 v[134:135], v17 offset:20480
v_mov_b32_dpp v83, v49 row_shl:4 row_mask:0xf bank_mask:0x1
ds_read_b64_tr_b16 v[136:137], v18 offset:20480
v_mov_b32_dpp v84, v48 row_shr:4 row_mask:0xf bank_mask:0x2
ds_read_b64_tr_b16 v[138:139], v19 offset:20480
s_waitcnt lgkmcnt(14)
v_mfma_f32_16x16x32_bf16 v[64:67], v[80:83], v[96:99], 0
ds_read_b64_tr_b16 v[140:141], v20 offset:20480
v_mov_b32_dpp v85, v49 row_shr:4 row_mask:0xf bank_mask:0x2
ds_read_b64_tr_b16 v[142:143], v21 offset:20480
s_waitcnt lgkmcnt(14)
v_mfma_f32_16x16x32_bf16 v[68:71], v[80:83], v[112:115], 0
v_mov_b32_dpp v86, v48 quad_perm:[0,1,2,3] row_mask:0xf bank_mask:0x2
v_mov_b32_dpp v87, v49 quad_perm:[0,1,2,3] row_mask:0xf bank_mask:0x2
v_mov_b32_dpp v88, v48 row_shr:8 row_mask:0xf bank_mask:0x4
v_mov_b32_dpp v89, v49 row_shr:8 row_mask:0xf bank_mask:0x4
v_mfma_f32_16x16x32_bf16 v[64:67], v[84:87], v[100:103], v[64:67]
v_mov_b32_dpp v90, v48 row_shr:4 row_mask:0xf bank_mask:0x4
v_mov_b32_dpp v91, v49 row_shr:4 row_mask:0xf bank_mask:0x4
s_waitcnt lgkmcnt(12)
v_mfma_f32_16x16x32_bf16 v[68:71], v[84:87], v[116:119], v[68:71]
v_mov_b32_dpp v92, v48 row_shr:12 row_mask:0xf bank_mask:0x8
v_mov_b32_dpp v93, v49 row_shr:12 row_mask:0xf bank_mask:0x8
v_mfma_f32_16x16x32_bf16 v[64:67], v[88:91], v[104:107], v[64:67]
v_mov_b32_dpp v94, v48 row_shr:8 row_mask:0xf bank_mask:0x8
v_mov_b32_dpp v95, v49 row_shr:8 row_mask:0xf bank_mask:0x8
s_waitcnt lgkmcnt(10)
v_mfma_f32_16x16x32_bf16 v[68:71], v[88:91], v[120:123], v[68:71]
v_mov_b32_dpp v80, v48 row_shl:8 row_mask:0xf bank_mask:0x1
v_mov_b32_dpp v81, v49 row_shl:8 row_mask:0xf bank_mask:0x1
v_mfma_f32_16x16x32_bf16 v[64:67], v[92:95], v[108:111], v[64:67]
v_mov_b32_dpp v82, v50 quad_perm:[0,1,2,3] row_mask:0xf bank_mask:0x1
v_mov_b32_dpp v83, v51 quad_perm:[0,1,2,3] row_mask:0xf bank_mask:0x1
s_waitcnt lgkmcnt(8)
v_mfma_f32_16x16x32_bf16 v[68:71], v[92:95], v[124:127], v[68:71]
v_perm_b32 v96, v196, v192, s40
v_perm_b32 v97, v204, v200, s40
s_waitcnt lgkmcnt(7)
v_mfma_f32_16x16x16_bf16 v[64:67], v[80:81], v[128:129], v[64:67]
v_perm_b32 v98, v212, v208, s40
v_perm_b32 v99, v220, v216, s40
s_waitcnt lgkmcnt(3)
v_mfma_f32_16x16x16_bf16 v[68:71], v[80:81], v[136:137], v[68:71]
v_mov_b32_dpp v80, v48 row_shl:12 row_mask:0xf bank_mask:0x1
v_mov_b32_dpp v81, v49 row_shl:12 row_mask:0xf bank_mask:0x1
v_perm_b32 v112, v198, v194, s40
v_perm_b32 v113, v206, v202, s40
v_perm_b32 v114, v214, v210, s40
v_perm_b32 v115, v222, v218, s40
v_mov_b32_dpp v84, v48 row_shl:4 row_mask:0xf bank_mask:0x2
v_mov_b32_dpp v85, v49 row_shl:4 row_mask:0xf bank_mask:0x2
v_mfma_f32_16x16x32_bf16 v[72:75], v[80:83], v[96:99], 0
v_mov_b32_dpp v86, v50 row_shr:4 row_mask:0xf bank_mask:0x2
v_mov_b32_dpp v87, v51 row_shr:4 row_mask:0xf bank_mask:0x2
v_mfma_f32_16x16x32_bf16 v[76:79], v[80:83], v[112:115], 0
v_perm_b32 v100, v196, v192, s41
v_perm_b32 v101, v204, v200, s41
v_mfma_f32_16x16x16_bf16 v[64:67], v[84:85], v[130:131], v[64:67]
v_perm_b32 v102, v212, v208, s41
v_perm_b32 v103, v220, v216, s41
s_waitcnt lgkmcnt(2)
v_mfma_f32_16x16x16_bf16 v[68:71], v[84:85], v[138:139], v[68:71]
v_mov_b32_dpp v84, v48 row_shl:8 row_mask:0xf bank_mask:0x2
v_mov_b32_dpp v85, v49 row_shl:8 row_mask:0xf bank_mask:0x2
v_perm_b32 v116, v198, v194, s41
v_perm_b32 v117, v206, v202, s41
v_perm_b32 v118, v214, v210, s41
v_perm_b32 v119, v222, v218, s41
v_mov_b32_dpp v88, v48 quad_perm:[0,1,2,3] row_mask:0xf bank_mask:0x4
v_mov_b32_dpp v89, v49 quad_perm:[0,1,2,3] row_mask:0xf bank_mask:0x4
v_mfma_f32_16x16x32_bf16 v[72:75], v[84:87], v[100:103], v[72:75]
v_mov_b32_dpp v90, v50 row_shr:8 row_mask:0xf bank_mask:0x4
v_mov_b32_dpp v91, v51 row_shr:8 row_mask:0xf bank_mask:0x4
v_mfma_f32_16x16x32_bf16 v[76:79], v[84:87], v[116:119], v[76:79]
v_perm_b32 v104, v197, v193, s40
v_perm_b32 v105, v205, v201, s40
v_mfma_f32_16x16x16_bf16 v[64:67], v[88:89], v[132:133], v[64:67]
v_perm_b32 v106, v213, v209, s40
v_perm_b32 v107, v221, v217, s40
s_waitcnt lgkmcnt(1)
v_mfma_f32_16x16x16_bf16 v[68:71], v[88:89], v[140:141], v[68:71]
v_mov_b32_dpp v88, v48 row_shl:4 row_mask:0xf bank_mask:0x4
v_mov_b32_dpp v89, v49 row_shl:4 row_mask:0xf bank_mask:0x4
v_perm_b32 v120, v199, v195, s40
v_perm_b32 v121, v207, v203, s40
v_perm_b32 v122, v215, v211, s40
v_perm_b32 v123, v223, v219, s40
v_mov_b32_dpp v92, v48 row_shr:4 row_mask:0xf bank_mask:0x8
v_mov_b32_dpp v93, v49 row_shr:4 row_mask:0xf bank_mask:0x8
v_add_f32 v26, v26, v27
v_add_f32 v28, v28, v29
v_add_f32 v26, v26, v28
v_mfma_f32_16x16x32_bf16 v[72:75], v[88:91], v[104:107], v[72:75]
v_mov_b32_dpp v94, v50 row_shr:12 row_mask:0xf bank_mask:0x8
v_mov_b32_dpp v95, v51 row_shr:12 row_mask:0xf bank_mask:0x8
v_mfma_f32_16x16x32_bf16 v[76:79], v[88:91], v[120:123], v[76:79]
v_perm_b32 v108, v197, v193, s41
v_perm_b32 v109, v205, v201, s41
v_mfma_f32_16x16x16_bf16 v[64:67], v[92:93], v[134:135], v[64:67]
v_perm_b32 v110, v213, v209, s41
v_perm_b32 v111, v221, v217, s41
s_waitcnt lgkmcnt(0)
v_mfma_f32_16x16x16_bf16 v[68:71], v[92:93], v[142:143], v[68:71]
v_mov_b32_dpp v92, v48 quad_perm:[0,1,2,3] row_mask:0xf bank_mask:0x8
v_mov_b32_dpp v93, v49 quad_perm:[0,1,2,3] row_mask:0xf bank_mask:0x8
v_perm_b32 v124, v199, v195, s41
v_perm_b32 v125, v207, v203, s41
v_perm_b32 v126, v215, v211, s41
v_perm_b32 v127, v223, v219, s41
v_add_f32_dpp v26, v26, v26 row_ror:4 row_mask:0xf bank_mask:0xf bound_ctrl:1
v_mfma_f32_16x16x32_bf16 v[72:75], v[92:95], v[108:111], v[72:75]
s_nop 0
v_add_f32_dpp v26, v26, v26 row_ror:8 row_mask:0xf bank_mask:0xf bound_ctrl:1
v_mov_b32 v27, v26
v_mfma_f32_16x16x32_bf16 v[76:79], v[92:95], v[124:127], v[76:79]
s_nop 0
v_permlane16_swap_b32 v26, v27
v_add_f32 v26, v26, v27
v_mov_b32 v27, v26
v_cvt_pk_bf16_f32 v128, v64, v68
v_cvt_pk_bf16_f32 v129, v65, v69
v_permlane32_swap_b32 v26, v27
v_add_f32 v26, v26, v27
v_mov_b32 v23, v26
v_cvt_pk_bf16_f32 v130, v66, v70
v_cvt_pk_bf16_f32 v131, v67, v71
v_add_u32 v29, s35, v11
v_cvt_pk_bf16_f32 v132, v72, v76
v_cvt_pk_bf16_f32 v133, v73, v77
v_cvt_pk_bf16_f32 v134, v74, v78
v_cvt_pk_bf16_f32 v135, v75, v79
ds_write2st64_b32 v10, v128, v129 offset0:0 offset1:1
ds_write2st64_b32 v10, v132, v133 offset0:4 offset1:5
ds_write2st64_b32 v10, v130, v131 offset0:2 offset1:3
ds_write2st64_b32 v10, v134, v135 offset0:6 offset1:7
ds_write_b64 v29, v[22:23] offset:2048
s_waitcnt lgkmcnt(0)
s_barrier
v_add_u32 v130, 0x25800, v13
ds_read_b64 v[96:97], v130 offset:0
v_add_u32 v128, 0x25800, v12
ds_read_b64 v[98:99], v130 offset:2176
v_add_u32 v129, 0x25800, v24
ds_read_b64 v[100:101], v130 offset:4352
v_mov_b32 v137, s12
ds_read_b64 v[102:103], v130 offset:6528
v_mov_b32 v139, s12
ds_read_b32 v104, v128 offset:0
ds_read2_b32 v[108:109], v129 offset0:0 offset1:8
ds_read_b32 v105, v128 offset:2176
ds_read_b32 v110, v129 offset:2176
ds_read_b32 v111, v129 offset:2208
ds_read_b32 v106, v128 offset:4352
ds_read_b32 v112, v129 offset:4352
ds_read_b32 v113, v129 offset:4384
ds_read_b32 v107, v128 offset:6528
ds_read_b32 v114, v129 offset:6528
ds_read_b32 v115, v129 offset:6560
s_waitcnt lgkmcnt(12)
v_max3_f32 v26, v96, v98, v100
s_waitcnt lgkmcnt(11)
v_max_f32 v26, v26, v102
v_sub_f32 v96, v96, v26
v_sub_f32 v98, v98, v26
v_exp_f32 v96, v96
v_sub_f32 v100, v100, v26
v_exp_f32 v98, v98
s_waitcnt lgkmcnt(10)
v_lshlrev_b32 v116, 16, v104
v_and_b32 v117, 0xffff0000, v104
s_waitcnt lgkmcnt(9)
v_perm_b32 v118, v108, v108, v25
v_perm_b32 v119, v109, v109, v25
v_sub_f32 v102, v102, v26
v_exp_f32 v100, v100
v_pk_add_f32 v[116:117], v[116:117], v[118:119]
s_waitcnt lgkmcnt(8)
v_lshlrev_b32 v120, 16, v105
v_and_b32 v121, 0xffff0000, v105
s_waitcnt lgkmcnt(7)
v_perm_b32 v122, v110, v110, v25
s_waitcnt lgkmcnt(6)
v_perm_b32 v123, v111, v111, v25
v_exp_f32 v102, v102
v_pk_mul_f32 v[134:135], v[116:117], v[96:97] op_sel_hi:[1,0]
v_pk_add_f32 v[120:121], v[120:121], v[122:123]
s_waitcnt lgkmcnt(5)
v_lshlrev_b32 v116, 16, v106
v_and_b32 v117, 0xffff0000, v106
s_waitcnt lgkmcnt(4)
v_perm_b32 v118, v112, v112, v25
s_waitcnt lgkmcnt(3)
v_perm_b32 v119, v113, v113, v25
v_pk_fma_f32 v[134:135], v[120:121], v[98:99], v[134:135] op_sel_hi:[1,0,1]
v_pk_add_f32 v[116:117], v[116:117], v[118:119]
s_waitcnt lgkmcnt(2)
v_lshlrev_b32 v120, 16, v107
v_and_b32 v121, 0xffff0000, v107
s_waitcnt lgkmcnt(1)
v_perm_b32 v122, v114, v114, v25
s_waitcnt lgkmcnt(0)
v_perm_b32 v123, v115, v115, v25
v_pk_fma_f32 v[134:135], v[116:117], v[100:101], v[134:135] op_sel_hi:[1,0,1]
v_pk_add_f32 v[120:121], v[120:121], v[122:123]
v_pk_fma_f32 v[134:135], v[120:121], v[102:103], v[134:135] op_sel_hi:[1,0,1]
v_mul_f32 v27, v96, v97
v_cvt_pk_bf16_f32 v136, v134, v135
v_fma_f32 v27, v98, v99, v27
v_fma_f32 v27, v100, v101, v27
v_fma_f32 v27, v102, v103, v27
v_mov_b32_dpp v138, v136 quad_perm:[1,0,3,2] row_mask:0xf bank_mask:0xf bound_ctrl:1
v_cndmask_b32 v136, v136, v26, s[56:57]
v_cndmask_b32 v138, v138, v27, s[56:57]
s_mov_b32 exec_lo, 0x55555557
s_mov_b32 exec_hi, 0x55555555
buffer_store_dwordx4 v[136:139], v6, s[24:27], 0 offen sc1
s_mov_b64 exec, -1
s_nop 0
v_sub_u32 v6, v6, v8
s_add_u32 s38, s37, 0x7000
s_mov_b32 m0, s38
s_nop 0
buffer_load_dwordx4 v3, s[20:23], s29 offen  lds
buffer_load_dwordx4 v4, s[20:23], s29 offen offset:1024  lds
buffer_load_dwordx4 v3, s[20:23], s29 offen offset:2048  lds
buffer_load_dwordx4 v4, s[20:23], s29 offen offset:3072  lds
s_add_u32 s38, s37, 0x8000
s_mov_b32 m0, s38
s_nop 0
buffer_load_dwordx4 v3, s[20:23], s30 offen  lds
buffer_load_dwordx4 v4, s[20:23], s30 offen offset:1024  lds
buffer_load_dwordx4 v3, s[20:23], s30 offen offset:2048  lds
buffer_load_dwordx4 v4, s[20:23], s30 offen offset:3072  lds
v_mfma_f32_16x16x32_bf16 v[128:131], a[0:3], v[144:147], 0
v_mfma_f32_16x16x32_bf16 v[132:135], a[8:11], v[144:147], 0
v_mfma_f32_16x16x32_bf16 v[136:139], a[16:19], v[144:147], 0
v_mfma_f32_16x16x32_bf16 v[140:143], a[24:27], v[144:147], 0
v_mfma_f32_16x16x32_bf16 v[128:131], a[4:7], v[148:151], v[128:131]
v_mfma_f32_16x16x32_bf16 v[132:135], a[12:15], v[148:151], v[132:135]
v_mfma_f32_16x16x32_bf16 v[136:139], a[20:23], v[148:151], v[136:139]
v_mfma_f32_16x16x32_bf16 v[140:143], a[28:31], v[148:151], v[140:143]
v_mfma_f32_16x16x32_bf16 v[96:99], a[32:35], v[144:147], 0
s_nop 4
v_permlane32_swap_b32 v128, v132
v_permlane32_swap_b32 v129, v133
v_permlane32_swap_b32 v130, v134
v_permlane32_swap_b32 v131, v135
v_mfma_f32_16x16x32_bf16 v[100:103], a[40:43], v[144:147], 0
v_add_f32_dpp v40, v132, v128 row_shl:4 row_mask:0xf bank_mask:0x1
v_add_f32_dpp v41, v133, v129 row_shl:4 row_mask:0xf bank_mask:0x1
v_add_f32_dpp v42, v134, v130 row_shl:4 row_mask:0xf bank_mask:0x1
v_add_f32_dpp v43, v135, v131 row_shl:4 row_mask:0xf bank_mask:0x1
v_permlane32_swap_b32 v136, v140
v_permlane32_swap_b32 v137, v141
v_permlane32_swap_b32 v138, v142
v_permlane32_swap_b32 v139, v143
v_mfma_f32_16x16x32_bf16 v[128:131], a[48:51], v[144:147], v[56:59]
v_add_f32_dpp v40, v136, v140 row_shr:4 row_mask:0xf bank_mask:0x2
v_add_f32_dpp v41, v137, v141 row_shr:4 row_mask:0xf bank_mask:0x2
v_mfma_f32_16x16x32_bf16 v[132:135], a[56:59], v[144:147], v[56:59]
v_add_f32_dpp v42, v138, v142 row_shr:4 row_mask:0xf bank_mask:0x2
v_add_f32_dpp v43, v139, v143 row_shr:4 row_mask:0xf bank_mask:0x2
v_mfma_f32_16x16x32_bf16 v[96:99], a[36:39], v[148:151], v[96:99]
v_mfma_f32_16x16x32_bf16 v[100:103], a[44:47], v[148:151], v[100:103]
v_mfma_f32_16x16x32_bf16 v[136:139], a[64:67], v[144:147], v[56:59]
v_mfma_f32_16x16x32_bf16 v[140:143], a[72:75], v[144:147], v[56:59]
v_mfma_f32_16x16x32_bf16 v[128:131], a[52:55], v[148:151], v[128:131]
s_nop 4
v_permlane32_swap_b32 v96, v100
v_permlane32_swap_b32 v97, v101
v_mfma_f32_16x16x32_bf16 v[132:135], a[60:63], v[148:151], v[132:135]
v_permlane32_swap_b32 v98, v102
v_permlane32_swap_b32 v99, v103
v_mfma_f32_16x16x32_bf16 v[136:139], a[68:71], v[148:151], v[136:139]
v_add_f32_dpp v40, v100, v96 row_shl:4 row_mask:0xf bank_mask:0x4
v_add_f32_dpp v41, v101, v97 row_shl:4 row_mask:0xf bank_mask:0x4
v_mfma_f32_16x16x32_bf16 v[140:143], a[76:79], v[148:151], v[140:143]
v_add_f32_dpp v42, v102, v98 row_shl:4 row_mask:0xf bank_mask:0x4
v_add_f32_dpp v43, v103, v99 row_shl:4 row_mask:0xf bank_mask:0x4
ds_read_b64_tr_b16 v[96:97], v14 offset:0
v_permlane32_swap_b32 v128, v132
v_permlane32_swap_b32 v129, v133
v_permlane32_swap_b32 v130, v134
v_permlane32_swap_b32 v131, v135
v_add_f32_dpp v40, v128, v132 row_shr:4 row_mask:0xf bank_mask:0x8
v_add_f32_dpp v41, v129, v133 row_shr:4 row_mask:0xf bank_mask:0x8
v_add_f32_dpp v42, v130, v134 row_shr:4 row_mask:0xf bank_mask:0x8
v_add_f32_dpp v43, v131, v135 row_shr:4 row_mask:0xf bank_mask:0x8
v_permlane32_swap_b32 v136, v140
v_permlane32_swap_b32 v137, v141
v_permlane32_swap_b32 v138, v142
v_permlane32_swap_b32 v139, v143
v_add_f32_dpp v44, v140, v136 row_shl:4 row_mask:0xf bank_mask:0x1
v_add_f32_dpp v45, v141, v137 row_shl:4 row_mask:0xf bank_mask:0x1
v_add_f32_dpp v46, v142, v138 row_shl:4 row_mask:0xf bank_mask:0x1
v_add_f32_dpp v47, v143, v139 row_shl:4 row_mask:0xf bank_mask:0x1
v_max3_f32 v26, v40, v41, v42
v_max3_f32 v27, v43, v44, v45
v_max3_f32 v26, v26, v46, v47
v_max_f32 v26, v26, v27
ds_read_b64_tr_b16 v[98:99], v14 offset:4096
s_nop 0
v_max_f32_dpp v26, v26, v26 row_ror:4 row_mask:0xf bank_mask:0xf bound_ctrl:1
ds_read_b64_tr_b16 v[100:101], v15 offset:0
s_nop 0
v_max_f32_dpp v26, v26, v26 row_ror:8 row_mask:0xf bank_mask:0xf bound_ctrl:1
ds_read_b64_tr_b16 v[102:103], v15 offset:4096
v_mov_b32 v27, v26
ds_read_b64_tr_b16 v[104:105], v16 offset:0
ds_read_b64_tr_b16 v[106:107], v16 offset:4096
v_permlane16_swap_b32 v26, v27
ds_read_b64_tr_b16 v[108:109], v17 offset:0
v_max_f32 v26, v26, v27
ds_read_b64_tr_b16 v[110:111], v17 offset:4096
v_mov_b32 v27, v26
ds_read_b64_tr_b16 v[112:113], v18 offset:0
ds_read_b64_tr_b16 v[114:115], v18 offset:4096
v_permlane32_swap_b32 v26, v27
ds_read_b64_tr_b16 v[116:117], v19 offset:0
v_max_f32 v26, v26, v27
ds_read_b64_tr_b16 v[118:119], v19 offset:4096
v_mul_f32 v22, s34, v26
ds_read_b64_tr_b16 v[120:121], v20 offset:0
v_fma_f32 v128, v40, s34, -v22
ds_read_b64_tr_b16 v[122:123], v20 offset:4096
v_fma_f32 v129, v41, s34, -v22
v_fma_f32 v130, v42, s34, -v22
v_fma_f32 v131, v43, s34, -v22
v_fma_f32 v132, v44, s34, -v22
v_fma_f32 v133, v45, s34, -v22
v_fma_f32 v134, v46, s34, -v22
v_fma_f32 v135, v47, s34, -v22
ds_read_b64_tr_b16 v[124:125], v21 offset:0
v_exp_f32 v128, v128
ds_read_b64_tr_b16 v[126:127], v21 offset:4096
v_exp_f32 v129, v129
v_exp_f32 v130, v130
v_exp_f32 v131, v131
v_exp_f32 v132, v132
v_exp_f32 v133, v133
v_exp_f32 v134, v134
v_exp_f32 v135, v135
v_add_f32 v26, v128, v129
v_add_f32 v27, v130, v131
v_cvt_pk_bf16_f32 v48, v128, v129
v_cvt_pk_bf16_f32 v49, v130, v131
v_add_f32 v28, v132, v133
v_add_f32 v29, v134, v135
v_cvt_pk_bf16_f32 v50, v132, v133
v_cvt_pk_bf16_f32 v51, v134, v135
ds_read_b64_tr_b16 v[128:129], v14 offset:8192
v_mov_b32_dpp v80, v48 quad_perm:[0,1,2,3] row_mask:0xf bank_mask:0x1
ds_read_b64_tr_b16 v[130:131], v15 offset:8192
v_mov_b32_dpp v81, v49 quad_perm:[0,1,2,3] row_mask:0xf bank_mask:0x1
ds_read_b64_tr_b16 v[132:133], v16 offset:8192
v_mov_b32_dpp v82, v48 row_shl:4 row_mask:0xf bank_mask:0x1
ds_read_b64_tr_b16 v[134:135], v17 offset:8192
v_mov_b32_dpp v83, v49 row_shl:4 row_mask:0xf bank_mask:0x1
ds_read_b64_tr_b16 v[136:137], v18 offset:8192
v_mov_b32_dpp v84, v48 row_shr:4 row_mask:0xf bank_mask:0x2
ds_read_b64_tr_b16 v[138:139], v19 offset:8192
s_waitcnt lgkmcnt(14)
v_mfma_f32_16x16x32_bf16 v[64:67], v[80:83], v[96:99], 0
ds_read_b64_tr_b16 v[140:141], v20 offset:8192
v_mov_b32_dpp v85, v49 row_shr:4 row_mask:0xf bank_mask:0x2
ds_read_b64_tr_b16 v[142:143], v21 offset:8192
s_waitcnt lgkmcnt(14)
v_mfma_f32_16x16x32_bf16 v[68:71], v[80:83], v[112:115], 0
v_mov_b32_dpp v86, v48 quad_perm:[0,1,2,3] row_mask:0xf bank_mask:0x2
v_mov_b32_dpp v87, v49 quad_perm:[0,1,2,3] row_mask:0xf bank_mask:0x2
v_mov_b32_dpp v88, v48 row_shr:8 row_mask:0xf bank_mask:0x4
v_mov_b32_dpp v89, v49 row_shr:8 row_mask:0xf bank_mask:0x4
v_mfma_f32_16x16x32_bf16 v[64:67], v[84:87], v[100:103], v[64:67]
v_mov_b32_dpp v90, v48 row_shr:4 row_mask:0xf bank_mask:0x4
v_mov_b32_dpp v91, v49 row_shr:4 row_mask:0xf bank_mask:0x4
s_waitcnt lgkmcnt(12)
v_mfma_f32_16x16x32_bf16 v[68:71], v[84:87], v[116:119], v[68:71]
v_mov_b32_dpp v92, v48 row_shr:12 row_mask:0xf bank_mask:0x8
v_mov_b32_dpp v93, v49 row_shr:12 row_mask:0xf bank_mask:0x8
v_mfma_f32_16x16x32_bf16 v[64:67], v[88:91], v[104:107], v[64:67]
v_mov_b32_dpp v94, v48 row_shr:8 row_mask:0xf bank_mask:0x8
v_mov_b32_dpp v95, v49 row_shr:8 row_mask:0xf bank_mask:0x8
s_waitcnt lgkmcnt(10)
v_mfma_f32_16x16x32_bf16 v[68:71], v[88:91], v[120:123], v[68:71]
v_mov_b32_dpp v80, v48 row_shl:8 row_mask:0xf bank_mask:0x1
v_mov_b32_dpp v81, v49 row_shl:8 row_mask:0xf bank_mask:0x1
v_mfma_f32_16x16x32_bf16 v[64:67], v[92:95], v[108:111], v[64:67]
v_mov_b32_dpp v82, v50 quad_perm:[0,1,2,3] row_mask:0xf bank_mask:0x1
v_mov_b32_dpp v83, v51 quad_perm:[0,1,2,3] row_mask:0xf bank_mask:0x1
s_waitcnt lgkmcnt(8)
v_mfma_f32_16x16x32_bf16 v[68:71], v[92:95], v[124:127], v[68:71]
v_perm_b32 v96, v164, v160, s40
v_perm_b32 v97, v172, v168, s40
s_waitcnt lgkmcnt(7)
v_mfma_f32_16x16x16_bf16 v[64:67], v[80:81], v[128:129], v[64:67]
v_perm_b32 v98, v180, v176, s40
v_perm_b32 v99, v188, v184, s40
s_waitcnt lgkmcnt(3)
v_mfma_f32_16x16x16_bf16 v[68:71], v[80:81], v[136:137], v[68:71]
v_mov_b32_dpp v80, v48 row_shl:12 row_mask:0xf bank_mask:0x1
v_mov_b32_dpp v81, v49 row_shl:12 row_mask:0xf bank_mask:0x1
v_perm_b32 v112, v166, v162, s40
v_perm_b32 v113, v174, v170, s40
v_perm_b32 v114, v182, v178, s40
v_perm_b32 v115, v190, v186, s40
v_mov_b32_dpp v84, v48 row_shl:4 row_mask:0xf bank_mask:0x2
v_mov_b32_dpp v85, v49 row_shl:4 row_mask:0xf bank_mask:0x2
v_mfma_f32_16x16x32_bf16 v[72:75], v[80:83], v[96:99], 0
v_mov_b32_dpp v86, v50 row_shr:4 row_mask:0xf bank_mask:0x2
v_mov_b32_dpp v87, v51 row_shr:4 row_mask:0xf bank_mask:0x2
v_mfma_f32_16x16x32_bf16 v[76:79], v[80:83], v[112:115], 0
v_perm_b32 v100, v164, v160, s41
v_perm_b32 v101, v172, v168, s41
v_mfma_f32_16x16x16_bf16 v[64:67], v[84:85], v[130:131], v[64:67]
v_perm_b32 v102, v180, v176, s41
v_perm_b32 v103, v188, v184, s41
s_waitcnt lgkmcnt(2)
v_mfma_f32_16x16x16_bf16 v[68:71], v[84:85], v[138:139], v[68:71]
v_mov_b32_dpp v84, v48 row_shl:8 row_mask:0xf bank_mask:0x2
v_mov_b32_dpp v85, v49 row_shl:8 row_mask:0xf bank_mask:0x2
v_perm_b32 v116, v166, v162, s41
v_perm_b32 v117, v174, v170, s41
v_perm_b32 v118, v182, v178, s41
v_perm_b32 v119, v190, v186, s41
v_mov_b32_dpp v88, v48 quad_perm:[0,1,2,3] row_mask:0xf bank_mask:0x4
v_mov_b32_dpp v89, v49 quad_perm:[0,1,2,3] row_mask:0xf bank_mask:0x4
v_mfma_f32_16x16x32_bf16 v[72:75], v[84:87], v[100:103], v[72:75]
v_mov_b32_dpp v90, v50 row_shr:8 row_mask:0xf bank_mask:0x4
v_mov_b32_dpp v91, v51 row_shr:8 row_mask:0xf bank_mask:0x4
v_mfma_f32_16x16x32_bf16 v[76:79], v[84:87], v[116:119], v[76:79]
v_perm_b32 v104, v165, v161, s40
v_perm_b32 v105, v173, v169, s40
v_mfma_f32_16x16x16_bf16 v[64:67], v[88:89], v[132:133], v[64:67]
v_perm_b32 v106, v181, v177, s40
v_perm_b32 v107, v189, v185, s40
s_waitcnt lgkmcnt(1)
v_mfma_f32_16x16x16_bf16 v[68:71], v[88:89], v[140:141], v[68:71]
v_mov_b32_dpp v88, v48 row_shl:4 row_mask:0xf bank_mask:0x4
v_mov_b32_dpp v89, v49 row_shl:4 row_mask:0xf bank_mask:0x4
v_perm_b32 v120, v167, v163, s40
v_perm_b32 v121, v175, v171, s40
v_perm_b32 v122, v183, v179, s40
v_perm_b32 v123, v191, v187, s40
v_mov_b32_dpp v92, v48 row_shr:4 row_mask:0xf bank_mask:0x8
v_mov_b32_dpp v93, v49 row_shr:4 row_mask:0xf bank_mask:0x8
v_add_f32 v26, v26, v27
v_add_f32 v28, v28, v29
v_add_f32 v26, v26, v28
v_mfma_f32_16x16x32_bf16 v[72:75], v[88:91], v[104:107], v[72:75]
v_mov_b32_dpp v94, v50 row_shr:12 row_mask:0xf bank_mask:0x8
v_mov_b32_dpp v95, v51 row_shr:12 row_mask:0xf bank_mask:0x8
v_mfma_f32_16x16x32_bf16 v[76:79], v[88:91], v[120:123], v[76:79]
v_perm_b32 v108, v165, v161, s41
v_perm_b32 v109, v173, v169, s41
v_mfma_f32_16x16x16_bf16 v[64:67], v[92:93], v[134:135], v[64:67]
v_perm_b32 v110, v181, v177, s41
v_perm_b32 v111, v189, v185, s41
s_waitcnt lgkmcnt(0)
v_mfma_f32_16x16x16_bf16 v[68:71], v[92:93], v[142:143], v[68:71]
v_mov_b32_dpp v92, v48 quad_perm:[0,1,2,3] row_mask:0xf bank_mask:0x8
v_mov_b32_dpp v93, v49 quad_perm:[0,1,2,3] row_mask:0xf bank_mask:0x8
v_perm_b32 v124, v167, v163, s41
v_perm_b32 v125, v175, v171, s41
v_perm_b32 v126, v183, v179, s41
v_perm_b32 v127, v191, v187, s41
v_add_f32_dpp v26, v26, v26 row_ror:4 row_mask:0xf bank_mask:0xf bound_ctrl:1
v_mfma_f32_16x16x32_bf16 v[72:75], v[92:95], v[108:111], v[72:75]
s_nop 0
v_add_f32_dpp v26, v26, v26 row_ror:8 row_mask:0xf bank_mask:0xf bound_ctrl:1
v_mov_b32 v27, v26
v_mfma_f32_16x16x32_bf16 v[76:79], v[92:95], v[124:127], v[76:79]
s_nop 0
v_permlane16_swap_b32 v26, v27
v_add_f32 v26, v26, v27
v_mov_b32 v27, v26
v_cvt_pk_bf16_f32 v128, v64, v68
v_cvt_pk_bf16_f32 v129, v65, v69
v_permlane32_swap_b32 v26, v27
v_add_f32 v26, v26, v27
v_mov_b32 v23, v26
v_cvt_pk_bf16_f32 v130, v66, v70
v_cvt_pk_bf16_f32 v131, v67, v71
ds_write2st64_b32 v9, v128, v129 offset0:16 offset1:17
v_cvt_pk_bf16_f32 v132, v72, v76
v_cvt_pk_bf16_f32 v133, v73, v77
v_cvt_pk_bf16_f32 v134, v74, v78
v_cvt_pk_bf16_f32 v135, v75, v79
ds_write2st64_b32 v9, v132, v133 offset0:20 offset1:21
ds_write2st64_b32 v9, v130, v131 offset0:18 offset1:19
ds_write2st64_b32 v9, v134, v135 offset0:22 offset1:23
ds_write_b64 v11, v[22:23] offset:6144
s_waitcnt lgkmcnt(0)
s_barrier
v_add_u32 v130, 0x1000, v13
v_add_u32 v133, 0x13000, v13
ds_read_b64 v[96:97], v130 offset:0
v_add_u32 v128, 0x1000, v12
ds_read_b64 v[98:99], v130 offset:36864
v_add_u32 v129, 0x1000, v24
ds_read_b64 v[100:101], v133 offset:0
v_add_u32 v131, 0x13000, v12
ds_read_b64 v[102:103], v133 offset:36864
v_add_u32 v132, 0x13000, v24
ds_read_b32 v104, v128 offset:0
v_mov_b32 v137, s12
ds_read2_b32 v[108:109], v129 offset0:0 offset1:8
v_mov_b32 v139, s12
ds_read_b32 v105, v128 offset:36864
ds_read_b32 v110, v129 offset:36864
ds_read_b32 v111, v129 offset:36896
ds_read_b32 v106, v131 offset:0
ds_read2_b32 v[112:113], v132 offset0:0 offset1:8
ds_read_b32 v107, v131 offset:36864
ds_read_b32 v114, v132 offset:36864
ds_read_b32 v115, v132 offset:36896
s_waitcnt lgkmcnt(11)
v_max3_f32 v26, v96, v98, v100
s_waitcnt lgkmcnt(10)
v_max_f32 v26, v26, v102
v_sub_f32 v96, v96, v26
v_sub_f32 v98, v98, v26
v_exp_f32 v96, v96
v_sub_f32 v100, v100, v26
v_exp_f32 v98, v98
s_waitcnt lgkmcnt(9)
v_lshlrev_b32 v116, 16, v104
v_and_b32 v117, 0xffff0000, v104
s_waitcnt lgkmcnt(8)
v_perm_b32 v118, v108, v108, v25
v_perm_b32 v119, v109, v109, v25
v_sub_f32 v102, v102, v26
v_exp_f32 v100, v100
v_pk_add_f32 v[116:117], v[116:117], v[118:119]
s_waitcnt lgkmcnt(7)
v_lshlrev_b32 v120, 16, v105
v_and_b32 v121, 0xffff0000, v105
s_waitcnt lgkmcnt(6)
v_perm_b32 v122, v110, v110, v25
s_waitcnt lgkmcnt(5)
v_perm_b32 v123, v111, v111, v25
v_exp_f32 v102, v102
v_pk_mul_f32 v[134:135], v[116:117], v[96:97] op_sel_hi:[1,0]
v_pk_add_f32 v[120:121], v[120:121], v[122:123]
s_waitcnt lgkmcnt(4)
v_lshlrev_b32 v116, 16, v106
v_and_b32 v117, 0xffff0000, v106
s_waitcnt lgkmcnt(3)
v_perm_b32 v118, v112, v112, v25
v_perm_b32 v119, v113, v113, v25
v_pk_fma_f32 v[134:135], v[120:121], v[98:99], v[134:135] op_sel_hi:[1,0,1]
v_pk_add_f32 v[116:117], v[116:117], v[118:119]
s_waitcnt lgkmcnt(2)
v_lshlrev_b32 v120, 16, v107
v_and_b32 v121, 0xffff0000, v107
s_waitcnt lgkmcnt(1)
v_perm_b32 v122, v114, v114, v25
s_waitcnt lgkmcnt(0)
v_perm_b32 v123, v115, v115, v25
v_pk_fma_f32 v[134:135], v[116:117], v[100:101], v[134:135] op_sel_hi:[1,0,1]
v_pk_add_f32 v[120:121], v[120:121], v[122:123]
v_pk_fma_f32 v[134:135], v[120:121], v[102:103], v[134:135] op_sel_hi:[1,0,1]
v_mul_f32 v27, v96, v97
v_cvt_pk_bf16_f32 v136, v134, v135
v_fma_f32 v27, v98, v99, v27
v_fma_f32 v27, v100, v101, v27
v_fma_f32 v27, v102, v103, v27
v_mov_b32_dpp v138, v136 quad_perm:[1,0,3,2] row_mask:0xf bank_mask:0xf bound_ctrl:1
v_cndmask_b32 v136, v136, v26, s[56:57]
v_cndmask_b32 v138, v138, v27, s[56:57]
s_mov_b32 exec_lo, 0x55555557
s_mov_b32 exec_hi, 0x55555555
buffer_store_dwordx4 v[136:139], v6, s[24:27], 0 offen sc1
s_mov_b64 exec, -1
s_nop 0
v_add_u32 v6, v6, v8
v_add_u32 v6, v6, v8
buffer_load_dwordx4 v[112:115], v5, s[24:27], s42 offen 
buffer_load_dwordx4 v[116:119], v5, s[24:27], s42 offen offset:512 
buffer_load_dwordx4 v[120:123], v5, s[24:27], s42 offen offset:1024 
buffer_load_dwordx4 v[124:127], v5, s[24:27], s42 offen offset:1536 
buffer_load_dwordx4 v[224:227], v2, s[20:23], s31 offen 
buffer_load_dwordx4 v[228:231], v2, s[20:23], s31 offen offset:256 
buffer_load_dwordx4 v[232:235], v2, s[20:23], s31 offen offset:512 
buffer_load_dwordx4 v[236:239], v2, s[20:23], s31 offen offset:768 
buffer_load_dwordx4 v[240:243], v2, s[20:23], s32 offen 
buffer_load_dwordx4 v[244:247], v2, s[20:23], s32 offen offset:256 
buffer_load_dwordx4 v[248:251], v2, s[20:23], s32 offen offset:512 
buffer_load_dwordx4 v[252:255], v2, s[20:23], s32 offen offset:768 
s_add_u32 s16, s16, 0x1000000
s_addc_u32 s17, s17, 0
buffer_load_dwordx4 a[240:243], v1, s[16:19], s28 offen nt
buffer_load_dwordx4 a[244:247], v1, s[16:19], s28 offen offset:128 nt
buffer_load_dwordx4 a[248:251], v1, s[16:19], s28 offen offset:2048 nt
buffer_load_dwordx4 a[252:255], v1, s[16:19], s28 offen offset:2176 nt
s_waitcnt vmcnt(12)
s_mov_b32 s39, 0
Lchk_9_%=:
v_bitop3_b32 v26, v113, v115, s12 bitop3:0x7e
v_bitop3_b32 v27, v117, v119, s12 bitop3:0x7e
v_bitop3_b32 v28, v121, v123, s12 bitop3:0x7e
v_bitop3_b32 v29, v125, v127, s12 bitop3:0x7e
v_or3_b32 v26, v26, v27, v28
v_or_b32 v26, v26, v29
v_cmp_ne_u32 vcc, 0, v26
v_mov_b32 v32, v112
v_mov_b32 v33, v114
v_mov_b32 v34, v116
v_mov_b32 v35, v118
v_mov_b32 v36, v120
v_mov_b32 v37, v122
v_mov_b32 v38, v124
v_mov_b32 v39, v126
s_cbranch_vccnz Lslow_10_%=
Lcont_11_%=:
s_add_u32 s42, s42, 0x4000
s_cmp_lg_u32 s13, 0
s_cbranch_scc1 Lqdo_13_%=
s_mov_b32 m0, 149504
s_nop 0
buffer_load_dwordx4 v52, s[24:27], s42 offen  lds
buffer_load_dwordx4 v52, s[24:27], s42 offen offset:1024  lds
s_branch Lqdj_14_%=
Lqdo_13_%=:
buffer_load_dword v61, v0, s[24:27], 0 offen
buffer_load_dword v61, v0, s[24:27], 0 offen
Lqdj_14_%=:
v_mfma_f32_16x16x32_bf16 v[128:131], a[160:163], v[32:35], 0
v_mfma_f32_16x16x32_bf16 v[132:135], a[168:171], v[32:35], 0
v_mfma_f32_16x16x32_bf16 v[136:139], a[176:179], v[32:35], 0
v_mfma_f32_16x16x32_bf16 v[140:143], a[184:187], v[32:35], 0
v_mfma_f32_16x16x32_bf16 v[128:131], a[164:167], v[36:39], v[128:131]
v_mfma_f32_16x16x32_bf16 v[132:135], a[172:175], v[36:39], v[132:135]
v_mfma_f32_16x16x32_bf16 v[136:139], a[180:183], v[36:39], v[136:139]
v_mfma_f32_16x16x32_bf16 v[140:143], a[188:191], v[36:39], v[140:143]
v_mfma_f32_16x16x32_bf16 v[96:99], a[192:195], v[32:35], 0
s_nop 4
v_permlane32_swap_b32 v128, v132
v_permlane32_swap_b32 v129, v133
v_permlane32_swap_b32 v130, v134
v_permlane32_swap_b32 v131, v135
v_mfma_f32_16x16x32_bf16 v[100:103], a[200:203], v[32:35], 0
v_add_f32_dpp v40, v132, v128 row_shl:4 row_mask:0xf bank_mask:0x1
v_add_f32_dpp v41, v133, v129 row_shl:4 row_mask:0xf bank_mask:0x1
v_add_f32_dpp v42, v134, v130 row_shl:4 row_mask:0xf bank_mask:0x1
v_add_f32_dpp v43, v135, v131 row_shl:4 row_mask:0xf bank_mask:0x1
v_permlane32_swap_b32 v136, v140
v_permlane32_swap_b32 v137, v141
v_permlane32_swap_b32 v138, v142
v_permlane32_swap_b32 v139, v143
v_mfma_f32_16x16x32_bf16 v[128:131], a[208:211], v[32:35], v[56:59]
v_add_f32_dpp v40, v136, v140 row_shr:4 row_mask:0xf bank_mask:0x2
v_add_f32_dpp v41, v137, v141 row_shr:4 row_mask:0xf bank_mask:0x2
v_mfma_f32_16x16x32_bf16 v[132:135], a[216:219], v[32:35], v[56:59]
v_add_f32_dpp v42, v138, v142 row_shr:4 row_mask:0xf bank_mask:0x2
v_add_f32_dpp v43, v139, v143 row_shr:4 row_mask:0xf bank_mask:0x2
v_mfma_f32_16x16x32_bf16 v[96:99], a[196:199], v[36:39], v[96:99]
v_mfma_f32_16x16x32_bf16 v[100:103], a[204:207], v[36:39], v[100:103]
v_mfma_f32_16x16x32_bf16 v[136:139], a[224:227], v[32:35], v[56:59]
v_mfma_f32_16x16x32_bf16 v[140:143], a[232:235], v[32:35], v[56:59]
v_mfma_f32_16x16x32_bf16 v[128:131], a[212:215], v[36:39], v[128:131]
s_nop 4
v_permlane32_swap_b32 v96, v100
v_permlane32_swap_b32 v97, v101
v_mfma_f32_16x16x32_bf16 v[132:135], a[220:223], v[36:39], v[132:135]
v_permlane32_swap_b32 v98, v102
v_permlane32_swap_b32 v99, v103
v_mfma_f32_16x16x32_bf16 v[136:139], a[228:231], v[36:39], v[136:139]
v_add_f32_dpp v40, v100, v96 row_shl:4 row_mask:0xf bank_mask:0x4
v_add_f32_dpp v41, v101, v97 row_shl:4 row_mask:0xf bank_mask:0x4
v_mfma_f32_16x16x32_bf16 v[140:143], a[236:239], v[36:39], v[140:143]
v_add_f32_dpp v42, v102, v98 row_shl:4 row_mask:0xf bank_mask:0x4
v_add_f32_dpp v43, v103, v99 row_shl:4 row_mask:0xf bank_mask:0x4
v_permlane32_swap_b32 v128, v132
v_permlane32_swap_b32 v129, v133
v_permlane32_swap_b32 v130, v134
v_permlane32_swap_b32 v131, v135
v_add_f32_dpp v40, v128, v132 row_shr:4 row_mask:0xf bank_mask:0x8
v_add_f32_dpp v41, v129, v133 row_shr:4 row_mask:0xf bank_mask:0x8
v_add_f32_dpp v42, v130, v134 row_shr:4 row_mask:0xf bank_mask:0x8
v_add_f32_dpp v43, v131, v135 row_shr:4 row_mask:0xf bank_mask:0x8
v_permlane32_swap_b32 v136, v140
v_permlane32_swap_b32 v137, v141
v_permlane32_swap_b32 v138, v142
v_permlane32_swap_b32 v139, v143
v_add_f32_dpp v44, v140, v136 row_shl:4 row_mask:0xf bank_mask:0x1
v_add_f32_dpp v45, v141, v137 row_shl:4 row_mask:0xf bank_mask:0x1
v_add_f32_dpp v46, v142, v138 row_shl:4 row_mask:0xf bank_mask:0x1
v_add_f32_dpp v47, v143, v139 row_shl:4 row_mask:0xf bank_mask:0x1
v_max3_f32 v26, v40, v41, v42
v_max3_f32 v27, v43, v44, v45
v_max3_f32 v26, v26, v46, v47
v_max_f32 v26, v26, v27
s_nop 1
v_max_f32_dpp v26, v26, v26 row_ror:4 row_mask:0xf bank_mask:0xf bound_ctrl:1
s_nop 1
v_max_f32_dpp v26, v26, v26 row_ror:8 row_mask:0xf bank_mask:0xf bound_ctrl:1
v_mov_b32 v27, v26
s_nop 1
v_permlane16_swap_b32 v26, v27
v_max_f32 v26, v26, v27
v_mov_b32 v27, v26
s_nop 1
v_permlane32_swap_b32 v26, v27
v_max_f32 v26, v26, v27
v_mul_f32 v22, s34, v26
v_fma_f32 v128, v40, s34, -v22
v_fma_f32 v129, v41, s34, -v22
v_fma_f32 v130, v42, s34, -v22
v_fma_f32 v131, v43, s34, -v22
v_fma_f32 v132, v44, s34, -v22
v_fma_f32 v133, v45, s34, -v22
v_fma_f32 v134, v46, s34, -v22
v_fma_f32 v135, v47, s34, -v22
v_exp_f32 v128, v128
v_exp_f32 v129, v129
v_exp_f32 v130, v130
v_exp_f32 v131, v131
v_exp_f32 v132, v132
v_exp_f32 v133, v133
v_exp_f32 v134, v134
v_exp_f32 v135, v135
v_add_f32 v26, v128, v129
v_add_f32 v27, v130, v131
v_add_f32 v28, v132, v133
v_add_f32 v29, v134, v135
v_add_f32 v26, v26, v27
v_add_f32 v28, v28, v29
v_add_f32 v26, v26, v28
v_cvt_pk_bf16_f32 v48, v128, v129
v_cvt_pk_bf16_f32 v49, v130, v131
v_add_f32_dpp v26, v26, v26 row_ror:4 row_mask:0xf bank_mask:0xf bound_ctrl:1
v_cvt_pk_bf16_f32 v50, v132, v133
v_cvt_pk_bf16_f32 v51, v134, v135
v_add_f32_dpp v26, v26, v26 row_ror:8 row_mask:0xf bank_mask:0xf bound_ctrl:1
v_mov_b32 v27, v26
s_nop 1
v_permlane16_swap_b32 v26, v27
v_add_f32 v26, v26, v27
v_mov_b32 v27, v26
s_nop 1
v_permlane32_swap_b32 v26, v27
v_add_f32 v26, v26, v27
v_mov_b32 v23, v26
buffer_load_dwordx4 v[144:147], v1, s[16:19], s29 offen nt
buffer_load_dwordx4 v[148:151], v1, s[16:19], s29 offen offset:128 nt
buffer_load_dwordx4 v[152:155], v1, s[16:19], s29 offen offset:2048 nt
buffer_load_dwordx4 v[156:159], v1, s[16:19], s29 offen offset:2176 nt
buffer_load_dwordx4 a[0:3], v1, s[16:19], s30 offen nt
buffer_load_dwordx4 a[4:7], v1, s[16:19], s30 offen offset:128 nt
buffer_load_dwordx4 a[8:11], v1, s[16:19], s30 offen offset:2048 nt
buffer_load_dwordx4 a[12:15], v1, s[16:19], s30 offen offset:2176 nt
s_waitcnt vmcnt(14)
buffer_load_dwordx4 a[16:19], v1, s[16:19], s31 offen nt
buffer_load_dwordx4 a[20:23], v1, s[16:19], s31 offen offset:128 nt
buffer_load_dwordx4 a[24:27], v1, s[16:19], s31 offen offset:2048 nt
buffer_load_dwordx4 a[28:31], v1, s[16:19], s31 offen offset:2176 nt
ds_read_b64_tr_b16 v[96:97], v14 offset:24576
v_mov_b32_dpp v80, v48 quad_perm:[0,1,2,3] row_mask:0xf bank_mask:0x1
ds_read_b64_tr_b16 v[98:99], v14 offset:28672
v_mov_b32_dpp v81, v49 quad_perm:[0,1,2,3] row_mask:0xf bank_mask:0x1
ds_read_b64_tr_b16 v[100:101], v15 offset:24576
v_mov_b32_dpp v82, v48 row_shl:4 row_mask:0xf bank_mask:0x1
ds_read_b64_tr_b16 v[102:103], v15 offset:28672
v_mov_b32_dpp v83, v49 row_shl:4 row_mask:0xf bank_mask:0x1
ds_read_b64_tr_b16 v[104:105], v16 offset:24576
v_mov_b32_dpp v84, v48 row_shr:4 row_mask:0xf bank_mask:0x2
ds_read_b64_tr_b16 v[106:107], v16 offset:28672
v_mov_b32_dpp v85, v49 row_shr:4 row_mask:0xf bank_mask:0x2
ds_read_b64_tr_b16 v[108:109], v17 offset:24576
v_mov_b32_dpp v86, v48 quad_perm:[0,1,2,3] row_mask:0xf bank_mask:0x2
ds_read_b64_tr_b16 v[110:111], v17 offset:28672
v_mov_b32_dpp v87, v49 quad_perm:[0,1,2,3] row_mask:0xf bank_mask:0x2
ds_read_b64_tr_b16 v[112:113], v18 offset:24576
v_mov_b32_dpp v88, v48 row_shr:8 row_mask:0xf bank_mask:0x4
ds_read_b64_tr_b16 v[114:115], v18 offset:28672
v_mov_b32_dpp v89, v49 row_shr:8 row_mask:0xf bank_mask:0x4
ds_read_b64_tr_b16 v[116:117], v19 offset:24576
v_mov_b32_dpp v90, v48 row_shr:4 row_mask:0xf bank_mask:0x4
ds_read_b64_tr_b16 v[118:119], v19 offset:28672
v_mov_b32_dpp v91, v49 row_shr:4 row_mask:0xf bank_mask:0x4
ds_read_b64_tr_b16 v[120:121], v20 offset:24576
v_mov_b32_dpp v92, v48 row_shr:12 row_mask:0xf bank_mask:0x8
ds_read_b64_tr_b16 v[122:123], v20 offset:28672
v_mov_b32_dpp v93, v49 row_shr:12 row_mask:0xf bank_mask:0x8
ds_read_b64_tr_b16 v[124:125], v21 offset:24576
v_mov_b32_dpp v94, v48 row_shr:8 row_mask:0xf bank_mask:0x8
ds_read_b64_tr_b16 v[126:127], v21 offset:28672
v_mov_b32_dpp v95, v49 row_shr:8 row_mask:0xf bank_mask:0x8
ds_read_b64_tr_b16 v[128:129], v14 offset:32768
s_waitcnt lgkmcnt(14)
v_mfma_f32_16x16x32_bf16 v[64:67], v[80:83], v[96:99], 0
ds_read_b64_tr_b16 v[130:131], v15 offset:32768
v_perm_b32 v96, v228, v224, s40
ds_read_b64_tr_b16 v[132:133], v16 offset:32768
s_waitcnt lgkmcnt(14)
v_mfma_f32_16x16x32_bf16 v[64:67], v[84:87], v[100:103], v[64:67]
ds_read_b64_tr_b16 v[134:135], v17 offset:32768
v_perm_b32 v97, v236, v232, s40
ds_read_b64_tr_b16 v[136:137], v18 offset:32768
s_waitcnt lgkmcnt(14)
v_mfma_f32_16x16x32_bf16 v[64:67], v[88:91], v[104:107], v[64:67]
ds_read_b64_tr_b16 v[138:139], v19 offset:32768
v_perm_b32 v98, v244, v240, s40
ds_read_b64_tr_b16 v[140:141], v20 offset:32768
s_waitcnt lgkmcnt(13)
v_mfma_f32_16x16x32_bf16 v[68:71], v[80:83], v[112:115], 0
ds_read_b64_tr_b16 v[142:143], v21 offset:32768
v_mov_b32_dpp v80, v48 row_shl:8 row_mask:0xf bank_mask:0x1
s_waitcnt lgkmcnt(12)
v_mfma_f32_16x16x32_bf16 v[68:71], v[84:87], v[116:119], v[68:71]
v_mov_b32_dpp v81, v49 row_shl:8 row_mask:0xf bank_mask:0x1
v_mov_b32_dpp v82, v50 quad_perm:[0,1,2,3] row_mask:0xf bank_mask:0x1
s_waitcnt lgkmcnt(10)
v_mfma_f32_16x16x32_bf16 v[68:71], v[88:91], v[120:123], v[68:71]
v_mov_b32_dpp v83, v51 quad_perm:[0,1,2,3] row_mask:0xf bank_mask:0x1
v_perm_b32 v99, v252, v248, s40
v_mfma_f32_16x16x32_bf16 v[64:67], v[92:95], v[108:111], v[64:67]
v_perm_b32 v112, v230, v226, s40
v_perm_b32 v113, v238, v234, s40
s_waitcnt lgkmcnt(8)
v_mfma_f32_16x16x32_bf16 v[68:71], v[92:95], v[124:127], v[68:71]
v_perm_b32 v114, v246, v242, s40
v_perm_b32 v115, v254, v250, s40
s_waitcnt lgkmcnt(7)
v_mfma_f32_16x16x16_bf16 v[64:67], v[80:81], v[128:129], v[64:67]
v_mov_b32_dpp v84, v48 row_shl:4 row_mask:0xf bank_mask:0x2
v_mov_b32_dpp v85, v49 row_shl:4 row_mask:0xf bank_mask:0x2
s_waitcnt lgkmcnt(3)
v_mfma_f32_16x16x16_bf16 v[68:71], v[80:81], v[136:137], v[68:71]
v_mov_b32_dpp v80, v48 row_shl:12 row_mask:0xf bank_mask:0x1
v_mov_b32_dpp v81, v49 row_shl:12 row_mask:0xf bank_mask:0x1
v_mfma_f32_16x16x16_bf16 v[64:67], v[84:85], v[130:131], v[64:67]
v_mov_b32_dpp v86, v50 row_shr:4 row_mask:0xf bank_mask:0x2
v_mov_b32_dpp v87, v51 row_shr:4 row_mask:0xf bank_mask:0x2
v_mfma_f32_16x16x32_bf16 v[72:75], v[80:83], v[96:99], 0
v_perm_b32 v100, v228, v224, s41
v_perm_b32 v101, v236, v232, s41
v_mfma_f32_16x16x32_bf16 v[76:79], v[80:83], v[112:115], 0
v_perm_b32 v102, v244, v240, s41
v_perm_b32 v103, v252, v248, s41
s_waitcnt lgkmcnt(2)
v_mfma_f32_16x16x16_bf16 v[68:71], v[84:85], v[138:139], v[68:71]
v_mov_b32_dpp v84, v48 row_shl:8 row_mask:0xf bank_mask:0x2
v_mov_b32_dpp v85, v49 row_shl:8 row_mask:0xf bank_mask:0x2
v_perm_b32 v116, v230, v226, s41
v_perm_b32 v117, v238, v234, s41
v_perm_b32 v118, v246, v242, s41
v_perm_b32 v119, v254, v250, s41
v_mov_b32_dpp v88, v48 quad_perm:[0,1,2,3] row_mask:0xf bank_mask:0x4
v_mov_b32_dpp v89, v49 quad_perm:[0,1,2,3] row_mask:0xf bank_mask:0x4
v_mfma_f32_16x16x32_bf16 v[72:75], v[84:87], v[100:103], v[72:75]
v_mov_b32_dpp v90, v50 row_shr:8 row_mask:0xf bank_mask:0x4
v_mov_b32_dpp v91, v51 row_shr:8 row_mask:0xf bank_mask:0x4
v_mfma_f32_16x16x32_bf16 v[76:79], v[84:87], v[116:119], v[76:79]
v_perm_b32 v104, v229, v225, s40
v_perm_b32 v105, v237, v233, s40
v_mfma_f32_16x16x16_bf16 v[64:67], v[88:89], v[132:133], v[64:67]
v_perm_b32 v106, v245, v241, s40
v_perm_b32 v107, v253, v249, s40
s_waitcnt lgkmcnt(1)
v_mfma_f32_16x16x16_bf16 v[68:71], v[88:89], v[140:141], v[68:71]
v_mov_b32_dpp v88, v48 row_shl:4 row_mask:0xf bank_mask:0x4
v_mov_b32_dpp v89, v49 row_shl:4 row_mask:0xf bank_mask:0x4
v_perm_b32 v120, v231, v227, s40
v_perm_b32 v121, v239, v235, s40
v_perm_b32 v122, v247, v243, s40
v_perm_b32 v123, v255, v251, s40
v_mov_b32_dpp v92, v48 row_shr:4 row_mask:0xf bank_mask:0x8
v_mov_b32_dpp v93, v49 row_shr:4 row_mask:0xf bank_mask:0x8
v_mfma_f32_16x16x32_bf16 v[72:75], v[88:91], v[104:107], v[72:75]
v_mov_b32_dpp v94, v50 row_shr:12 row_mask:0xf bank_mask:0x8
v_mov_b32_dpp v95, v51 row_shr:12 row_mask:0xf bank_mask:0x8
v_mfma_f32_16x16x32_bf16 v[76:79], v[88:91], v[120:123], v[76:79]
v_perm_b32 v108, v229, v225, s41
v_perm_b32 v109, v237, v233, s41
v_mfma_f32_16x16x16_bf16 v[64:67], v[92:93], v[134:135], v[64:67]
v_perm_b32 v110, v245, v241, s41
v_perm_b32 v111, v253, v249, s41
s_waitcnt lgkmcnt(0)
v_mfma_f32_16x16x16_bf16 v[68:71], v[92:93], v[142:143], v[68:71]
v_mov_b32_dpp v92, v48 quad_perm:[0,1,2,3] row_mask:0xf bank_mask:0x8
v_mov_b32_dpp v93, v49 quad_perm:[0,1,2,3] row_mask:0xf bank_mask:0x8
v_perm_b32 v124, v231, v227, s41
v_perm_b32 v125, v239, v235, s41
v_perm_b32 v126, v247, v243, s41
v_perm_b32 v127, v255, v251, s41
v_mfma_f32_16x16x32_bf16 v[72:75], v[92:95], v[108:111], v[72:75]
v_add_u32 v29, s35, v11
v_mfma_f32_16x16x32_bf16 v[76:79], v[92:95], v[124:127], v[76:79]
v_cvt_pk_bf16_f32 v128, v64, v68
v_cvt_pk_bf16_f32 v129, v65, v69
v_cvt_pk_bf16_f32 v130, v66, v70
v_cvt_pk_bf16_f32 v131, v67, v71
ds_write2st64_b32 v10, v128, v129 offset0:0 offset1:1
s_nop 2
v_cvt_pk_bf16_f32 v132, v72, v76
v_cvt_pk_bf16_f32 v133, v73, v77
v_cvt_pk_bf16_f32 v134, v74, v78
v_cvt_pk_bf16_f32 v135, v75, v79
ds_write2st64_b32 v10, v132, v133 offset0:4 offset1:5
ds_write2st64_b32 v10, v130, v131 offset0:2 offset1:3
ds_write2st64_b32 v10, v134, v135 offset0:6 offset1:7
ds_write_b64 v29, v[22:23] offset:2048
s_waitcnt vmcnt(12)
s_waitcnt lgkmcnt(0)
s_barrier
v_add_u32 v130, 0x25800, v13
ds_read_b64 v[96:97], v130 offset:0
v_add_u32 v128, 0x25800, v12
ds_read_b64 v[98:99], v130 offset:2176
v_add_u32 v129, 0x25800, v24
ds_read_b64 v[100:101], v130 offset:4352
v_mov_b32 v137, s12
ds_read_b64 v[102:103], v130 offset:6528
v_mov_b32 v139, s12
ds_read_b32 v104, v128 offset:0
ds_read2_b32 v[108:109], v129 offset0:0 offset1:8
ds_read_b32 v105, v128 offset:2176
ds_read_b32 v110, v129 offset:2176
ds_read_b32 v111, v129 offset:2208
ds_read_b32 v106, v128 offset:4352
ds_read_b32 v112, v129 offset:4352
ds_read_b32 v113, v129 offset:4384
ds_read_b32 v107, v128 offset:6528
ds_read_b32 v114, v129 offset:6528
ds_read_b32 v115, v129 offset:6560
s_waitcnt lgkmcnt(12)
v_max3_f32 v26, v96, v98, v100
s_waitcnt lgkmcnt(11)
v_max_f32 v26, v26, v102
v_sub_f32 v96, v96, v26
v_sub_f32 v98, v98, v26
v_exp_f32 v96, v96
v_sub_f32 v100, v100, v26
v_exp_f32 v98, v98
s_waitcnt lgkmcnt(10)
v_lshlrev_b32 v116, 16, v104
v_and_b32 v117, 0xffff0000, v104
s_waitcnt lgkmcnt(9)
v_perm_b32 v118, v108, v108, v25
v_perm_b32 v119, v109, v109, v25
v_sub_f32 v102, v102, v26
v_exp_f32 v100, v100
v_pk_add_f32 v[116:117], v[116:117], v[118:119]
s_waitcnt lgkmcnt(8)
v_lshlrev_b32 v120, 16, v105
v_and_b32 v121, 0xffff0000, v105
s_waitcnt lgkmcnt(7)
v_perm_b32 v122, v110, v110, v25
s_waitcnt lgkmcnt(6)
v_perm_b32 v123, v111, v111, v25
v_exp_f32 v102, v102
v_pk_mul_f32 v[134:135], v[116:117], v[96:97] op_sel_hi:[1,0]
v_pk_add_f32 v[120:121], v[120:121], v[122:123]
s_waitcnt lgkmcnt(5)
v_lshlrev_b32 v116, 16, v106
v_and_b32 v117, 0xffff0000, v106
s_waitcnt lgkmcnt(4)
v_perm_b32 v118, v112, v112, v25
s_waitcnt lgkmcnt(3)
v_perm_b32 v119, v113, v113, v25
v_pk_fma_f32 v[134:135], v[120:121], v[98:99], v[134:135] op_sel_hi:[1,0,1]
v_pk_add_f32 v[116:117], v[116:117], v[118:119]
s_waitcnt lgkmcnt(2)
v_lshlrev_b32 v120, 16, v107
v_and_b32 v121, 0xffff0000, v107
s_waitcnt lgkmcnt(1)
v_perm_b32 v122, v114, v114, v25
s_waitcnt lgkmcnt(0)
v_perm_b32 v123, v115, v115, v25
v_pk_fma_f32 v[134:135], v[116:117], v[100:101], v[134:135] op_sel_hi:[1,0,1]
v_pk_add_f32 v[120:121], v[120:121], v[122:123]
v_pk_fma_f32 v[134:135], v[120:121], v[102:103], v[134:135] op_sel_hi:[1,0,1]
v_mul_f32 v27, v96, v97
v_cvt_pk_bf16_f32 v136, v134, v135
v_fma_f32 v27, v98, v99, v27
v_fma_f32 v27, v100, v101, v27
v_fma_f32 v27, v102, v103, v27
v_mov_b32_dpp v138, v136 quad_perm:[1,0,3,2] row_mask:0xf bank_mask:0xf bound_ctrl:1
v_cndmask_b32 v136, v136, v26, s[56:57]
v_cndmask_b32 v138, v138, v27, s[56:57]
s_mov_b32 exec_lo, 0x55555557
s_mov_b32 exec_hi, 0x55555555
buffer_store_dwordx4 v[136:139], v6, s[24:27], 0 offen sc1
s_mov_b64 exec, -1
s_nop 0
v_add_u32 v6, v6, v8
s_mov_b32 s39, 0
Lqchk_15_%=:
ds_read_b128 v[96:99], v60 offset:2048
ds_read_b128 v[100:103], v60 offset:2560
ds_read_b128 v[104:107], v60 offset:3072
ds_read_b128 v[108:111], v60 offset:3584
s_waitcnt lgkmcnt(0)
v_bitop3_b32 v26, v97, v99, s12 bitop3:0x7e
v_bitop3_b32 v27, v101, v103, s12 bitop3:0x7e
v_bitop3_b32 v28, v105, v107, s12 bitop3:0x7e
v_bitop3_b32 v29, v109, v111, s12 bitop3:0x7e
v_or3_b32 v26, v26, v27, v28
v_or_b32 v26, v26, v29
v_cmp_ne_u32 vcc, 0, v26
v_mov_b32 v32, v96
v_mov_b32 v33, v98
v_mov_b32 v34, v100
v_mov_b32 v35, v102
v_mov_b32 v36, v104
v_mov_b32 v37, v106
v_mov_b32 v38, v108
v_mov_b32 v39, v110
s_cbranch_vccnz Lqslow_16_%=
Lqcont_17_%=:
buffer_load_dwordx4 a[32:35], v1, s[16:19], s32 offen nt
buffer_load_dwordx4 a[36:39], v1, s[16:19], s32 offen offset:128 nt
buffer_load_dwordx4 a[40:43], v1, s[16:19], s32 offen offset:2048 nt
buffer_load_dwordx4 a[44:47], v1, s[16:19], s32 offen offset:2176 nt
s_add_u32 s20, s20, 0x1000000
s_addc_u32 s21, s21, 0
s_add_u32 s38, s37, 0
s_mov_b32 m0, s38
s_nop 0
buffer_load_dwordx4 v3, s[20:23], s28 offen  lds
buffer_load_dwordx4 v4, s[20:23], s28 offen offset:1024  lds
buffer_load_dwordx4 v3, s[20:23], s28 offen offset:2048  lds
buffer_load_dwordx4 v4, s[20:23], s28 offen offset:3072  lds
buffer_load_dwordx4 v[160:163], v2, s[20:23], s31 offen 
buffer_load_dwordx4 v[164:167], v2, s[20:23], s31 offen offset:256 
buffer_load_dwordx4 v[168:171], v2, s[20:23], s31 offen offset:512 
buffer_load_dwordx4 v[172:175], v2, s[20:23], s31 offen offset:768 
buffer_load_dwordx4 v[176:179], v2, s[20:23], s32 offen 
buffer_load_dwordx4 v[180:183], v2, s[20:23], s32 offen offset:256 
buffer_load_dwordx4 v[184:187], v2, s[20:23], s32 offen offset:512 
buffer_load_dwordx4 v[188:191], v2, s[20:23], s32 offen offset:768 
Lit3_%=:
v_mfma_f32_16x16x32_bf16 v[128:131], a[240:243], v[32:35], 0
v_mfma_f32_16x16x32_bf16 v[132:135], a[248:251], v[32:35], 0
v_mfma_f32_16x16x32_bf16 v[128:131], a[244:247], v[36:39], v[128:131]
v_mfma_f32_16x16x32_bf16 v[132:135], a[252:255], v[36:39], v[132:135]
s_nop 7
v_permlane32_swap_b32 v128, v132
v_permlane32_swap_b32 v129, v133
v_permlane32_swap_b32 v130, v134
v_permlane32_swap_b32 v131, v135
v_add_f32_dpp v40, v132, v128 row_shl:4 row_mask:0xf bank_mask:0x1
v_add_f32_dpp v41, v133, v129 row_shl:4 row_mask:0xf bank_mask:0x1
v_add_f32_dpp v42, v134, v130 row_shl:4 row_mask:0xf bank_mask:0x1
v_add_f32_dpp v43, v135, v131 row_shl:4 row_mask:0xf bank_mask:0x1
s_barrier
s_add_u32 s38, s37, 0x1000
s_mov_b32 m0, s38
s_nop 0
buffer_load_dwordx4 v3, s[20:23], s29 offen  lds
buffer_load_dwordx4 v4, s[20:23], s29 offen offset:1024  lds
buffer_load_dwordx4 v3, s[20:23], s29 offen offset:2048  lds
buffer_load_dwordx4 v4, s[20:23], s29 offen offset:3072  lds
s_add_u32 s68, s64, Lit4_%=-Lcode0_%=
s_addc_u32 s69, s65, 0
s_and_b32 s69, s69, 0xffff
s_mov_b32 s70, Lit5_%=-Lit4_%=
s_mov_b32 s71, 0x20000
buffer_load_dword v61, v62, s[68:71], 0 offen
s_waitcnt vmcnt(30)
v_mfma_f32_16x16x32_bf16 v[128:131], v[144:147], v[32:35], 0
v_mfma_f32_16x16x32_bf16 v[132:135], v[152:155], v[32:35], 0
v_mfma_f32_16x16x32_bf16 v[128:131], v[148:151], v[36:39], v[128:131]
v_mfma_f32_16x16x32_bf16 v[132:135], v[156:159], v[36:39], v[132:135]
s_nop 7
v_permlane32_swap_b32 v128, v132
v_permlane32_swap_b32 v129, v133
v_permlane32_swap_b32 v130, v134
v_permlane32_swap_b32 v131, v135
v_add_f32_dpp v40, v128, v132 row_shr:4 row_mask:0xf bank_mask:0x2
v_add_f32_dpp v41, v129, v133 row_shr:4 row_mask:0xf bank_mask:0x2
v_add_f32_dpp v42, v130, v134 row_shr:4 row_mask:0xf bank_mask:0x2
v_add_f32_dpp v43, v131, v135 row_shr:4 row_mask:0xf bank_mask:0x2
s_add_u32 s38, s37, 0x2000
s_mov_b32 m0, s38
s_nop 0
buffer_load_dwordx4 v3, s[20:23], s30 offen  lds
buffer_load_dwordx4 v4, s[20:23], s30 offen offset:1024  lds
buffer_load_dwordx4 v3, s[20:23], s30 offen offset:2048  lds
buffer_load_dwordx4 v4, s[20:23], s30 offen offset:3072  lds
s_add_u32 s42, s42, 0x4000
s_cmp_lg_u32 s13, 0
s_cbranch_scc1 Lqdo_21_%=
s_mov_b32 m0, 149504
s_nop 0
buffer_load_dwordx4 v52, s[24:27], s42 offen  lds
buffer_load_dwordx4 v52, s[24:27], s42 offen offset:1024  lds
s_branch Lqdj_22_%=
Lqdo_21_%=:
buffer_load_dword v61, v0, s[24:27], 0 offen
buffer_load_dword v61, v0, s[24:27], 0 offen
Lqdj_22_%=:
s_waitcnt vmcnt(32)
v_mfma_f32_16x16x32_bf16 v[128:131], a[0:3], v[32:35], 0
v_mfma_f32_16x16x32_bf16 v[132:135], a[8:11], v[32:35], 0
v_mfma_f32_16x16x32_bf16 v[128:131], a[4:7], v[36:39], v[128:131]
v_mfma_f32_16x16x32_bf16 v[132:135], a[12:15], v[36:39], v[132:135]
s_nop 7
v_permlane32_swap_b32 v128, v132
v_permlane32_swap_b32 v129, v133
v_permlane32_swap_b32 v130, v134
v_permlane32_swap_b32 v131, v135
v_add_f32_dpp v40, v132, v128 row_shl:4 row_mask:0xf bank_mask:0x4
v_add_f32_dpp v41, v133, v129 row_shl:4 row_mask:0xf bank_mask:0x4
v_add_f32_dpp v42, v134, v130 row_shl:4 row_mask:0xf bank_mask:0x4
v_add_f32_dpp v43, v135, v131 row_shl:4 row_mask:0xf bank_mask:0x4
s_waitcnt vmcnt(28)
v_mfma_f32_16x16x32_bf16 v[128:131], a[16:19], v[32:35], v[56:59]
v_mfma_f32_16x16x32_bf16 v[132:135], a[24:27], v[32:35], v[56:59]
v_mfma_f32_16x16x32_bf16 v[128:131], a[20:23], v[36:39], v[128:131]
v_mfma_f32_16x16x32_bf16 v[132:135], a[28:31], v[36:39], v[132:135]
s_nop 7
v_permlane32_swap_b32 v128, v132
v_permlane32_swap_b32 v129, v133
v_permlane32_swap_b32 v130, v134
v_permlane32_swap_b32 v131, v135
v_add_f32_dpp v40, v128, v132 row_shr:4 row_mask:0xf bank_mask:0x8
v_add_f32_dpp v41, v129, v133 row_shr:4 row_mask:0xf bank_mask:0x8
v_add_f32_dpp v42, v130, v134 row_shr:4 row_mask:0xf bank_mask:0x8
v_add_f32_dpp v43, v131, v135 row_shr:4 row_mask:0xf bank_mask:0x8
s_add_u32 s16, s16, 0x1000000
s_addc_u32 s17, s17, 0
buffer_load_dwordx4 a[48:51], v1, s[16:19], s28 offen nt
buffer_load_dwordx4 a[52:55], v1, s[16:19], s28 offen offset:128 nt
buffer_load_dwordx4 a[56:59], v1, s[16:19], s28 offen offset:2048 nt
buffer_load_dwordx4 a[60:63], v1, s[16:19], s28 offen offset:2176 nt
s_waitcnt vmcnt(27)
v_mfma_f32_16x16x32_bf16 v[128:131], a[32:35], v[32:35], v[56:59]
v_max3_f32 v26, v40, v41, v42
v_mfma_f32_16x16x32_bf16 v[132:135], a[40:43], v[32:35], v[56:59]
v_mfma_f32_16x16x32_bf16 v[128:131], a[36:39], v[36:39], v[128:131]
v_mfma_f32_16x16x32_bf16 v[132:135], a[44:47], v[36:39], v[132:135]
s_nop 7
v_permlane32_swap_b32 v128, v132
v_permlane32_swap_b32 v129, v133
v_permlane32_swap_b32 v130, v134
v_permlane32_swap_b32 v131, v135
v_add_f32_dpp v44, v132, v128 row_shl:4 row_mask:0xf bank_mask:0x1
v_add_f32_dpp v45, v133, v129 row_shl:4 row_mask:0xf bank_mask:0x1
v_add_f32_dpp v46, v134, v130 row_shl:4 row_mask:0xf bank_mask:0x1
v_add_f32_dpp v47, v135, v131 row_shl:4 row_mask:0xf bank_mask:0x1
v_max3_f32 v27, v43, v44, v45
v_max3_f32 v26, v26, v46, v47
v_max_f32 v26, v26, v27
s_nop 1
v_max_f32_dpp v26, v26, v26 row_ror:4 row_mask:0xf bank_mask:0xf bound_ctrl:1
s_nop 1
v_max_f32_dpp v26, v26, v26 row_ror:8 row_mask:0xf bank_mask:0xf bound_ctrl:1
v_mov_b32 v27, v26
s_nop 1
v_permlane16_swap_b32 v26, v27
v_max_f32 v26, v26, v27
v_mov_b32 v27, v26
s_nop 1
v_permlane32_swap_b32 v26, v27
v_max_f32 v26, v26, v27
v_mul_f32 v22, s34, v26
v_fma_f32 v128, v40, s34, -v22
v_fma_f32 v129, v41, s34, -v22
v_fma_f32 v130, v42, s34, -v22
v_fma_f32 v131, v43, s34, -v22
v_fma_f32 v132, v44, s34, -v22
v_fma_f32 v133, v45, s34, -v22
v_fma_f32 v134, v46, s34, -v22
v_fma_f32 v135, v47, s34, -v22
v_exp_f32 v128, v128
v_exp_f32 v129, v129
v_exp_f32 v130, v130
v_exp_f32 v131, v131
v_exp_f32 v132, v132
v_exp_f32 v133, v133
v_exp_f32 v134, v134
v_exp_f32 v135, v135
v_add_f32 v26, v128, v129
v_add_f32 v27, v130, v131
v_add_f32 v28, v132, v133
v_add_f32 v29, v134, v135
v_add_f32 v26, v26, v27
v_add_f32 v28, v28, v29
v_add_f32 v26, v26, v28
v_cvt_pk_bf16_f32 v48, v128, v129
v_cvt_pk_bf16_f32 v49, v130, v131
v_add_f32_dpp v26, v26, v26 row_ror:4 row_mask:0xf bank_mask:0xf bound_ctrl:1
v_cvt_pk_bf16_f32 v50, v132, v133
v_cvt_pk_bf16_f32 v51, v134, v135
v_add_f32_dpp v26, v26, v26 row_ror:8 row_mask:0xf bank_mask:0xf bound_ctrl:1
v_mov_b32 v27, v26
s_nop 1
v_permlane16_swap_b32 v26, v27
v_add_f32 v26, v26, v27
v_mov_b32 v27, v26
s_nop 1
v_permlane32_swap_b32 v26, v27
v_add_f32 v26, v26, v27
v_mov_b32 v23, v26
buffer_load_dwordx4 a[64:67], v1, s[16:19], s29 offen nt
buffer_load_dwordx4 a[68:71], v1, s[16:19], s29 offen offset:128 nt
buffer_load_dwordx4 a[72:75], v1, s[16:19], s29 offen offset:2048 nt
buffer_load_dwordx4 a[76:79], v1, s[16:19], s29 offen offset:2176 nt
s_waitcnt vmcnt(19)
v_mov_b32_dpp v80, v48 row_shl:12 row_mask:0xf bank_mask:0x1
v_mov_b32_dpp v81, v49 row_shl:12 row_mask:0xf bank_mask:0x1
v_mov_b32_dpp v82, v50 quad_perm:[0,1,2,3] row_mask:0xf bank_mask:0x1
v_mov_b32_dpp v83, v51 quad_perm:[0,1,2,3] row_mask:0xf bank_mask:0x1
v_perm_b32 v96, v164, v160, s40
v_perm_b32 v97, v172, v168, s40
v_perm_b32 v98, v180, v176, s40
v_perm_b32 v99, v188, v184, s40
v_perm_b32 v112, v166, v162, s40
v_perm_b32 v113, v174, v170, s40
v_perm_b32 v114, v182, v178, s40
v_perm_b32 v115, v190, v186, s40
v_mfma_f32_16x16x32_bf16 v[72:75], v[80:83], v[96:99], 0
v_mov_b32_dpp v84, v48 row_shl:8 row_mask:0xf bank_mask:0x2
v_mov_b32_dpp v85, v49 row_shl:8 row_mask:0xf bank_mask:0x2
v_mfma_f32_16x16x32_bf16 v[76:79], v[80:83], v[112:115], 0
v_mov_b32_dpp v86, v50 row_shr:4 row_mask:0xf bank_mask:0x2
v_mov_b32_dpp v87, v51 row_shr:4 row_mask:0xf bank_mask:0x2
v_perm_b32 v100, v164, v160, s41
v_perm_b32 v101, v172, v168, s41
v_perm_b32 v102, v180, v176, s41
v_perm_b32 v103, v188, v184, s41
v_perm_b32 v116, v166, v162, s41
v_perm_b32 v117, v174, v170, s41
v_perm_b32 v118, v182, v178, s41
v_perm_b32 v119, v190, v186, s41
v_mfma_f32_16x16x32_bf16 v[72:75], v[84:87], v[100:103], v[72:75]
v_mov_b32_dpp v88, v48 row_shl:4 row_mask:0xf bank_mask:0x4
v_mov_b32_dpp v89, v49 row_shl:4 row_mask:0xf bank_mask:0x4
v_mfma_f32_16x16x32_bf16 v[76:79], v[84:87], v[116:119], v[76:79]
v_mov_b32_dpp v90, v50 row_shr:8 row_mask:0xf bank_mask:0x4
v_mov_b32_dpp v91, v51 row_shr:8 row_mask:0xf bank_mask:0x4
v_perm_b32 v104, v165, v161, s40
v_perm_b32 v105, v173, v169, s40
v_perm_b32 v106, v181, v177, s40
v_perm_b32 v107, v189, v185, s40
v_perm_b32 v120, v167, v163, s40
v_perm_b32 v121, v175, v171, s40
v_perm_b32 v122, v183, v179, s40
v_perm_b32 v123, v191, v187, s40
v_mfma_f32_16x16x32_bf16 v[72:75], v[88:91], v[104:107], v[72:75]
v_mov_b32_dpp v92, v48 quad_perm:[0,1,2,3] row_mask:0xf bank_mask:0x8
v_mov_b32_dpp v93, v49 quad_perm:[0,1,2,3] row_mask:0xf bank_mask:0x8
v_mfma_f32_16x16x32_bf16 v[76:79], v[88:91], v[120:123], v[76:79]
v_mov_b32_dpp v94, v50 row_shr:12 row_mask:0xf bank_mask:0x8
v_mov_b32_dpp v95, v51 row_shr:12 row_mask:0xf bank_mask:0x8
v_perm_b32 v108, v165, v161, s41
v_perm_b32 v109, v173, v169, s41
v_perm_b32 v110, v181, v177, s41
v_perm_b32 v111, v189, v185, s41
v_perm_b32 v124, v167, v163, s41
v_perm_b32 v125, v175, v171, s41
v_perm_b32 v126, v183, v179, s41
v_perm_b32 v127, v191, v187, s41
v_mfma_f32_16x16x32_bf16 v[72:75], v[92:95], v[108:111], v[72:75]
s_nop 0
v_mfma_f32_16x16x32_bf16 v[76:79], v[92:95], v[124:127], v[76:79]
s_waitcnt vmcnt(10)
s_cmp_lg_u32 s13, 0
s_cbranch_scc1 Lqdo_23_%=
s_mov_b32 m0, 147456
s_nop 0
buffer_load_dwordx4 v52, s[24:27], s43 offen sc1 lds
buffer_load_dwordx4 v52, s[24:27], s43 offen offset:1024 sc1 lds
s_branch Lqdj_24_%=
Lqdo_23_%=:
buffer_load_dword v61, v0, s[24:27], 0 offen
buffer_load_dword v61, v0, s[24:27], 0 offen
Lqdj_24_%=:
ds_read_b64_tr_b16 v[96:97], v14 offset:0
v_mov_b32_dpp v80, v48 quad_perm:[0,1,2,3] row_mask:0xf bank_mask:0x1
ds_read_b64_tr_b16 v[98:99], v14 offset:4096
v_mov_b32_dpp v81, v49 quad_perm:[0,1,2,3] row_mask:0xf bank_mask:0x1
ds_read_b64_tr_b16 v[100:101], v15 offset:0
v_mov_b32_dpp v82, v48 row_shl:4 row_mask:0xf bank_mask:0x1
ds_read_b64_tr_b16 v[102:103], v15 offset:4096
v_mov_b32_dpp v83, v49 row_shl:4 row_mask:0xf bank_mask:0x1
ds_read_b64_tr_b16 v[104:105], v16 offset:0
v_mov_b32_dpp v84, v48 row_shr:4 row_mask:0xf bank_mask:0x2
ds_read_b64_tr_b16 v[106:107], v16 offset:4096
v_mov_b32_dpp v85, v49 row_shr:4 row_mask:0xf bank_mask:0x2
ds_read_b64_tr_b16 v[108:109], v17 offset:0
v_mov_b32_dpp v86, v48 quad_perm:[0,1,2,3] row_mask:0xf bank_mask:0x2
ds_read_b64_tr_b16 v[110:111], v17 offset:4096
v_mov_b32_dpp v87, v49 quad_perm:[0,1,2,3] row_mask:0xf bank_mask:0x2
ds_read_b64_tr_b16 v[112:113], v18 offset:0
v_mov_b32_dpp v88, v48 row_shr:8 row_mask:0xf bank_mask:0x4
ds_read_b64_tr_b16 v[114:115], v18 offset:4096
v_mov_b32_dpp v89, v49 row_shr:8 row_mask:0xf bank_mask:0x4
ds_read_b64_tr_b16 v[116:117], v19 offset:0
v_mov_b32_dpp v90, v48 row_shr:4 row_mask:0xf bank_mask:0x4
ds_read_b64_tr_b16 v[118:119], v19 offset:4096
v_mov_b32_dpp v91, v49 row_shr:4 row_mask:0xf bank_mask:0x4
ds_read_b64_tr_b16 v[120:121], v20 offset:0
v_mov_b32_dpp v92, v48 row_shr:12 row_mask:0xf bank_mask:0x8
ds_read_b64_tr_b16 v[122:123], v20 offset:4096
v_mov_b32_dpp v93, v49 row_shr:12 row_mask:0xf bank_mask:0x8
ds_read_b64_tr_b16 v[124:125], v21 offset:0
v_mov_b32_dpp v94, v48 row_shr:8 row_mask:0xf bank_mask:0x8
ds_read_b64_tr_b16 v[126:127], v21 offset:4096
v_mov_b32_dpp v95, v49 row_shr:8 row_mask:0xf bank_mask:0x8
ds_read_b64_tr_b16 v[128:129], v14 offset:8192
s_waitcnt lgkmcnt(14)
v_mfma_f32_16x16x32_bf16 v[64:67], v[80:83], v[96:99], 0
ds_read_b64_tr_b16 v[130:131], v15 offset:8192
v_add_u32 v29, s35, v11
ds_read_b64_tr_b16 v[132:133], v16 offset:8192
s_waitcnt lgkmcnt(14)
v_mfma_f32_16x16x32_bf16 v[64:67], v[84:87], v[100:103], v[64:67]
ds_read_b64_tr_b16 v[134:135], v17 offset:8192
ds_read_b64_tr_b16 v[136:137], v18 offset:8192
s_waitcnt lgkmcnt(14)
v_mfma_f32_16x16x32_bf16 v[64:67], v[88:91], v[104:107], v[64:67]
ds_read_b64_tr_b16 v[138:139], v19 offset:8192
ds_read_b64_tr_b16 v[140:141], v20 offset:8192
s_waitcnt lgkmcnt(13)
v_mfma_f32_16x16x32_bf16 v[68:71], v[80:83], v[112:115], 0
ds_read_b64_tr_b16 v[142:143], v21 offset:8192
v_mov_b32_dpp v80, v48 row_shl:8 row_mask:0xf bank_mask:0x1
s_waitcnt lgkmcnt(12)
v_mfma_f32_16x16x32_bf16 v[68:71], v[84:87], v[116:119], v[68:71]
v_mov_b32_dpp v81, v49 row_shl:8 row_mask:0xf bank_mask:0x1
v_mov_b32_dpp v84, v48 row_shl:4 row_mask:0xf bank_mask:0x2
s_waitcnt lgkmcnt(10)
v_mfma_f32_16x16x32_bf16 v[68:71], v[88:91], v[120:123], v[68:71]
v_mov_b32_dpp v85, v49 row_shl:4 row_mask:0xf bank_mask:0x2
v_mov_b32_dpp v88, v48 quad_perm:[0,1,2,3] row_mask:0xf bank_mask:0x4
v_mfma_f32_16x16x32_bf16 v[64:67], v[92:95], v[108:111], v[64:67]
v_mov_b32_dpp v89, v49 quad_perm:[0,1,2,3] row_mask:0xf bank_mask:0x4
s_waitcnt lgkmcnt(8)
v_mfma_f32_16x16x32_bf16 v[68:71], v[92:95], v[124:127], v[68:71]
v_mov_b32_dpp v92, v48 row_shr:4 row_mask:0xf bank_mask:0x8
v_mov_b32_dpp v93, v49 row_shr:4 row_mask:0xf bank_mask:0x8
s_waitcnt lgkmcnt(7)
v_mfma_f32_16x16x16_bf16 v[64:67], v[80:81], v[128:129], v[64:67]
s_waitcnt lgkmcnt(3)
v_mfma_f32_16x16x16_bf16 v[68:71], v[80:81], v[136:137], v[68:71]
v_mfma_f32_16x16x16_bf16 v[64:67], v[84:85], v[130:131], v[64:67]
s_waitcnt lgkmcnt(2)
v_mfma_f32_16x16x16_bf16 v[68:71], v[84:85], v[138:139], v[68:71]
v_mfma_f32_16x16x16_bf16 v[64:67], v[88:89], v[132:133], v[64:67]
v_cvt_pk_bf16_f32 v132, v72, v76
v_cvt_pk_bf16_f32 v133, v73, v77
s_waitcnt lgkmcnt(1)
v_mfma_f32_16x16x16_bf16 v[68:71], v[88:89], v[140:141], v[68:71]
v_mfma_f32_16x16x16_bf16 v[64:67], v[92:93], v[134:135], v[64:67]
v_cvt_pk_bf16_f32 v134, v74, v78
v_cvt_pk_bf16_f32 v135, v75, v79
s_waitcnt lgkmcnt(0)
v_mfma_f32_16x16x16_bf16 v[68:71], v[92:93], v[142:143], v[68:71]
s_nop 7
v_cvt_pk_bf16_f32 v128, v64, v68
v_cvt_pk_bf16_f32 v129, v65, v69
v_cvt_pk_bf16_f32 v130, v66, v70
v_cvt_pk_bf16_f32 v131, v67, v71
ds_write2st64_b32 v10, v128, v129 offset0:0 offset1:1
ds_write2st64_b32 v10, v132, v133 offset0:4 offset1:5
ds_write2st64_b32 v10, v130, v131 offset0:2 offset1:3
ds_write2st64_b32 v10, v134, v135 offset0:6 offset1:7
ds_write_b64 v29, v[22:23] offset:2048
s_waitcnt lgkmcnt(0)
s_barrier
v_add_u32 v130, 0x25800, v13
ds_read_b64 v[96:97], v130 offset:0
v_add_u32 v128, 0x25800, v12
ds_read_b64 v[98:99], v130 offset:2176
v_add_u32 v129, 0x25800, v24
ds_read_b64 v[100:101], v130 offset:4352
v_mov_b32 v137, s12
ds_read_b64 v[102:103], v130 offset:6528
v_mov_b32 v139, s12
ds_read_b32 v104, v128 offset:0
ds_read2_b32 v[108:109], v129 offset0:0 offset1:8
ds_read_b32 v105, v128 offset:2176
ds_read_b32 v110, v129 offset:2176
ds_read_b32 v111, v129 offset:2208
ds_read_b32 v106, v128 offset:4352
ds_read_b32 v112, v129 offset:4352
ds_read_b32 v113, v129 offset:4384
ds_read_b32 v107, v128 offset:6528
ds_read_b32 v114, v129 offset:6528
ds_read_b32 v115, v129 offset:6560
s_waitcnt lgkmcnt(12)
v_max3_f32 v26, v96, v98, v100
s_waitcnt lgkmcnt(11)
v_max_f32 v26, v26, v102
v_sub_f32 v96, v96, v26
v_sub_f32 v98, v98, v26
v_exp_f32 v96, v96
v_sub_f32 v100, v100, v26
v_exp_f32 v98, v98
s_waitcnt lgkmcnt(10)
v_lshlrev_b32 v116, 16, v104
v_and_b32 v117, 0xffff0000, v104
s_waitcnt lgkmcnt(9)
v_perm_b32 v118, v108, v108, v25
v_perm_b32 v119, v109, v109, v25
v_sub_f32 v102, v102, v26
v_exp_f32 v100, v100
v_pk_add_f32 v[116:117], v[116:117], v[118:119]
s_waitcnt lgkmcnt(8)
v_lshlrev_b32 v120, 16, v105
v_and_b32 v121, 0xffff0000, v105
s_waitcnt lgkmcnt(7)
v_perm_b32 v122, v110, v110, v25
s_waitcnt lgkmcnt(6)
v_perm_b32 v123, v111, v111, v25
v_exp_f32 v102, v102
v_pk_mul_f32 v[134:135], v[116:117], v[96:97] op_sel_hi:[1,0]
v_pk_add_f32 v[120:121], v[120:121], v[122:123]
s_waitcnt lgkmcnt(5)
v_lshlrev_b32 v116, 16, v106
v_and_b32 v117, 0xffff0000, v106
s_waitcnt lgkmcnt(4)
v_perm_b32 v118, v112, v112, v25
s_waitcnt lgkmcnt(3)
v_perm_b32 v119, v113, v113, v25
v_pk_fma_f32 v[134:135], v[120:121], v[98:99], v[134:135] op_sel_hi:[1,0,1]
v_pk_add_f32 v[116:117], v[116:117], v[118:119]
s_waitcnt lgkmcnt(2)
v_lshlrev_b32 v120, 16, v107
v_and_b32 v121, 0xffff0000, v107
s_waitcnt lgkmcnt(1)
v_perm_b32 v122, v114, v114, v25
s_waitcnt lgkmcnt(0)
v_perm_b32 v123, v115, v115, v25
v_pk_fma_f32 v[134:135], v[116:117], v[100:101], v[134:135] op_sel_hi:[1,0,1]
v_pk_add_f32 v[120:121], v[120:121], v[122:123]
v_pk_fma_f32 v[134:135], v[120:121], v[102:103], v[134:135] op_sel_hi:[1,0,1]
v_mul_f32 v27, v96, v97
v_cvt_pk_bf16_f32 v136, v134, v135
v_fma_f32 v27, v98, v99, v27
v_fma_f32 v27, v100, v101, v27
v_fma_f32 v27, v102, v103, v27
v_mov_b32_dpp v138, v136 quad_perm:[1,0,3,2] row_mask:0xf bank_mask:0xf bound_ctrl:1
v_cndmask_b32 v136, v136, v26, s[56:57]
v_cndmask_b32 v138, v138, v27, s[56:57]
s_mov_b32 exec_lo, 0x55555557
s_mov_b32 exec_hi, 0x55555555
buffer_store_dwordx4 v[136:139], v6, s[24:27], 0 offen sc1
s_mov_b64 exec, -1
s_nop 0
v_add_u32 v6, v6, v8
s_waitcnt vmcnt(1)
s_barrier
s_mov_b32 s39, 0
Lqchk_25_%=:
ds_read_b128 v[96:99], v60 offset:0
ds_read_b128 v[100:103], v60 offset:512
ds_read_b128 v[104:107], v60 offset:1024
ds_read_b128 v[108:111], v60 offset:1536
s_waitcnt lgkmcnt(0)
v_bitop3_b32 v26, v97, v99, s12 bitop3:0x7e
v_bitop3_b32 v27, v101, v103, s12 bitop3:0x7e
v_bitop3_b32 v28, v105, v107, s12 bitop3:0x7e
v_bitop3_b32 v29, v109, v111, s12 bitop3:0x7e
v_or3_b32 v26, v26, v27, v28
v_or_b32 v26, v26, v29
v_cmp_ne_u32 vcc, 0, v26
v_mov_b32 v120, v96
v_mov_b32 v121, v98
v_mov_b32 v122, v100
v_mov_b32 v123, v102
v_mov_b32 v124, v104
v_mov_b32 v125, v106
v_mov_b32 v126, v108
v_mov_b32 v127, v110
s_cbranch_vccnz Lqslow_26_%=
Lqcont_27_%=:
s_add_u32 s43, s43, 0x4000
v_mfma_f32_16x16x32_bf16 v[128:131], a[80:83], v[120:123], 0
buffer_load_dwordx4 a[80:83], v1, s[16:19], s30 offen nt
v_perm_b32 v104, v197, v193, s40
v_mfma_f32_16x16x32_bf16 v[132:135], a[88:91], v[120:123], 0
v_perm_b32 v105, v205, v201, s40
v_perm_b32 v106, v213, v209, s40
v_mfma_f32_16x16x32_bf16 v[136:139], a[96:99], v[120:123], 0
v_perm_b32 v107, v221, v217, s40
v_perm_b32 v108, v197, v193, s41
v_mfma_f32_16x16x32_bf16 v[140:143], a[104:107], v[120:123], 0
v_perm_b32 v109, v205, v201, s41
v_perm_b32 v110, v213, v209, s41
v_mfma_f32_16x16x32_bf16 v[128:131], a[84:87], v[124:127], v[128:131]
buffer_load_dwordx4 a[84:87], v1, s[16:19], s30 offen offset:128 nt
buffer_load_dwordx4 a[88:91], v1, s[16:19], s30 offen offset:2048 nt
v_mfma_f32_16x16x32_bf16 v[132:135], a[92:95], v[124:127], v[132:135]
buffer_load_dwordx4 a[92:95], v1, s[16:19], s30 offen offset:2176 nt
buffer_load_dwordx4 a[96:99], v1, s[16:19], s31 offen nt
v_mfma_f32_16x16x32_bf16 v[136:139], a[100:103], v[124:127], v[136:139]
buffer_load_dwordx4 a[100:103], v1, s[16:19], s31 offen offset:128 nt
buffer_load_dwordx4 a[104:107], v1, s[16:19], s31 offen offset:2048 nt
v_mfma_f32_16x16x32_bf16 v[140:143], a[108:111], v[124:127], v[140:143]
buffer_load_dwordx4 a[108:111], v1, s[16:19], s31 offen offset:2176 nt
v_perm_b32 v111, v221, v217, s41
v_mfma_f32_16x16x32_bf16 v[96:99], a[112:115], v[120:123], 0
v_permlane32_swap_b32 v128, v132
v_permlane32_swap_b32 v129, v133
v_permlane32_swap_b32 v130, v134
v_permlane32_swap_b32 v131, v135
v_mfma_f32_16x16x32_bf16 v[100:103], a[120:123], v[120:123], 0
v_add_f32_dpp v40, v132, v128 row_shl:4 row_mask:0xf bank_mask:0x1
v_add_f32_dpp v41, v133, v129 row_shl:4 row_mask:0xf bank_mask:0x1
v_add_f32_dpp v42, v134, v130 row_shl:4 row_mask:0xf bank_mask:0x1
v_add_f32_dpp v43, v135, v131 row_shl:4 row_mask:0xf bank_mask:0x1
v_permlane32_swap_b32 v136, v140
v_permlane32_swap_b32 v137, v141
v_permlane32_swap_b32 v138, v142
v_permlane32_swap_b32 v139, v143
v_mfma_f32_16x16x32_bf16 v[128:131], a[128:131], v[120:123], v[56:59]
v_add_f32_dpp v40, v136, v140 row_shr:4 row_mask:0xf bank_mask:0x2
v_add_f32_dpp v41, v137, v141 row_shr:4 row_mask:0xf bank_mask:0x2
v_mfma_f32_16x16x32_bf16 v[132:135], a[136:139], v[120:123], v[56:59]
v_add_f32_dpp v42, v138, v142 row_shr:4 row_mask:0xf bank_mask:0x2
v_add_f32_dpp v43, v139, v143 row_shr:4 row_mask:0xf bank_mask:0x2
v_mfma_f32_16x16x32_bf16 v[96:99], a[116:119], v[124:127], v[96:99]
v_perm_b32 v112, v198, v194, s40
v_perm_b32 v113, v206, v202, s40
v_mfma_f32_16x16x32_bf16 v[100:103], a[124:127], v[124:127], v[100:103]
v_perm_b32 v114, v214, v210, s40
v_perm_b32 v115, v222, v218, s40
v_mfma_f32_16x16x32_bf16 v[136:139], a[144:147], v[120:123], v[56:59]
v_perm_b32 v116, v198, v194, s41
v_perm_b32 v117, v206, v202, s41
v_mfma_f32_16x16x32_bf16 v[140:143], a[152:155], v[120:123], v[56:59]
v_perm_b32 v118, v214, v210, s41
v_perm_b32 v119, v222, v218, s41
v_mfma_f32_16x16x32_bf16 v[128:131], a[132:135], v[124:127], v[128:131]
v_permlane32_swap_b32 v96, v100
v_permlane32_swap_b32 v97, v101
v_mfma_f32_16x16x32_bf16 v[132:135], a[140:143], v[124:127], v[132:135]
v_permlane32_swap_b32 v98, v102
v_permlane32_swap_b32 v99, v103
v_mfma_f32_16x16x32_bf16 v[136:139], a[148:151], v[124:127], v[136:139]
v_add_f32_dpp v40, v100, v96 row_shl:4 row_mask:0xf bank_mask:0x4
v_add_f32_dpp v41, v101, v97 row_shl:4 row_mask:0xf bank_mask:0x4
v_mfma_f32_16x16x32_bf16 v[140:143], a[156:159], v[124:127], v[140:143]
v_add_f32_dpp v42, v102, v98 row_shl:4 row_mask:0xf bank_mask:0x4
v_add_f32_dpp v43, v103, v99 row_shl:4 row_mask:0xf bank_mask:0x4
v_perm_b32 v96, v196, v192, s40
v_permlane32_swap_b32 v128, v132
v_permlane32_swap_b32 v129, v133
v_permlane32_swap_b32 v130, v134
v_permlane32_swap_b32 v131, v135
v_add_f32_dpp v40, v128, v132 row_shr:4 row_mask:0xf bank_mask:0x8
v_add_f32_dpp v41, v129, v133 row_shr:4 row_mask:0xf bank_mask:0x8
v_add_f32_dpp v42, v130, v134 row_shr:4 row_mask:0xf bank_mask:0x8
v_add_f32_dpp v43, v131, v135 row_shr:4 row_mask:0xf bank_mask:0x8
v_permlane32_swap_b32 v136, v140
v_permlane32_swap_b32 v137, v141
v_permlane32_swap_b32 v138, v142
v_permlane32_swap_b32 v139, v143
v_add_f32_dpp v44, v140, v136 row_shl:4 row_mask:0xf bank_mask:0x1
v_add_f32_dpp v45, v141, v137 row_shl:4 row_mask:0xf bank_mask:0x1
v_add_f32_dpp v46, v142, v138 row_shl:4 row_mask:0xf bank_mask:0x1
v_add_f32_dpp v47, v143, v139 row_shl:4 row_mask:0xf bank_mask:0x1
v_max3_f32 v26, v40, v41, v42
v_max3_f32 v27, v43, v44, v45
v_max3_f32 v26, v26, v46, v47
v_max_f32 v26, v26, v27
v_perm_b32 v97, v204, v200, s40
v_perm_b32 v98, v212, v208, s40
v_max_f32_dpp v26, v26, v26 row_ror:4 row_mask:0xf bank_mask:0xf bound_ctrl:1
v_perm_b32 v99, v220, v216, s40
v_perm_b32 v100, v196, v192, s41
v_max_f32_dpp v26, v26, v26 row_ror:8 row_mask:0xf bank_mask:0xf bound_ctrl:1
v_mov_b32 v27, v26
v_perm_b32 v101, v204, v200, s41
v_perm_b32 v102, v212, v208, s41
v_permlane16_swap_b32 v26, v27
v_max_f32 v26, v26, v27
v_mov_b32 v27, v26
v_perm_b32 v103, v220, v216, s41
v_perm_b32 v120, v199, v195, s40
v_permlane32_swap_b32 v26, v27
v_max_f32 v26, v26, v27
v_mul_f32 v22, s34, v26
v_fma_f32 v128, v40, s34, -v22
v_fma_f32 v129, v41, s34, -v22
v_fma_f32 v130, v42, s34, -v22
v_fma_f32 v131, v43, s34, -v22
v_fma_f32 v132, v44, s34, -v22
v_fma_f32 v133, v45, s34, -v22
v_fma_f32 v134, v46, s34, -v22
v_fma_f32 v135, v47, s34, -v22
v_exp_f32 v128, v128
v_exp_f32 v129, v129
v_exp_f32 v130, v130
v_exp_f32 v131, v131
v_exp_f32 v132, v132
v_exp_f32 v133, v133
v_exp_f32 v134, v134
v_exp_f32 v135, v135
v_cvt_pk_bf16_f32 v48, v128, v129
v_cvt_pk_bf16_f32 v49, v130, v131
v_cvt_pk_bf16_f32 v50, v132, v133
v_cvt_pk_bf16_f32 v51, v134, v135
v_mov_b32_dpp v80, v48 row_shl:12 row_mask:0xf bank_mask:0x1
v_mov_b32_dpp v81, v49 row_shl:12 row_mask:0xf bank_mask:0x1
v_mov_b32_dpp v82, v50 quad_perm:[0,1,2,3] row_mask:0xf bank_mask:0x1
v_mov_b32_dpp v83, v51 quad_perm:[0,1,2,3] row_mask:0xf bank_mask:0x1
v_mov_b32_dpp v84, v48 row_shl:8 row_mask:0xf bank_mask:0x2
v_mov_b32_dpp v85, v49 row_shl:8 row_mask:0xf bank_mask:0x2
v_mfma_f32_16x16x32_bf16 v[72:75], v[80:83], v[96:99], 0
v_mov_b32_dpp v86, v50 row_shr:4 row_mask:0xf bank_mask:0x2
v_mov_b32_dpp v87, v51 row_shr:4 row_mask:0xf bank_mask:0x2
v_mov_b32_dpp v88, v48 row_shl:4 row_mask:0xf bank_mask:0x4
v_mov_b32_dpp v89, v49 row_shl:4 row_mask:0xf bank_mask:0x4
v_mfma_f32_16x16x32_bf16 v[72:75], v[84:87], v[100:103], v[72:75]
v_mov_b32_dpp v90, v50 row_shr:8 row_mask:0xf bank_mask:0x4
v_mov_b32_dpp v91, v51 row_shr:8 row_mask:0xf bank_mask:0x4
v_mov_b32_dpp v92, v48 quad_perm:[0,1,2,3] row_mask:0xf bank_mask:0x8
v_mov_b32_dpp v93, v49 quad_perm:[0,1,2,3] row_mask:0xf bank_mask:0x8
v_mfma_f32_16x16x32_bf16 v[72:75], v[88:91], v[104:107], v[72:75]
v_mov_b32_dpp v94, v50 row_shr:12 row_mask:0xf bank_mask:0x8
v_mov_b32_dpp v95, v51 row_shr:12 row_mask:0xf bank_mask:0x8
v_mfma_f32_16x16x32_bf16 v[76:79], v[80:83], v[112:115], 0
ds_read_b64_tr_b16 v[96:97], v14 offset:12288
v_perm_b32 v121, v207, v203, s40
v_mfma_f32_16x16x32_bf16 v[72:75], v[92:95], v[108:111], v[72:75]
ds_read_b64_tr_b16 v[98:99], v14 offset:16384
v_perm_b32 v122, v215, v211, s40
ds_read_b64_tr_b16 v[100:101], v15 offset:12288
v_mfma_f32_16x16x32_bf16 v[76:79], v[84:87], v[116:119], v[76:79]
ds_read_b64_tr_b16 v[102:103], v15 offset:16384
v_perm_b32 v123, v223, v219, s40
ds_read_b64_tr_b16 v[104:105], v16 offset:12288
v_perm_b32 v124, v199, v195, s41
ds_read_b64_tr_b16 v[106:107], v16 offset:16384
v_mfma_f32_16x16x32_bf16 v[76:79], v[88:91], v[120:123], v[76:79]
ds_read_b64_tr_b16 v[108:109], v17 offset:12288
v_perm_b32 v125, v207, v203, s41
ds_read_b64_tr_b16 v[110:111], v17 offset:16384
v_perm_b32 v126, v215, v211, s41
ds_read_b64_tr_b16 v[112:113], v18 offset:12288
v_perm_b32 v127, v223, v219, s41
ds_read_b64_tr_b16 v[114:115], v18 offset:16384
v_add_f32 v26, v128, v129
ds_read_b64_tr_b16 v[116:117], v19 offset:12288
v_mfma_f32_16x16x32_bf16 v[76:79], v[92:95], v[124:127], v[76:79]
ds_read_b64_tr_b16 v[118:119], v19 offset:16384
v_add_f32 v27, v130, v131
ds_read_b64_tr_b16 v[120:121], v20 offset:12288
v_add_f32 v28, v132, v133
ds_read_b64_tr_b16 v[122:123], v20 offset:16384
v_add_f32 v29, v134, v135
ds_read_b64_tr_b16 v[124:125], v21 offset:12288
v_mov_b32_dpp v80, v48 quad_perm:[0,1,2,3] row_mask:0xf bank_mask:0x1
ds_read_b64_tr_b16 v[126:127], v21 offset:16384
v_mov_b32_dpp v81, v49 quad_perm:[0,1,2,3] row_mask:0xf bank_mask:0x1
ds_read_b64_tr_b16 v[128:129], v14 offset:20480
v_mov_b32_dpp v82, v48 row_shl:4 row_mask:0xf bank_mask:0x1
ds_read_b64_tr_b16 v[130:131], v15 offset:20480
v_mov_b32_dpp v83, v49 row_shl:4 row_mask:0xf bank_mask:0x1
ds_read_b64_tr_b16 v[132:133], v16 offset:20480
v_mov_b32_dpp v84, v48 row_shr:4 row_mask:0xf bank_mask:0x2
ds_read_b64_tr_b16 v[134:135], v17 offset:20480
s_waitcnt lgkmcnt(14)
v_mfma_f32_16x16x32_bf16 v[64:67], v[80:83], v[96:99], 0
ds_read_b64_tr_b16 v[136:137], v18 offset:20480
v_mov_b32_dpp v85, v49 row_shr:4 row_mask:0xf bank_mask:0x2
ds_read_b64_tr_b16 v[138:139], v19 offset:20480
v_mov_b32_dpp v86, v48 quad_perm:[0,1,2,3] row_mask:0xf bank_mask:0x2
ds_read_b64_tr_b16 v[140:141], v20 offset:20480
v_mov_b32_dpp v87, v49 quad_perm:[0,1,2,3] row_mask:0xf bank_mask:0x2
ds_read_b64_tr_b16 v[142:143], v21 offset:20480
s_waitcnt lgkmcnt(14)
v_mfma_f32_16x16x32_bf16 v[68:71], v[80:83], v[112:115], 0
v_mov_b32_dpp v88, v48 row_shr:8 row_mask:0xf bank_mask:0x4
v_mov_b32_dpp v89, v49 row_shr:8 row_mask:0xf bank_mask:0x4
v_mfma_f32_16x16x32_bf16 v[64:67], v[84:87], v[100:103], v[64:67]
v_mov_b32_dpp v90, v48 row_shr:4 row_mask:0xf bank_mask:0x4
v_mov_b32_dpp v91, v49 row_shr:4 row_mask:0xf bank_mask:0x4
s_waitcnt lgkmcnt(12)
v_mfma_f32_16x16x32_bf16 v[68:71], v[84:87], v[116:119], v[68:71]
v_mov_b32_dpp v92, v48 row_shr:12 row_mask:0xf bank_mask:0x8
v_mov_b32_dpp v93, v49 row_shr:12 row_mask:0xf bank_mask:0x8
v_mfma_f32_16x16x32_bf16 v[64:67], v[88:91], v[104:107], v[64:67]
v_mov_b32_dpp v94, v48 row_shr:8 row_mask:0xf bank_mask:0x8
v_mov_b32_dpp v95, v49 row_shr:8 row_mask:0xf bank_mask:0x8
s_waitcnt lgkmcnt(10)
v_mfma_f32_16x16x32_bf16 v[68:71], v[88:91], v[120:123], v[68:71]
v_mov_b32_dpp v80, v48 row_shl:8 row_mask:0xf bank_mask:0x1
v_mov_b32_dpp v81, v49 row_shl:8 row_mask:0xf bank_mask:0x1
v_mfma_f32_16x16x32_bf16 v[64:67], v[92:95], v[108:111], v[64:67]
v_mov_b32_dpp v84, v48 row_shl:4 row_mask:0xf bank_mask:0x2
v_mov_b32_dpp v85, v49 row_shl:4 row_mask:0xf bank_mask:0x2
s_waitcnt lgkmcnt(8)
v_mfma_f32_16x16x32_bf16 v[68:71], v[92:95], v[124:127], v[68:71]
v_mov_b32_dpp v88, v48 quad_perm:[0,1,2,3] row_mask:0xf bank_mask:0x4
v_mov_b32_dpp v89, v49 quad_perm:[0,1,2,3] row_mask:0xf bank_mask:0x4
s_waitcnt lgkmcnt(7)
v_mfma_f32_16x16x16_bf16 v[64:67], v[80:81], v[128:129], v[64:67]
v_add_f32 v26, v26, v27
v_add_f32 v28, v28, v29
s_waitcnt lgkmcnt(3)
v_mfma_f32_16x16x16_bf16 v[68:71], v[80:81], v[136:137], v[68:71]
v_add_f32 v26, v26, v28
v_mov_b32_dpp v92, v48 row_shr:4 row_mask:0xf bank_mask:0x8
v_mfma_f32_16x16x16_bf16 v[64:67], v[84:85], v[130:131], v[64:67]
v_mov_b32_dpp v93, v49 row_shr:4 row_mask:0xf bank_mask:0x8
v_add_f32_dpp v26, v26, v26 row_ror:4 row_mask:0xf bank_mask:0xf bound_ctrl:1
s_waitcnt lgkmcnt(2)
v_mfma_f32_16x16x16_bf16 v[68:71], v[84:85], v[138:139], v[68:71]
v_add_f32_dpp v26, v26, v26 row_ror:8 row_mask:0xf bank_mask:0xf bound_ctrl:1
v_mov_b32 v27, v26
v_mfma_f32_16x16x16_bf16 v[64:67], v[88:89], v[132:133], v[64:67]
s_nop 0
v_permlane16_swap_b32 v26, v27
v_add_f32 v26, v26, v27
s_waitcnt lgkmcnt(1)
v_mfma_f32_16x16x16_bf16 v[68:71], v[88:89], v[140:141], v[68:71]
v_mov_b32 v27, v26
v_cvt_pk_bf16_f32 v132, v72, v76
v_mfma_f32_16x16x16_bf16 v[64:67], v[92:93], v[134:135], v[64:67]
v_permlane32_swap_b32 v26, v27
v_add_f32 v26, v26, v27
s_waitcnt lgkmcnt(0)
v_mfma_f32_16x16x16_bf16 v[68:71], v[92:93], v[142:143], v[68:71]
v_mov_b32 v23, v26
v_cvt_pk_bf16_f32 v133, v73, v77
v_cvt_pk_bf16_f32 v134, v74, v78
v_cvt_pk_bf16_f32 v135, v75, v79
s_nop 3
v_cvt_pk_bf16_f32 v128, v64, v68
v_cvt_pk_bf16_f32 v129, v65, v69
v_cvt_pk_bf16_f32 v130, v66, v70
v_cvt_pk_bf16_f32 v131, v67, v71
ds_write2st64_b32 v9, v128, v129 offset0:64 offset1:65
ds_write2st64_b32 v9, v132, v133 offset0:68 offset1:69
ds_write2st64_b32 v9, v130, v131 offset0:66 offset1:67
ds_write2st64_b32 v9, v134, v135 offset0:70 offset1:71
ds_write_b64 v11, v[22:23] offset:18432
buffer_load_dwordx4 a[112:115], v1, s[16:19], s32 offen nt
buffer_load_dwordx4 a[116:119], v1, s[16:19], s32 offen offset:128 nt
buffer_load_dwordx4 a[120:123], v1, s[16:19], s32 offen offset:2048 nt
buffer_load_dwordx4 a[124:127], v1, s[16:19], s32 offen offset:2176 nt
s_add_u32 s20, s20, 0x1000000
s_addc_u32 s21, s21, 0
s_add_u32 s38, s37, 0x3000
s_mov_b32 m0, s38
s_nop 0
buffer_load_dwordx4 v3, s[20:23], s28 offen  lds
buffer_load_dwordx4 v4, s[20:23], s28 offen offset:1024  lds
buffer_load_dwordx4 v3, s[20:23], s28 offen offset:2048  lds
buffer_load_dwordx4 v4, s[20:23], s28 offen offset:3072  lds
s_waitcnt lgkmcnt(0)
s_barrier
v_add_u32 v130, 0x4000, v13
v_add_u32 v133, 0x16000, v13
ds_read_b64 v[96:97], v130 offset:0
v_add_u32 v128, 0x4000, v12
ds_read_b64 v[98:99], v130 offset:36864
v_add_u32 v129, 0x4000, v24
ds_read_b64 v[100:101], v133 offset:0
v_add_u32 v131, 0x16000, v12
ds_read_b64 v[102:103], v133 offset:36864
v_add_u32 v132, 0x16000, v24
ds_read_b32 v104, v128 offset:0
v_mov_b32 v137, s12
ds_read2_b32 v[108:109], v129 offset0:0 offset1:8
v_mov_b32 v139, s12
ds_read_b32 v105, v128 offset:36864
ds_read_b32 v110, v129 offset:36864
ds_read_b32 v111, v129 offset:36896
ds_read_b32 v106, v131 offset:0
ds_read2_b32 v[112:113], v132 offset0:0 offset1:8
ds_read_b32 v107, v131 offset:36864
ds_read_b32 v114, v132 offset:36864
ds_read_b32 v115, v132 offset:36896
s_waitcnt lgkmcnt(11)
v_max3_f32 v26, v96, v98, v100
s_waitcnt lgkmcnt(10)
v_max_f32 v26, v26, v102
v_sub_f32 v96, v96, v26
v_sub_f32 v98, v98, v26
v_exp_f32 v96, v96
v_sub_f32 v100, v100, v26
v_exp_f32 v98, v98
s_waitcnt lgkmcnt(9)
v_lshlrev_b32 v116, 16, v104
v_and_b32 v117, 0xffff0000, v104
s_waitcnt lgkmcnt(8)
v_perm_b32 v118, v108, v108, v25
v_perm_b32 v119, v109, v109, v25
v_sub_f32 v102, v102, v26
v_exp_f32 v100, v100
v_pk_add_f32 v[116:117], v[116:117], v[118:119]
s_waitcnt lgkmcnt(7)
v_lshlrev_b32 v120, 16, v105
v_and_b32 v121, 0xffff0000, v105
s_waitcnt lgkmcnt(6)
v_perm_b32 v122, v110, v110, v25
s_waitcnt lgkmcnt(5)
v_perm_b32 v123, v111, v111, v25
v_exp_f32 v102, v102
v_pk_mul_f32 v[134:135], v[116:117], v[96:97] op_sel_hi:[1,0]
v_pk_add_f32 v[120:121], v[120:121], v[122:123]
s_waitcnt lgkmcnt(4)
v_lshlrev_b32 v116, 16, v106
v_and_b32 v117, 0xffff0000, v106
s_waitcnt lgkmcnt(3)
v_perm_b32 v118, v112, v112, v25
v_perm_b32 v119, v113, v113, v25
v_pk_fma_f32 v[134:135], v[120:121], v[98:99], v[134:135] op_sel_hi:[1,0,1]
v_pk_add_f32 v[116:117], v[116:117], v[118:119]
s_waitcnt lgkmcnt(2)
v_lshlrev_b32 v120, 16, v107
v_and_b32 v121, 0xffff0000, v107
s_waitcnt lgkmcnt(1)
v_perm_b32 v122, v114, v114, v25
s_waitcnt lgkmcnt(0)
v_perm_b32 v123, v115, v115, v25
v_pk_fma_f32 v[134:135], v[116:117], v[100:101], v[134:135] op_sel_hi:[1,0,1]
v_pk_add_f32 v[120:121], v[120:121], v[122:123]
v_pk_fma_f32 v[134:135], v[120:121], v[102:103], v[134:135] op_sel_hi:[1,0,1]
v_mul_f32 v27, v96, v97
v_cvt_pk_bf16_f32 v136, v134, v135
v_fma_f32 v27, v98, v99, v27
v_fma_f32 v27, v100, v101, v27
v_fma_f32 v27, v102, v103, v27
v_mov_b32_dpp v138, v136 quad_perm:[1,0,3,2] row_mask:0xf bank_mask:0xf bound_ctrl:1
v_cndmask_b32 v136, v136, v26, s[56:57]
v_cndmask_b32 v138, v138, v27, s[56:57]
s_mov_b32 exec_lo, 0x55555557
s_mov_b32 exec_hi, 0x55555555
buffer_store_dwordx4 v[136:139], v7, s[24:27], 0 offen sc1
s_mov_b64 exec, -1
s_nop 0
v_add_u32 v7, v7, v8
s_mov_b32 s39, 0
Lqchk_31_%=:
ds_read_b128 v[96:99], v60 offset:2048
ds_read_b128 v[100:103], v60 offset:2560
ds_read_b128 v[104:107], v60 offset:3072
ds_read_b128 v[108:111], v60 offset:3584
s_waitcnt lgkmcnt(0)
v_bitop3_b32 v26, v97, v99, s12 bitop3:0x7e
v_bitop3_b32 v27, v101, v103, s12 bitop3:0x7e
v_bitop3_b32 v28, v105, v107, s12 bitop3:0x7e
v_bitop3_b32 v29, v109, v111, s12 bitop3:0x7e
v_or3_b32 v26, v26, v27, v28
v_or_b32 v26, v26, v29
v_cmp_ne_u32 vcc, 0, v26
v_mov_b32 v32, v96
v_mov_b32 v33, v98
v_mov_b32 v34, v100
v_mov_b32 v35, v102
v_mov_b32 v36, v104
v_mov_b32 v37, v106
v_mov_b32 v38, v108
v_mov_b32 v39, v110
s_cbranch_vccnz Lqslow_32_%=
Lqcont_33_%=:
buffer_load_dwordx4 v[192:195], v2, s[20:23], s31 offen 
buffer_load_dwordx4 v[196:199], v2, s[20:23], s31 offen offset:256 
buffer_load_dwordx4 v[200:203], v2, s[20:23], s31 offen offset:512 
buffer_load_dwordx4 v[204:207], v2, s[20:23], s31 offen offset:768 
buffer_load_dwordx4 v[208:211], v2, s[20:23], s32 offen 
buffer_load_dwordx4 v[212:215], v2, s[20:23], s32 offen offset:256 
buffer_load_dwordx4 v[216:219], v2, s[20:23], s32 offen offset:512 
buffer_load_dwordx4 v[220:223], v2, s[20:23], s32 offen offset:768 
Lit4_%=:
v_mfma_f32_16x16x32_bf16 v[128:131], a[48:51], v[32:35], 0
v_mfma_f32_16x16x32_bf16 v[132:135], a[56:59], v[32:35], 0
v_mfma_f32_16x16x32_bf16 v[128:131], a[52:55], v[36:39], v[128:131]
v_mfma_f32_16x16x32_bf16 v[132:135], a[60:63], v[36:39], v[132:135]
s_nop 7
v_permlane32_swap_b32 v128, v132
v_permlane32_swap_b32 v129, v133
v_permlane32_swap_b32 v130, v134
v_permlane32_swap_b32 v131, v135
v_add_f32_dpp v40, v132, v128 row_shl:4 row_mask:0xf bank_mask:0x1
v_add_f32_dpp v41, v133, v129 row_shl:4 row_mask:0xf bank_mask:0x1
v_add_f32_dpp v42, v134, v130 row_shl:4 row_mask:0xf bank_mask:0x1
v_add_f32_dpp v43, v135, v131 row_shl:4 row_mask:0xf bank_mask:0x1
s_barrier
s_add_u32 s38, s37, 0x4000
s_mov_b32 m0, s38
s_nop 0
buffer_load_dwordx4 v3, s[20:23], s29 offen  lds
buffer_load_dwordx4 v4, s[20:23], s29 offen offset:1024  lds
buffer_load_dwordx4 v3, s[20:23], s29 offen offset:2048  lds
buffer_load_dwordx4 v4, s[20:23], s29 offen offset:3072  lds
s_add_u32 s68, s64, Lit5_%=-Lcode0_%=
s_addc_u32 s69, s65, 0
s_and_b32 s69, s69, 0xffff
s_mov_b32 s70, Lit6_%=-Lit5_%=
s_mov_b32 s71, 0x20000
buffer_load_dword v61, v62, s[68:71], 0 offen
v_mfma_f32_16x16x32_bf16 v[128:131], a[64:67], v[32:35], 0
v_mfma_f32_16x16x32_bf16 v[132:135], a[72:75], v[32:35], 0
v_mfma_f32_16x16x32_bf16 v[128:131], a[68:71], v[36:39], v[128:131]
v_mfma_f32_16x16x32_bf16 v[132:135], a[76:79], v[36:39], v[132:135]
s_nop 7
v_permlane32_swap_b32 v128, v132
v_permlane32_swap_b32 v129, v133
v_permlane32_swap_b32 v130, v134
v_permlane32_swap_b32 v131, v135
v_add_f32_dpp v40, v128, v132 row_shr:4 row_mask:0xf bank_mask:0x2
v_add_f32_dpp v41, v129, v133 row_shr:4 row_mask:0xf bank_mask:0x2
v_add_f32_dpp v42, v130, v134 row_shr:4 row_mask:0xf bank_mask:0x2
v_add_f32_dpp v43, v131, v135 row_shr:4 row_mask:0xf bank_mask:0x2
s_add_u32 s38, s37, 0x5000
s_mov_b32 m0, s38
s_nop 0
buffer_load_dwordx4 v3, s[20:23], s30 offen  lds
buffer_load_dwordx4 v4, s[20:23], s30 offen offset:1024  lds
buffer_load_dwordx4 v3, s[20:23], s30 offen offset:2048  lds
buffer_load_dwordx4 v4, s[20:23], s30 offen offset:3072  lds
s_add_u32 s42, s42, 0x4000
s_cmp_lg_u32 s13, 0
s_cbranch_scc1 Lqdo_37_%=
s_mov_b32 m0, 149504
s_nop 0
buffer_load_dwordx4 v52, s[24:27], s42 offen  lds
buffer_load_dwordx4 v52, s[24:27], s42 offen offset:1024  lds
s_branch Lqdj_38_%=
Lqdo_37_%=:
buffer_load_dword v61, v0, s[24:27], 0 offen
buffer_load_dword v61, v0, s[24:27], 0 offen
Lqdj_38_%=:
s_waitcnt vmcnt(32)
v_mfma_f32_16x16x32_bf16 v[128:131], a[80:83], v[32:35], 0
v_mfma_f32_16x16x32_bf16 v[132:135], a[88:91], v[32:35], 0
v_mfma_f32_16x16x32_bf16 v[128:131], a[84:87], v[36:39], v[128:131]
v_mfma_f32_16x16x32_bf16 v[132:135], a[92:95], v[36:39], v[132:135]
s_nop 7
v_permlane32_swap_b32 v128, v132
v_permlane32_swap_b32 v129, v133
v_permlane32_swap_b32 v130, v134
v_permlane32_swap_b32 v131, v135
v_add_f32_dpp v40, v132, v128 row_shl:4 row_mask:0xf bank_mask:0x4
v_add_f32_dpp v41, v133, v129 row_shl:4 row_mask:0xf bank_mask:0x4
v_add_f32_dpp v42, v134, v130 row_shl:4 row_mask:0xf bank_mask:0x4
v_add_f32_dpp v43, v135, v131 row_shl:4 row_mask:0xf bank_mask:0x4
s_waitcnt vmcnt(28)
v_mfma_f32_16x16x32_bf16 v[128:131], a[96:99], v[32:35], v[56:59]
v_mfma_f32_16x16x32_bf16 v[132:135], a[104:107], v[32:35], v[56:59]
v_mfma_f32_16x16x32_bf16 v[128:131], a[100:103], v[36:39], v[128:131]
v_mfma_f32_16x16x32_bf16 v[132:135], a[108:111], v[36:39], v[132:135]
s_nop 7
v_permlane32_swap_b32 v128, v132
v_permlane32_swap_b32 v129, v133
v_permlane32_swap_b32 v130, v134
v_permlane32_swap_b32 v131, v135
v_add_f32_dpp v40, v128, v132 row_shr:4 row_mask:0xf bank_mask:0x8
v_add_f32_dpp v41, v129, v133 row_shr:4 row_mask:0xf bank_mask:0x8
v_add_f32_dpp v42, v130, v134 row_shr:4 row_mask:0xf bank_mask:0x8
v_add_f32_dpp v43, v131, v135 row_shr:4 row_mask:0xf bank_mask:0x8
s_add_u32 s16, s16, 0x1000000
s_addc_u32 s17, s17, 0
buffer_load_dwordx4 a[128:131], v1, s[16:19], s28 offen nt
buffer_load_dwordx4 a[132:135], v1, s[16:19], s28 offen offset:128 nt
buffer_load_dwordx4 a[136:139], v1, s[16:19], s28 offen offset:2048 nt
buffer_load_dwordx4 a[140:143], v1, s[16:19], s28 offen offset:2176 nt
s_waitcnt vmcnt(28)
v_mfma_f32_16x16x32_bf16 v[128:131], a[112:115], v[32:35], v[56:59]
v_max3_f32 v26, v40, v41, v42
v_mfma_f32_16x16x32_bf16 v[132:135], a[120:123], v[32:35], v[56:59]
v_mfma_f32_16x16x32_bf16 v[128:131], a[116:119], v[36:39], v[128:131]
v_mfma_f32_16x16x32_bf16 v[132:135], a[124:127], v[36:39], v[132:135]
s_nop 7
v_permlane32_swap_b32 v128, v132
v_permlane32_swap_b32 v129, v133
v_permlane32_swap_b32 v130, v134
v_permlane32_swap_b32 v131, v135
v_add_f32_dpp v44, v132, v128 row_shl:4 row_mask:0xf bank_mask:0x1
v_add_f32_dpp v45, v133, v129 row_shl:4 row_mask:0xf bank_mask:0x1
v_add_f32_dpp v46, v134, v130 row_shl:4 row_mask:0xf bank_mask:0x1
v_add_f32_dpp v47, v135, v131 row_shl:4 row_mask:0xf bank_mask:0x1
v_max3_f32 v27, v43, v44, v45
v_max3_f32 v26, v26, v46, v47
v_max_f32 v26, v26, v27
s_nop 1
v_max_f32_dpp v26, v26, v26 row_ror:4 row_mask:0xf bank_mask:0xf bound_ctrl:1
s_nop 1
v_max_f32_dpp v26, v26, v26 row_ror:8 row_mask:0xf bank_mask:0xf bound_ctrl:1
v_mov_b32 v27, v26
s_nop 1
v_permlane16_swap_b32 v26, v27
v_max_f32 v26, v26, v27
v_mov_b32 v27, v26
s_nop 1
v_permlane32_swap_b32 v26, v27
v_max_f32 v26, v26, v27
v_mul_f32 v22, s34, v26
v_fma_f32 v128, v40, s34, -v22
v_fma_f32 v129, v41, s34, -v22
v_fma_f32 v130, v42, s34, -v22
v_fma_f32 v131, v43, s34, -v22
v_fma_f32 v132, v44, s34, -v22
v_fma_f32 v133, v45, s34, -v22
v_fma_f32 v134, v46, s34, -v22
v_fma_f32 v135, v47, s34, -v22
v_exp_f32 v128, v128
v_exp_f32 v129, v129
v_exp_f32 v130, v130
v_exp_f32 v131, v131
v_exp_f32 v132, v132
v_exp_f32 v133, v133
v_exp_f32 v134, v134
v_exp_f32 v135, v135
v_add_f32 v26, v128, v129
v_add_f32 v27, v130, v131
v_add_f32 v28, v132, v133
v_add_f32 v29, v134, v135
v_add_f32 v26, v26, v27
v_add_f32 v28, v28, v29
v_add_f32 v26, v26, v28
v_cvt_pk_bf16_f32 v48, v128, v129
v_cvt_pk_bf16_f32 v49, v130, v131
v_add_f32_dpp v26, v26, v26 row_ror:4 row_mask:0xf bank_mask:0xf bound_ctrl:1
v_cvt_pk_bf16_f32 v50, v132, v133
v_cvt_pk_bf16_f32 v51, v134, v135
v_add_f32_dpp v26, v26, v26 row_ror:8 row_mask:0xf bank_mask:0xf bound_ctrl:1
v_mov_b32 v27, v26
s_nop 1
v_permlane16_swap_b32 v26, v27
v_add_f32 v26, v26, v27
v_mov_b32 v27, v26
s_nop 1
v_permlane32_swap_b32 v26, v27
v_add_f32 v26, v26, v27
v_mov_b32 v23, v26
buffer_load_dwordx4 a[144:147], v1, s[16:19], s29 offen nt
buffer_load_dwordx4 a[148:151], v1, s[16:19], s29 offen offset:128 nt
buffer_load_dwordx4 a[152:155], v1, s[16:19], s29 offen offset:2048 nt
buffer_load_dwordx4 a[156:159], v1, s[16:19], s29 offen offset:2176 nt
s_waitcnt vmcnt(19)
v_mov_b32_dpp v80, v48 row_shl:12 row_mask:0xf bank_mask:0x1
v_mov_b32_dpp v81, v49 row_shl:12 row_mask:0xf bank_mask:0x1
v_mov_b32_dpp v82, v50 quad_perm:[0,1,2,3] row_mask:0xf bank_mask:0x1
v_mov_b32_dpp v83, v51 quad_perm:[0,1,2,3] row_mask:0xf bank_mask:0x1
v_perm_b32 v96, v196, v192, s40
v_perm_b32 v97, v204, v200, s40
v_perm_b32 v98, v212, v208, s40
v_perm_b32 v99, v220, v216, s40
v_perm_b32 v112, v198, v194, s40
v_perm_b32 v113, v206, v202, s40
v_perm_b32 v114, v214, v210, s40
v_perm_b32 v115, v222, v218, s40
v_mfma_f32_16x16x32_bf16 v[72:75], v[80:83], v[96:99], 0
v_mov_b32_dpp v84, v48 row_shl:8 row_mask:0xf bank_mask:0x2
v_mov_b32_dpp v85, v49 row_shl:8 row_mask:0xf bank_mask:0x2
v_mfma_f32_16x16x32_bf16 v[76:79], v[80:83], v[112:115], 0
v_mov_b32_dpp v86, v50 row_shr:4 row_mask:0xf bank_mask:0x2
v_mov_b32_dpp v87, v51 row_shr:4 row_mask:0xf bank_mask:0x2
v_perm_b32 v100, v196, v192, s41
v_perm_b32 v101, v204, v200, s41
v_perm_b32 v102, v212, v208, s41
v_perm_b32 v103, v220, v216, s41
v_perm_b32 v116, v198, v194, s41
v_perm_b32 v117, v206, v202, s41
v_perm_b32 v118, v214, v210, s41
v_perm_b32 v119, v222, v218, s41
v_mfma_f32_16x16x32_bf16 v[72:75], v[84:87], v[100:103], v[72:75]
v_mov_b32_dpp v88, v48 row_shl:4 row_mask:0xf bank_mask:0x4
v_mov_b32_dpp v89, v49 row_shl:4 row_mask:0xf bank_mask:0x4
v_mfma_f32_16x16x32_bf16 v[76:79], v[84:87], v[116:119], v[76:79]
v_mov_b32_dpp v90, v50 row_shr:8 row_mask:0xf bank_mask:0x4
v_mov_b32_dpp v91, v51 row_shr:8 row_mask:0xf bank_mask:0x4
v_perm_b32 v104, v197, v193, s40
v_perm_b32 v105, v205, v201, s40
v_perm_b32 v106, v213, v209, s40
v_perm_b32 v107, v221, v217, s40
v_perm_b32 v120, v199, v195, s40
v_perm_b32 v121, v207, v203, s40
v_perm_b32 v122, v215, v211, s40
v_perm_b32 v123, v223, v219, s40
v_mfma_f32_16x16x32_bf16 v[72:75], v[88:91], v[104:107], v[72:75]
v_mov_b32_dpp v92, v48 quad_perm:[0,1,2,3] row_mask:0xf bank_mask:0x8
v_mov_b32_dpp v93, v49 quad_perm:[0,1,2,3] row_mask:0xf bank_mask:0x8
v_mfma_f32_16x16x32_bf16 v[76:79], v[88:91], v[120:123], v[76:79]
v_mov_b32_dpp v94, v50 row_shr:12 row_mask:0xf bank_mask:0x8
v_mov_b32_dpp v95, v51 row_shr:12 row_mask:0xf bank_mask:0x8
v_perm_b32 v108, v197, v193, s41
v_perm_b32 v109, v205, v201, s41
v_perm_b32 v110, v213, v209, s41
v_perm_b32 v111, v221, v217, s41
v_perm_b32 v124, v199, v195, s41
v_perm_b32 v125, v207, v203, s41
v_perm_b32 v126, v215, v211, s41
v_perm_b32 v127, v223, v219, s41
v_mfma_f32_16x16x32_bf16 v[72:75], v[92:95], v[108:111], v[72:75]
s_nop 0
v_mfma_f32_16x16x32_bf16 v[76:79], v[92:95], v[124:127], v[76:79]
s_waitcnt vmcnt(10)
s_cmp_lg_u32 s13, 0
s_cbranch_scc1 Lqdo_39_%=
s_mov_b32 m0, 147456
s_nop 0
buffer_load_dwordx4 v52, s[24:27], s43 offen sc1 lds
buffer_load_dwordx4 v52, s[24:27], s43 offen offset:1024 sc1 lds
s_branch Lqdj_40_%=
Lqdo_39_%=:
buffer_load_dword v61, v0, s[24:27], 0 offen
buffer_load_dword v61, v0, s[24:27], 0 offen
Lqdj_40_%=:
ds_read_b64_tr_b16 v[96:97], v14 offset:12288
v_mov_b32_dpp v80, v48 quad_perm:[0,1,2,3] row_mask:0xf bank_mask:0x1
ds_read_b64_tr_b16 v[98:99], v14 offset:16384
v_mov_b32_dpp v81, v49 quad_perm:[0,1,2,3] row_mask:0xf bank_mask:0x1
ds_read_b64_tr_b16 v[100:101], v15 offset:12288
v_mov_b32_dpp v82, v48 row_shl:4 row_mask:0xf bank_mask:0x1
ds_read_b64_tr_b16 v[102:103], v15 offset:16384
v_mov_b32_dpp v83, v49 row_shl:4 row_mask:0xf bank_mask:0x1
ds_read_b64_tr_b16 v[104:105], v16 offset:12288
v_mov_b32_dpp v84, v48 row_shr:4 row_mask:0xf bank_mask:0x2
ds_read_b64_tr_b16 v[106:107], v16 offset:16384
v_mov_b32_dpp v85, v49 row_shr:4 row_mask:0xf bank_mask:0x2
ds_read_b64_tr_b16 v[108:109], v17 offset:12288
v_mov_b32_dpp v86, v48 quad_perm:[0,1,2,3] row_mask:0xf bank_mask:0x2
ds_read_b64_tr_b16 v[110:111], v17 offset:16384
v_mov_b32_dpp v87, v49 quad_perm:[0,1,2,3] row_mask:0xf bank_mask:0x2
ds_read_b64_tr_b16 v[112:113], v18 offset:12288
v_mov_b32_dpp v88, v48 row_shr:8 row_mask:0xf bank_mask:0x4
ds_read_b64_tr_b16 v[114:115], v18 offset:16384
v_mov_b32_dpp v89, v49 row_shr:8 row_mask:0xf bank_mask:0x4
ds_read_b64_tr_b16 v[116:117], v19 offset:12288
v_mov_b32_dpp v90, v48 row_shr:4 row_mask:0xf bank_mask:0x4
ds_read_b64_tr_b16 v[118:119], v19 offset:16384
v_mov_b32_dpp v91, v49 row_shr:4 row_mask:0xf bank_mask:0x4
ds_read_b64_tr_b16 v[120:121], v20 offset:12288
v_mov_b32_dpp v92, v48 row_shr:12 row_mask:0xf bank_mask:0x8
ds_read_b64_tr_b16 v[122:123], v20 offset:16384
v_mov_b32_dpp v93, v49 row_shr:12 row_mask:0xf bank_mask:0x8
ds_read_b64_tr_b16 v[124:125], v21 offset:12288
v_mov_b32_dpp v94, v48 row_shr:8 row_mask:0xf bank_mask:0x8
ds_read_b64_tr_b16 v[126:127], v21 offset:16384
v_mov_b32_dpp v95, v49 row_shr:8 row_mask:0xf bank_mask:0x8
ds_read_b64_tr_b16 v[128:129], v14 offset:20480
s_waitcnt lgkmcnt(14)
v_mfma_f32_16x16x32_bf16 v[64:67], v[80:83], v[96:99], 0
ds_read_b64_tr_b16 v[130:131], v15 offset:20480
v_add_u32 v29, s35, v11
ds_read_b64_tr_b16 v[132:133], v16 offset:20480
s_waitcnt lgkmcnt(14)
v_mfma_f32_16x16x32_bf16 v[64:67], v[84:87], v[100:103], v[64:67]
ds_read_b64_tr_b16 v[134:135], v17 offset:20480
ds_read_b64_tr_b16 v[136:137], v18 offset:20480
s_waitcnt lgkmcnt(14)
v_mfma_f32_16x16x32_bf16 v[64:67], v[88:91], v[104:107], v[64:67]
ds_read_b64_tr_b16 v[138:139], v19 offset:20480
ds_read_b64_tr_b16 v[140:141], v20 offset:20480
s_waitcnt lgkmcnt(13)
v_mfma_f32_16x16x32_bf16 v[68:71], v[80:83], v[112:115], 0
ds_read_b64_tr_b16 v[142:143], v21 offset:20480
v_mov_b32_dpp v80, v48 row_shl:8 row_mask:0xf bank_mask:0x1
s_waitcnt lgkmcnt(12)
v_mfma_f32_16x16x32_bf16 v[68:71], v[84:87], v[116:119], v[68:71]
v_mov_b32_dpp v81, v49 row_shl:8 row_mask:0xf bank_mask:0x1
v_mov_b32_dpp v84, v48 row_shl:4 row_mask:0xf bank_mask:0x2
s_waitcnt lgkmcnt(10)
v_mfma_f32_16x16x32_bf16 v[68:71], v[88:91], v[120:123], v[68:71]
v_mov_b32_dpp v85, v49 row_shl:4 row_mask:0xf bank_mask:0x2
v_mov_b32_dpp v88, v48 quad_perm:[0,1,2,3] row_mask:0xf bank_mask:0x4
v_mfma_f32_16x16x32_bf16 v[64:67], v[92:95], v[108:111], v[64:67]
v_mov_b32_dpp v89, v49 quad_perm:[0,1,2,3] row_mask:0xf bank_mask:0x4
s_waitcnt lgkmcnt(8)
v_mfma_f32_16x16x32_bf16 v[68:71], v[92:95], v[124:127], v[68:71]
v_mov_b32_dpp v92, v48 row_shr:4 row_mask:0xf bank_mask:0x8
v_mov_b32_dpp v93, v49 row_shr:4 row_mask:0xf bank_mask:0x8
s_waitcnt lgkmcnt(7)
v_mfma_f32_16x16x16_bf16 v[64:67], v[80:81], v[128:129], v[64:67]
s_waitcnt lgkmcnt(3)
v_mfma_f32_16x16x16_bf16 v[68:71], v[80:81], v[136:137], v[68:71]
v_mfma_f32_16x16x16_bf16 v[64:67], v[84:85], v[130:131], v[64:67]
s_waitcnt lgkmcnt(2)
v_mfma_f32_16x16x16_bf16 v[68:71], v[84:85], v[138:139], v[68:71]
v_mfma_f32_16x16x16_bf16 v[64:67], v[88:89], v[132:133], v[64:67]
v_cvt_pk_bf16_f32 v132, v72, v76
v_cvt_pk_bf16_f32 v133, v73, v77
s_waitcnt lgkmcnt(1)
v_mfma_f32_16x16x16_bf16 v[68:71], v[88:89], v[140:141], v[68:71]
v_mfma_f32_16x16x16_bf16 v[64:67], v[92:93], v[134:135], v[64:67]
v_cvt_pk_bf16_f32 v134, v74, v78
v_cvt_pk_bf16_f32 v135, v75, v79
s_waitcnt lgkmcnt(0)
v_mfma_f32_16x16x16_bf16 v[68:71], v[92:93], v[142:143], v[68:71]
s_nop 7
v_cvt_pk_bf16_f32 v128, v64, v68
v_cvt_pk_bf16_f32 v129, v65, v69
v_cvt_pk_bf16_f32 v130, v66, v70
v_cvt_pk_bf16_f32 v131, v67, v71
ds_write2st64_b32 v10, v128, v129 offset0:0 offset1:1
ds_write2st64_b32 v10, v132, v133 offset0:4 offset1:5
ds_write2st64_b32 v10, v130, v131 offset0:2 offset1:3
ds_write2st64_b32 v10, v134, v135 offset0:6 offset1:7
ds_write_b64 v29, v[22:23] offset:2048
s_waitcnt lgkmcnt(0)
s_barrier
v_add_u32 v130, 0x25800, v13
ds_read_b64 v[96:97], v130 offset:0
v_add_u32 v128, 0x25800, v12
ds_read_b64 v[98:99], v130 offset:2176
v_add_u32 v129, 0x25800, v24
ds_read_b64 v[100:101], v130 offset:4352
v_mov_b32 v137, s12
ds_read_b64 v[102:103], v130 offset:6528
v_mov_b32 v139, s12
ds_read_b32 v104, v128 offset:0
ds_read2_b32 v[108:109], v129 offset0:0 offset1:8
ds_read_b32 v105, v128 offset:2176
ds_read_b32 v110, v129 offset:2176
ds_read_b32 v111, v129 offset:2208
ds_read_b32 v106, v128 offset:4352
ds_read_b32 v112, v129 offset:4352
ds_read_b32 v113, v129 offset:4384
ds_read_b32 v107, v128 offset:6528
ds_read_b32 v114, v129 offset:6528
ds_read_b32 v115, v129 offset:6560
s_waitcnt lgkmcnt(12)
v_max3_f32 v26, v96, v98, v100
s_waitcnt lgkmcnt(11)
v_max_f32 v26, v26, v102
v_sub_f32 v96, v96, v26
v_sub_f32 v98, v98, v26
v_exp_f32 v96, v96
v_sub_f32 v100, v100, v26
v_exp_f32 v98, v98
s_waitcnt lgkmcnt(10)
v_lshlrev_b32 v116, 16, v104
v_and_b32 v117, 0xffff0000, v104
s_waitcnt lgkmcnt(9)
v_perm_b32 v118, v108, v108, v25
v_perm_b32 v119, v109, v109, v25
v_sub_f32 v102, v102, v26
v_exp_f32 v100, v100
v_pk_add_f32 v[116:117], v[116:117], v[118:119]
s_waitcnt lgkmcnt(8)
v_lshlrev_b32 v120, 16, v105
v_and_b32 v121, 0xffff0000, v105
s_waitcnt lgkmcnt(7)
v_perm_b32 v122, v110, v110, v25
s_waitcnt lgkmcnt(6)
v_perm_b32 v123, v111, v111, v25
v_exp_f32 v102, v102
v_pk_mul_f32 v[134:135], v[116:117], v[96:97] op_sel_hi:[1,0]
v_pk_add_f32 v[120:121], v[120:121], v[122:123]
s_waitcnt lgkmcnt(5)
v_lshlrev_b32 v116, 16, v106
v_and_b32 v117, 0xffff0000, v106
s_waitcnt lgkmcnt(4)
v_perm_b32 v118, v112, v112, v25
s_waitcnt lgkmcnt(3)
v_perm_b32 v119, v113, v113, v25
v_pk_fma_f32 v[134:135], v[120:121], v[98:99], v[134:135] op_sel_hi:[1,0,1]
v_pk_add_f32 v[116:117], v[116:117], v[118:119]
s_waitcnt lgkmcnt(2)
v_lshlrev_b32 v120, 16, v107
v_and_b32 v121, 0xffff0000, v107
s_waitcnt lgkmcnt(1)
v_perm_b32 v122, v114, v114, v25
s_waitcnt lgkmcnt(0)
v_perm_b32 v123, v115, v115, v25
v_pk_fma_f32 v[134:135], v[116:117], v[100:101], v[134:135] op_sel_hi:[1,0,1]
v_pk_add_f32 v[120:121], v[120:121], v[122:123]
v_pk_fma_f32 v[134:135], v[120:121], v[102:103], v[134:135] op_sel_hi:[1,0,1]
v_mul_f32 v27, v96, v97
v_cvt_pk_bf16_f32 v136, v134, v135
v_fma_f32 v27, v98, v99, v27
v_fma_f32 v27, v100, v101, v27
v_fma_f32 v27, v102, v103, v27
v_mov_b32_dpp v138, v136 quad_perm:[1,0,3,2] row_mask:0xf bank_mask:0xf bound_ctrl:1
v_cndmask_b32 v136, v136, v26, s[56:57]
v_cndmask_b32 v138, v138, v27, s[56:57]
s_mov_b32 exec_lo, 0x55555557
s_mov_b32 exec_hi, 0x55555555
buffer_store_dwordx4 v[136:139], v6, s[24:27], 0 offen sc1
s_mov_b64 exec, -1
s_nop 0
v_add_u32 v6, v6, v8
s_waitcnt vmcnt(1)
s_barrier
s_mov_b32 s39, 0
Lqchk_41_%=:
ds_read_b128 v[96:99], v60 offset:0
ds_read_b128 v[100:103], v60 offset:512
ds_read_b128 v[104:107], v60 offset:1024
ds_read_b128 v[108:111], v60 offset:1536
s_waitcnt lgkmcnt(0)
v_bitop3_b32 v26, v97, v99, s12 bitop3:0x7e
v_bitop3_b32 v27, v101, v103, s12 bitop3:0x7e
v_bitop3_b32 v28, v105, v107, s12 bitop3:0x7e
v_bitop3_b32 v29, v109, v111, s12 bitop3:0x7e
v_or3_b32 v26, v26, v27, v28
v_or_b32 v26, v26, v29
v_cmp_ne_u32 vcc, 0, v26
v_mov_b32 v120, v96
v_mov_b32 v121, v98
v_mov_b32 v122, v100
v_mov_b32 v123, v102
v_mov_b32 v124, v104
v_mov_b32 v125, v106
v_mov_b32 v126, v108
v_mov_b32 v127, v110
s_cbranch_vccnz Lqslow_42_%=
Lqcont_43_%=:
s_add_u32 s43, s43, 0x4000
v_mfma_f32_16x16x32_bf16 v[128:131], a[160:163], v[120:123], 0
buffer_load_dwordx4 a[160:163], v1, s[16:19], s30 offen nt
v_perm_b32 v104, v229, v225, s40
v_mfma_f32_16x16x32_bf16 v[132:135], a[168:171], v[120:123], 0
v_perm_b32 v105, v237, v233, s40
v_perm_b32 v106, v245, v241, s40
v_mfma_f32_16x16x32_bf16 v[136:139], a[176:179], v[120:123], 0
v_perm_b32 v107, v253, v249, s40
v_perm_b32 v108, v229, v225, s41
v_mfma_f32_16x16x32_bf16 v[140:143], a[184:187], v[120:123], 0
v_perm_b32 v109, v237, v233, s41
v_perm_b32 v110, v245, v241, s41
v_mfma_f32_16x16x32_bf16 v[128:131], a[164:167], v[124:127], v[128:131]
buffer_load_dwordx4 a[164:167], v1, s[16:19], s30 offen offset:128 nt
buffer_load_dwordx4 a[168:171], v1, s[16:19], s30 offen offset:2048 nt
v_mfma_f32_16x16x32_bf16 v[132:135], a[172:175], v[124:127], v[132:135]
buffer_load_dwordx4 a[172:175], v1, s[16:19], s30 offen offset:2176 nt
buffer_load_dwordx4 a[176:179], v1, s[16:19], s31 offen nt
v_mfma_f32_16x16x32_bf16 v[136:139], a[180:183], v[124:127], v[136:139]
buffer_load_dwordx4 a[180:183], v1, s[16:19], s31 offen offset:128 nt
buffer_load_dwordx4 a[184:187], v1, s[16:19], s31 offen offset:2048 nt
v_mfma_f32_16x16x32_bf16 v[140:143], a[188:191], v[124:127], v[140:143]
buffer_load_dwordx4 a[188:191], v1, s[16:19], s31 offen offset:2176 nt
v_perm_b32 v111, v253, v249, s41
v_mfma_f32_16x16x32_bf16 v[96:99], a[192:195], v[120:123], 0
v_permlane32_swap_b32 v128, v132
v_permlane32_swap_b32 v129, v133
v_permlane32_swap_b32 v130, v134
v_permlane32_swap_b32 v131, v135
v_mfma_f32_16x16x32_bf16 v[100:103], a[200:203], v[120:123], 0
v_add_f32_dpp v40, v132, v128 row_shl:4 row_mask:0xf bank_mask:0x1
v_add_f32_dpp v41, v133, v129 row_shl:4 row_mask:0xf bank_mask:0x1
v_add_f32_dpp v42, v134, v130 row_shl:4 row_mask:0xf bank_mask:0x1
v_add_f32_dpp v43, v135, v131 row_shl:4 row_mask:0xf bank_mask:0x1
v_permlane32_swap_b32 v136, v140
v_permlane32_swap_b32 v137, v141
v_permlane32_swap_b32 v138, v142
v_permlane32_swap_b32 v139, v143
v_mfma_f32_16x16x32_bf16 v[128:131], a[208:211], v[120:123], v[56:59]
v_add_f32_dpp v40, v136, v140 row_shr:4 row_mask:0xf bank_mask:0x2
v_add_f32_dpp v41, v137, v141 row_shr:4 row_mask:0xf bank_mask:0x2
v_mfma_f32_16x16x32_bf16 v[132:135], a[216:219], v[120:123], v[56:59]
v_add_f32_dpp v42, v138, v142 row_shr:4 row_mask:0xf bank_mask:0x2
v_add_f32_dpp v43, v139, v143 row_shr:4 row_mask:0xf bank_mask:0x2
v_mfma_f32_16x16x32_bf16 v[96:99], a[196:199], v[124:127], v[96:99]
v_perm_b32 v112, v230, v226, s40
v_perm_b32 v113, v238, v234, s40
v_mfma_f32_16x16x32_bf16 v[100:103], a[204:207], v[124:127], v[100:103]
v_perm_b32 v114, v246, v242, s40
v_perm_b32 v115, v254, v250, s40
v_mfma_f32_16x16x32_bf16 v[136:139], a[224:227], v[120:123], v[56:59]
v_perm_b32 v116, v230, v226, s41
v_perm_b32 v117, v238, v234, s41
v_mfma_f32_16x16x32_bf16 v[140:143], a[232:235], v[120:123], v[56:59]
v_perm_b32 v118, v246, v242, s41
v_perm_b32 v119, v254, v250, s41
v_mfma_f32_16x16x32_bf16 v[128:131], a[212:215], v[124:127], v[128:131]
v_permlane32_swap_b32 v96, v100
v_permlane32_swap_b32 v97, v101
v_mfma_f32_16x16x32_bf16 v[132:135], a[220:223], v[124:127], v[132:135]
v_permlane32_swap_b32 v98, v102
v_permlane32_swap_b32 v99, v103
v_mfma_f32_16x16x32_bf16 v[136:139], a[228:231], v[124:127], v[136:139]
v_add_f32_dpp v40, v100, v96 row_shl:4 row_mask:0xf bank_mask:0x4
v_add_f32_dpp v41, v101, v97 row_shl:4 row_mask:0xf bank_mask:0x4
v_mfma_f32_16x16x32_bf16 v[140:143], a[236:239], v[124:127], v[140:143]
v_add_f32_dpp v42, v102, v98 row_shl:4 row_mask:0xf bank_mask:0x4
v_add_f32_dpp v43, v103, v99 row_shl:4 row_mask:0xf bank_mask:0x4
v_perm_b32 v96, v228, v224, s40
v_permlane32_swap_b32 v128, v132
v_permlane32_swap_b32 v129, v133
v_permlane32_swap_b32 v130, v134
v_permlane32_swap_b32 v131, v135
v_add_f32_dpp v40, v128, v132 row_shr:4 row_mask:0xf bank_mask:0x8
v_add_f32_dpp v41, v129, v133 row_shr:4 row_mask:0xf bank_mask:0x8
v_add_f32_dpp v42, v130, v134 row_shr:4 row_mask:0xf bank_mask:0x8
v_add_f32_dpp v43, v131, v135 row_shr:4 row_mask:0xf bank_mask:0x8
v_permlane32_swap_b32 v136, v140
v_permlane32_swap_b32 v137, v141
v_permlane32_swap_b32 v138, v142
v_permlane32_swap_b32 v139, v143
v_add_f32_dpp v44, v140, v136 row_shl:4 row_mask:0xf bank_mask:0x1
v_add_f32_dpp v45, v141, v137 row_shl:4 row_mask:0xf bank_mask:0x1
v_add_f32_dpp v46, v142, v138 row_shl:4 row_mask:0xf bank_mask:0x1
v_add_f32_dpp v47, v143, v139 row_shl:4 row_mask:0xf bank_mask:0x1
v_max3_f32 v26, v40, v41, v42
v_max3_f32 v27, v43, v44, v45
v_max3_f32 v26, v26, v46, v47
v_max_f32 v26, v26, v27
v_perm_b32 v97, v236, v232, s40
v_perm_b32 v98, v244, v240, s40
v_max_f32_dpp v26, v26, v26 row_ror:4 row_mask:0xf bank_mask:0xf bound_ctrl:1
v_perm_b32 v99, v252, v248, s40
v_perm_b32 v100, v228, v224, s41
v_max_f32_dpp v26, v26, v26 row_ror:8 row_mask:0xf bank_mask:0xf bound_ctrl:1
v_mov_b32 v27, v26
v_perm_b32 v101, v236, v232, s41
v_perm_b32 v102, v244, v240, s41
v_permlane16_swap_b32 v26, v27
v_max_f32 v26, v26, v27
v_mov_b32 v27, v26
v_perm_b32 v103, v252, v248, s41
v_perm_b32 v120, v231, v227, s40
v_permlane32_swap_b32 v26, v27
v_max_f32 v26, v26, v27
v_mul_f32 v22, s34, v26
v_fma_f32 v128, v40, s34, -v22
v_fma_f32 v129, v41, s34, -v22
v_fma_f32 v130, v42, s34, -v22
v_fma_f32 v131, v43, s34, -v22
v_fma_f32 v132, v44, s34, -v22
v_fma_f32 v133, v45, s34, -v22
v_fma_f32 v134, v46, s34, -v22
v_fma_f32 v135, v47, s34, -v22
v_exp_f32 v128, v128
v_exp_f32 v129, v129
v_exp_f32 v130, v130
v_exp_f32 v131, v131
v_exp_f32 v132, v132
v_exp_f32 v133, v133
v_exp_f32 v134, v134
v_exp_f32 v135, v135
v_cvt_pk_bf16_f32 v48, v128, v129
v_cvt_pk_bf16_f32 v49, v130, v131
v_cvt_pk_bf16_f32 v50, v132, v133
v_cvt_pk_bf16_f32 v51, v134, v135
v_mov_b32_dpp v80, v48 row_shl:12 row_mask:0xf bank_mask:0x1
v_mov_b32_dpp v81, v49 row_shl:12 row_mask:0xf bank_mask:0x1
v_mov_b32_dpp v82, v50 quad_perm:[0,1,2,3] row_mask:0xf bank_mask:0x1
v_mov_b32_dpp v83, v51 quad_perm:[0,1,2,3] row_mask:0xf bank_mask:0x1
v_mov_b32_dpp v84, v48 row_shl:8 row_mask:0xf bank_mask:0x2
v_mov_b32_dpp v85, v49 row_shl:8 row_mask:0xf bank_mask:0x2
v_mfma_f32_16x16x32_bf16 v[72:75], v[80:83], v[96:99], 0
v_mov_b32_dpp v86, v50 row_shr:4 row_mask:0xf bank_mask:0x2
v_mov_b32_dpp v87, v51 row_shr:4 row_mask:0xf bank_mask:0x2
v_mov_b32_dpp v88, v48 row_shl:4 row_mask:0xf bank_mask:0x4
v_mov_b32_dpp v89, v49 row_shl:4 row_mask:0xf bank_mask:0x4
v_mfma_f32_16x16x32_bf16 v[72:75], v[84:87], v[100:103], v[72:75]
v_mov_b32_dpp v90, v50 row_shr:8 row_mask:0xf bank_mask:0x4
v_mov_b32_dpp v91, v51 row_shr:8 row_mask:0xf bank_mask:0x4
v_mov_b32_dpp v92, v48 quad_perm:[0,1,2,3] row_mask:0xf bank_mask:0x8
v_mov_b32_dpp v93, v49 quad_perm:[0,1,2,3] row_mask:0xf bank_mask:0x8
v_mfma_f32_16x16x32_bf16 v[72:75], v[88:91], v[104:107], v[72:75]
v_mov_b32_dpp v94, v50 row_shr:12 row_mask:0xf bank_mask:0x8
v_mov_b32_dpp v95, v51 row_shr:12 row_mask:0xf bank_mask:0x8
v_mfma_f32_16x16x32_bf16 v[76:79], v[80:83], v[112:115], 0
ds_read_b64_tr_b16 v[96:97], v14 offset:24576
v_perm_b32 v121, v239, v235, s40
v_mfma_f32_16x16x32_bf16 v[72:75], v[92:95], v[108:111], v[72:75]
ds_read_b64_tr_b16 v[98:99], v14 offset:28672
v_perm_b32 v122, v247, v243, s40
ds_read_b64_tr_b16 v[100:101], v15 offset:24576
v_mfma_f32_16x16x32_bf16 v[76:79], v[84:87], v[116:119], v[76:79]
ds_read_b64_tr_b16 v[102:103], v15 offset:28672
v_perm_b32 v123, v255, v251, s40
ds_read_b64_tr_b16 v[104:105], v16 offset:24576
v_perm_b32 v124, v231, v227, s41
ds_read_b64_tr_b16 v[106:107], v16 offset:28672
v_mfma_f32_16x16x32_bf16 v[76:79], v[88:91], v[120:123], v[76:79]
ds_read_b64_tr_b16 v[108:109], v17 offset:24576
v_perm_b32 v125, v239, v235, s41
ds_read_b64_tr_b16 v[110:111], v17 offset:28672
v_perm_b32 v126, v247, v243, s41
ds_read_b64_tr_b16 v[112:113], v18 offset:24576
v_perm_b32 v127, v255, v251, s41
ds_read_b64_tr_b16 v[114:115], v18 offset:28672
v_add_f32 v26, v128, v129
ds_read_b64_tr_b16 v[116:117], v19 offset:24576
v_mfma_f32_16x16x32_bf16 v[76:79], v[92:95], v[124:127], v[76:79]
ds_read_b64_tr_b16 v[118:119], v19 offset:28672
v_add_f32 v27, v130, v131
ds_read_b64_tr_b16 v[120:121], v20 offset:24576
v_add_f32 v28, v132, v133
ds_read_b64_tr_b16 v[122:123], v20 offset:28672
v_add_f32 v29, v134, v135
ds_read_b64_tr_b16 v[124:125], v21 offset:24576
v_mov_b32_dpp v80, v48 quad_perm:[0,1,2,3] row_mask:0xf bank_mask:0x1
ds_read_b64_tr_b16 v[126:127], v21 offset:28672
v_mov_b32_dpp v81, v49 quad_perm:[0,1,2,3] row_mask:0xf bank_mask:0x1
ds_read_b64_tr_b16 v[128:129], v14 offset:32768
v_mov_b32_dpp v82, v48 row_shl:4 row_mask:0xf bank_mask:0x1
ds_read_b64_tr_b16 v[130:131], v15 offset:32768
v_mov_b32_dpp v83, v49 row_shl:4 row_mask:0xf bank_mask:0x1
ds_read_b64_tr_b16 v[132:133], v16 offset:32768
v_mov_b32_dpp v84, v48 row_shr:4 row_mask:0xf bank_mask:0x2
ds_read_b64_tr_b16 v[134:135], v17 offset:32768
s_waitcnt lgkmcnt(14)
v_mfma_f32_16x16x32_bf16 v[64:67], v[80:83], v[96:99], 0
ds_read_b64_tr_b16 v[136:137], v18 offset:32768
v_mov_b32_dpp v85, v49 row_shr:4 row_mask:0xf bank_mask:0x2
ds_read_b64_tr_b16 v[138:139], v19 offset:32768
v_mov_b32_dpp v86, v48 quad_perm:[0,1,2,3] row_mask:0xf bank_mask:0x2
ds_read_b64_tr_b16 v[140:141], v20 offset:32768
v_mov_b32_dpp v87, v49 quad_perm:[0,1,2,3] row_mask:0xf bank_mask:0x2
ds_read_b64_tr_b16 v[142:143], v21 offset:32768
s_waitcnt lgkmcnt(14)
v_mfma_f32_16x16x32_bf16 v[68:71], v[80:83], v[112:115], 0
v_mov_b32_dpp v88, v48 row_shr:8 row_mask:0xf bank_mask:0x4
v_mov_b32_dpp v89, v49 row_shr:8 row_mask:0xf bank_mask:0x4
v_mfma_f32_16x16x32_bf16 v[64:67], v[84:87], v[100:103], v[64:67]
v_mov_b32_dpp v90, v48 row_shr:4 row_mask:0xf bank_mask:0x4
v_mov_b32_dpp v91, v49 row_shr:4 row_mask:0xf bank_mask:0x4
s_waitcnt lgkmcnt(12)
v_mfma_f32_16x16x32_bf16 v[68:71], v[84:87], v[116:119], v[68:71]
v_mov_b32_dpp v92, v48 row_shr:12 row_mask:0xf bank_mask:0x8
v_mov_b32_dpp v93, v49 row_shr:12 row_mask:0xf bank_mask:0x8
v_mfma_f32_16x16x32_bf16 v[64:67], v[88:91], v[104:107], v[64:67]
v_mov_b32_dpp v94, v48 row_shr:8 row_mask:0xf bank_mask:0x8
v_mov_b32_dpp v95, v49 row_shr:8 row_mask:0xf bank_mask:0x8
s_waitcnt lgkmcnt(10)
v_mfma_f32_16x16x32_bf16 v[68:71], v[88:91], v[120:123], v[68:71]
v_mov_b32_dpp v80, v48 row_shl:8 row_mask:0xf bank_mask:0x1
v_mov_b32_dpp v81, v49 row_shl:8 row_mask:0xf bank_mask:0x1
v_mfma_f32_16x16x32_bf16 v[64:67], v[92:95], v[108:111], v[64:67]
v_mov_b32_dpp v84, v48 row_shl:4 row_mask:0xf bank_mask:0x2
v_mov_b32_dpp v85, v49 row_shl:4 row_mask:0xf bank_mask:0x2
s_waitcnt lgkmcnt(8)
v_mfma_f32_16x16x32_bf16 v[68:71], v[92:95], v[124:127], v[68:71]
v_mov_b32_dpp v88, v48 quad_perm:[0,1,2,3] row_mask:0xf bank_mask:0x4
v_mov_b32_dpp v89, v49 quad_perm:[0,1,2,3] row_mask:0xf bank_mask:0x4
s_waitcnt lgkmcnt(7)
v_mfma_f32_16x16x16_bf16 v[64:67], v[80:81], v[128:129], v[64:67]
v_add_f32 v26, v26, v27
v_add_f32 v28, v28, v29
s_waitcnt lgkmcnt(3)
v_mfma_f32_16x16x16_bf16 v[68:71], v[80:81], v[136:137], v[68:71]
v_add_f32 v26, v26, v28
v_mov_b32_dpp v92, v48 row_shr:4 row_mask:0xf bank_mask:0x8
v_mfma_f32_16x16x16_bf16 v[64:67], v[84:85], v[130:131], v[64:67]
v_mov_b32_dpp v93, v49 row_shr:4 row_mask:0xf bank_mask:0x8
v_add_f32_dpp v26, v26, v26 row_ror:4 row_mask:0xf bank_mask:0xf bound_ctrl:1
s_waitcnt lgkmcnt(2)
v_mfma_f32_16x16x16_bf16 v[68:71], v[84:85], v[138:139], v[68:71]
v_add_f32_dpp v26, v26, v26 row_ror:8 row_mask:0xf bank_mask:0xf bound_ctrl:1
v_mov_b32 v27, v26
v_mfma_f32_16x16x16_bf16 v[64:67], v[88:89], v[132:133], v[64:67]
s_nop 0
v_permlane16_swap_b32 v26, v27
v_add_f32 v26, v26, v27
s_waitcnt lgkmcnt(1)
v_mfma_f32_16x16x16_bf16 v[68:71], v[88:89], v[140:141], v[68:71]
v_mov_b32 v27, v26
v_cvt_pk_bf16_f32 v132, v72, v76
v_mfma_f32_16x16x16_bf16 v[64:67], v[92:93], v[134:135], v[64:67]
v_permlane32_swap_b32 v26, v27
v_add_f32 v26, v26, v27
s_waitcnt lgkmcnt(0)
v_mfma_f32_16x16x16_bf16 v[68:71], v[92:93], v[142:143], v[68:71]
v_mov_b32 v23, v26
v_cvt_pk_bf16_f32 v133, v73, v77
v_cvt_pk_bf16_f32 v134, v74, v78
v_cvt_pk_bf16_f32 v135, v75, v79
s_nop 3
v_cvt_pk_bf16_f32 v128, v64, v68
v_cvt_pk_bf16_f32 v129, v65, v69
v_cvt_pk_bf16_f32 v130, v66, v70
v_cvt_pk_bf16_f32 v131, v67, v71
ds_write2st64_b32 v9, v128, v129 offset0:112 offset1:113
ds_write2st64_b32 v9, v132, v133 offset0:116 offset1:117
ds_write2st64_b32 v9, v130, v131 offset0:114 offset1:115
ds_write2st64_b32 v9, v134, v135 offset0:118 offset1:119
ds_write_b64 v11, v[22:23] offset:30720
buffer_load_dwordx4 a[192:195], v1, s[16:19], s32 offen nt
buffer_load_dwordx4 a[196:199], v1, s[16:19], s32 offen offset:128 nt
buffer_load_dwordx4 a[200:203], v1, s[16:19], s32 offen offset:2048 nt
buffer_load_dwordx4 a[204:207], v1, s[16:19], s32 offen offset:2176 nt
s_add_u32 s20, s20, 0x1000000
s_addc_u32 s21, s21, 0
s_add_u32 s38, s37, 0x6000
s_mov_b32 m0, s38
s_nop 0
buffer_load_dwordx4 v3, s[20:23], s28 offen  lds
buffer_load_dwordx4 v4, s[20:23], s28 offen offset:1024  lds
buffer_load_dwordx4 v3, s[20:23], s28 offen offset:2048  lds
buffer_load_dwordx4 v4, s[20:23], s28 offen offset:3072  lds
s_waitcnt lgkmcnt(0)
s_barrier
v_add_u32 v130, 0x7000, v13
v_add_u32 v133, 0x19000, v13
ds_read_b64 v[96:97], v130 offset:0
v_add_u32 v128, 0x7000, v12
ds_read_b64 v[98:99], v130 offset:36864
v_add_u32 v129, 0x7000, v24
ds_read_b64 v[100:101], v133 offset:0
v_add_u32 v131, 0x19000, v12
ds_read_b64 v[102:103], v133 offset:36864
v_add_u32 v132, 0x19000, v24
ds_read_b32 v104, v128 offset:0
v_mov_b32 v137, s12
ds_read2_b32 v[108:109], v129 offset0:0 offset1:8
v_mov_b32 v139, s12
ds_read_b32 v105, v128 offset:36864
ds_read_b32 v110, v129 offset:36864
ds_read_b32 v111, v129 offset:36896
ds_read_b32 v106, v131 offset:0
ds_read2_b32 v[112:113], v132 offset0:0 offset1:8
ds_read_b32 v107, v131 offset:36864
ds_read_b32 v114, v132 offset:36864
ds_read_b32 v115, v132 offset:36896
s_waitcnt lgkmcnt(11)
v_max3_f32 v26, v96, v98, v100
s_waitcnt lgkmcnt(10)
v_max_f32 v26, v26, v102
v_sub_f32 v96, v96, v26
v_sub_f32 v98, v98, v26
v_exp_f32 v96, v96
v_sub_f32 v100, v100, v26
v_exp_f32 v98, v98
s_waitcnt lgkmcnt(9)
v_lshlrev_b32 v116, 16, v104
v_and_b32 v117, 0xffff0000, v104
s_waitcnt lgkmcnt(8)
v_perm_b32 v118, v108, v108, v25
v_perm_b32 v119, v109, v109, v25
v_sub_f32 v102, v102, v26
v_exp_f32 v100, v100
v_pk_add_f32 v[116:117], v[116:117], v[118:119]
s_waitcnt lgkmcnt(7)
v_lshlrev_b32 v120, 16, v105
v_and_b32 v121, 0xffff0000, v105
s_waitcnt lgkmcnt(6)
v_perm_b32 v122, v110, v110, v25
s_waitcnt lgkmcnt(5)
v_perm_b32 v123, v111, v111, v25
v_exp_f32 v102, v102
v_pk_mul_f32 v[134:135], v[116:117], v[96:97] op_sel_hi:[1,0]
v_pk_add_f32 v[120:121], v[120:121], v[122:123]
s_waitcnt lgkmcnt(4)
v_lshlrev_b32 v116, 16, v106
v_and_b32 v117, 0xffff0000, v106
s_waitcnt lgkmcnt(3)
v_perm_b32 v118, v112, v112, v25
v_perm_b32 v119, v113, v113, v25
v_pk_fma_f32 v[134:135], v[120:121], v[98:99], v[134:135] op_sel_hi:[1,0,1]
v_pk_add_f32 v[116:117], v[116:117], v[118:119]
s_waitcnt lgkmcnt(2)
v_lshlrev_b32 v120, 16, v107
v_and_b32 v121, 0xffff0000, v107
s_waitcnt lgkmcnt(1)
v_perm_b32 v122, v114, v114, v25
s_waitcnt lgkmcnt(0)
v_perm_b32 v123, v115, v115, v25
v_pk_fma_f32 v[134:135], v[116:117], v[100:101], v[134:135] op_sel_hi:[1,0,1]
v_pk_add_f32 v[120:121], v[120:121], v[122:123]
v_pk_fma_f32 v[134:135], v[120:121], v[102:103], v[134:135] op_sel_hi:[1,0,1]
v_mul_f32 v27, v96, v97
v_cvt_pk_bf16_f32 v136, v134, v135
v_fma_f32 v27, v98, v99, v27
v_fma_f32 v27, v100, v101, v27
v_fma_f32 v27, v102, v103, v27
v_mov_b32_dpp v138, v136 quad_perm:[1,0,3,2] row_mask:0xf bank_mask:0xf bound_ctrl:1
v_cndmask_b32 v136, v136, v26, s[56:57]
v_cndmask_b32 v138, v138, v27, s[56:57]
s_mov_b32 exec_lo, 0x55555557
s_mov_b32 exec_hi, 0x55555555
buffer_store_dwordx4 v[136:139], v7, s[24:27], 0 offen sc1
s_mov_b64 exec, -1
s_nop 0
v_add_u32 v7, v7, v8
s_mov_b32 s39, 0
Lqchk_47_%=:
ds_read_b128 v[96:99], v60 offset:2048
ds_read_b128 v[100:103], v60 offset:2560
ds_read_b128 v[104:107], v60 offset:3072
ds_read_b128 v[108:111], v60 offset:3584
s_waitcnt lgkmcnt(0)
v_bitop3_b32 v26, v97, v99, s12 bitop3:0x7e
v_bitop3_b32 v27, v101, v103, s12 bitop3:0x7e
v_bitop3_b32 v28, v105, v107, s12 bitop3:0x7e
v_bitop3_b32 v29, v109, v111, s12 bitop3:0x7e
v_or3_b32 v26, v26, v27, v28
v_or_b32 v26, v26, v29
v_cmp_ne_u32 vcc, 0, v26
v_mov_b32 v32, v96
v_mov_b32 v33, v98
v_mov_b32 v34, v100
v_mov_b32 v35, v102
v_mov_b32 v36, v104
v_mov_b32 v37, v106
v_mov_b32 v38, v108
v_mov_b32 v39, v110
s_cbranch_vccnz Lqslow_48_%=
Lqcont_49_%=:
buffer_load_dwordx4 v[224:227], v2, s[20:23], s31 offen 
buffer_load_dwordx4 v[228:231], v2, s[20:23], s31 offen offset:256 
buffer_load_dwordx4 v[232:235], v2, s[20:23], s31 offen offset:512 
buffer_load_dwordx4 v[236:239], v2, s[20:23], s31 offen offset:768 
buffer_load_dwordx4 v[240:243], v2, s[20:23], s32 offen 
buffer_load_dwordx4 v[244:247], v2, s[20:23], s32 offen offset:256 
buffer_load_dwordx4 v[248:251], v2, s[20:23], s32 offen offset:512 
buffer_load_dwordx4 v[252:255], v2, s[20:23], s32 offen offset:768 
Lit5_%=:
v_mfma_f32_16x16x32_bf16 v[128:131], a[128:131], v[32:35], 0
v_mfma_f32_16x16x32_bf16 v[132:135], a[136:139], v[32:35], 0
v_mfma_f32_16x16x32_bf16 v[128:131], a[132:135], v[36:39], v[128:131]
v_mfma_f32_16x16x32_bf16 v[132:135], a[140:143], v[36:39], v[132:135]
s_nop 7
v_permlane32_swap_b32 v128, v132
v_permlane32_swap_b32 v129, v133
v_permlane32_swap_b32 v130, v134
v_permlane32_swap_b32 v131, v135
v_add_f32_dpp v40, v132, v128 row_shl:4 row_mask:0xf bank_mask:0x1
v_add_f32_dpp v41, v133, v129 row_shl:4 row_mask:0xf bank_mask:0x1
v_add_f32_dpp v42, v134, v130 row_shl:4 row_mask:0xf bank_mask:0x1
v_add_f32_dpp v43, v135, v131 row_shl:4 row_mask:0xf bank_mask:0x1
s_barrier
s_add_u32 s38, s37, 0x7000
s_mov_b32 m0, s38
s_nop 0
buffer_load_dwordx4 v3, s[20:23], s29 offen  lds
buffer_load_dwordx4 v4, s[20:23], s29 offen offset:1024  lds
buffer_load_dwordx4 v3, s[20:23], s29 offen offset:2048  lds
buffer_load_dwordx4 v4, s[20:23], s29 offen offset:3072  lds
s_add_u32 s68, s64, Lit6_%=-Lcode0_%=
s_addc_u32 s69, s65, 0
s_and_b32 s69, s69, 0xffff
s_mov_b32 s70, Lit7_%=-Lit6_%=
s_mov_b32 s71, 0x20000
buffer_load_dword v61, v62, s[68:71], 0 offen
v_mfma_f32_16x16x32_bf16 v[128:131], a[144:147], v[32:35], 0
v_mfma_f32_16x16x32_bf16 v[132:135], a[152:155], v[32:35], 0
v_mfma_f32_16x16x32_bf16 v[128:131], a[148:151], v[36:39], v[128:131]
v_mfma_f32_16x16x32_bf16 v[132:135], a[156:159], v[36:39], v[132:135]
s_nop 7
v_permlane32_swap_b32 v128, v132
v_permlane32_swap_b32 v129, v133
v_permlane32_swap_b32 v130, v134
v_permlane32_swap_b32 v131, v135
v_add_f32_dpp v40, v128, v132 row_shr:4 row_mask:0xf bank_mask:0x2
v_add_f32_dpp v41, v129, v133 row_shr:4 row_mask:0xf bank_mask:0x2
v_add_f32_dpp v42, v130, v134 row_shr:4 row_mask:0xf bank_mask:0x2
v_add_f32_dpp v43, v131, v135 row_shr:4 row_mask:0xf bank_mask:0x2
s_add_u32 s38, s37, 0x8000
s_mov_b32 m0, s38
s_nop 0
buffer_load_dwordx4 v3, s[20:23], s30 offen  lds
buffer_load_dwordx4 v4, s[20:23], s30 offen offset:1024  lds
buffer_load_dwordx4 v3, s[20:23], s30 offen offset:2048  lds
buffer_load_dwordx4 v4, s[20:23], s30 offen offset:3072  lds
s_add_u32 s42, s42, 0x4000
s_cmp_lg_u32 s13, 0
s_cbranch_scc1 Lqdo_53_%=
s_mov_b32 m0, 149504
s_nop 0
buffer_load_dwordx4 v52, s[24:27], s42 offen  lds
buffer_load_dwordx4 v52, s[24:27], s42 offen offset:1024  lds
s_branch Lqdj_54_%=
Lqdo_53_%=:
buffer_load_dword v61, v0, s[24:27], 0 offen
buffer_load_dword v61, v0, s[24:27], 0 offen
Lqdj_54_%=:
s_waitcnt vmcnt(32)
v_mfma_f32_16x16x32_bf16 v[128:131], a[160:163], v[32:35], 0
v_mfma_f32_16x16x32_bf16 v[132:135], a[168:171], v[32:35], 0
v_mfma_f32_16x16x32_bf16 v[128:131], a[164:167], v[36:39], v[128:131]
v_mfma_f32_16x16x32_bf16 v[132:135], a[172:175], v[36:39], v[132:135]
s_nop 7
v_permlane32_swap_b32 v128, v132
v_permlane32_swap_b32 v129, v133
v_permlane32_swap_b32 v130, v134
v_permlane32_swap_b32 v131, v135
v_add_f32_dpp v40, v132, v128 row_shl:4 row_mask:0xf bank_mask:0x4
v_add_f32_dpp v41, v133, v129 row_shl:4 row_mask:0xf bank_mask:0x4
v_add_f32_dpp v42, v134, v130 row_shl:4 row_mask:0xf bank_mask:0x4
v_add_f32_dpp v43, v135, v131 row_shl:4 row_mask:0xf bank_mask:0x4
s_waitcnt vmcnt(28)
v_mfma_f32_16x16x32_bf16 v[128:131], a[176:179], v[32:35], v[56:59]
v_mfma_f32_16x16x32_bf16 v[132:135], a[184:187], v[32:35], v[56:59]
v_mfma_f32_16x16x32_bf16 v[128:131], a[180:183], v[36:39], v[128:131]
v_mfma_f32_16x16x32_bf16 v[132:135], a[188:191], v[36:39], v[132:135]
s_nop 7
v_permlane32_swap_b32 v128, v132
v_permlane32_swap_b32 v129, v133
v_permlane32_swap_b32 v130, v134
v_permlane32_swap_b32 v131, v135
v_add_f32_dpp v40, v128, v132 row_shr:4 row_mask:0xf bank_mask:0x8
v_add_f32_dpp v41, v129, v133 row_shr:4 row_mask:0xf bank_mask:0x8
v_add_f32_dpp v42, v130, v134 row_shr:4 row_mask:0xf bank_mask:0x8
v_add_f32_dpp v43, v131, v135 row_shr:4 row_mask:0xf bank_mask:0x8
s_add_u32 s16, s16, 0x1000000
s_addc_u32 s17, s17, 0
buffer_load_dwordx4 a[208:211], v1, s[16:19], s28 offen nt
buffer_load_dwordx4 a[212:215], v1, s[16:19], s28 offen offset:128 nt
buffer_load_dwordx4 a[216:219], v1, s[16:19], s28 offen offset:2048 nt
buffer_load_dwordx4 a[220:223], v1, s[16:19], s28 offen offset:2176 nt
s_waitcnt vmcnt(28)
v_mfma_f32_16x16x32_bf16 v[128:131], a[192:195], v[32:35], v[56:59]
v_max3_f32 v26, v40, v41, v42
v_mfma_f32_16x16x32_bf16 v[132:135], a[200:203], v[32:35], v[56:59]
v_mfma_f32_16x16x32_bf16 v[128:131], a[196:199], v[36:39], v[128:131]
v_mfma_f32_16x16x32_bf16 v[132:135], a[204:207], v[36:39], v[132:135]
s_nop 7
v_permlane32_swap_b32 v128, v132
v_permlane32_swap_b32 v129, v133
v_permlane32_swap_b32 v130, v134
v_permlane32_swap_b32 v131, v135
v_add_f32_dpp v44, v132, v128 row_shl:4 row_mask:0xf bank_mask:0x1
v_add_f32_dpp v45, v133, v129 row_shl:4 row_mask:0xf bank_mask:0x1
v_add_f32_dpp v46, v134, v130 row_shl:4 row_mask:0xf bank_mask:0x1
v_add_f32_dpp v47, v135, v131 row_shl:4 row_mask:0xf bank_mask:0x1
v_max3_f32 v27, v43, v44, v45
v_max3_f32 v26, v26, v46, v47
v_max_f32 v26, v26, v27
s_nop 1
v_max_f32_dpp v26, v26, v26 row_ror:4 row_mask:0xf bank_mask:0xf bound_ctrl:1
s_nop 1
v_max_f32_dpp v26, v26, v26 row_ror:8 row_mask:0xf bank_mask:0xf bound_ctrl:1
v_mov_b32 v27, v26
s_nop 1
v_permlane16_swap_b32 v26, v27
v_max_f32 v26, v26, v27
v_mov_b32 v27, v26
s_nop 1
v_permlane32_swap_b32 v26, v27
v_max_f32 v26, v26, v27
v_mul_f32 v22, s34, v26
v_fma_f32 v128, v40, s34, -v22
v_fma_f32 v129, v41, s34, -v22
v_fma_f32 v130, v42, s34, -v22
v_fma_f32 v131, v43, s34, -v22
v_fma_f32 v132, v44, s34, -v22
v_fma_f32 v133, v45, s34, -v22
v_fma_f32 v134, v46, s34, -v22
v_fma_f32 v135, v47, s34, -v22
v_exp_f32 v128, v128
v_exp_f32 v129, v129
v_exp_f32 v130, v130
v_exp_f32 v131, v131
v_exp_f32 v132, v132
v_exp_f32 v133, v133
v_exp_f32 v134, v134
v_exp_f32 v135, v135
v_add_f32 v26, v128, v129
v_add_f32 v27, v130, v131
v_add_f32 v28, v132, v133
v_add_f32 v29, v134, v135
v_add_f32 v26, v26, v27
v_add_f32 v28, v28, v29
v_add_f32 v26, v26, v28
v_cvt_pk_bf16_f32 v48, v128, v129
v_cvt_pk_bf16_f32 v49, v130, v131
v_add_f32_dpp v26, v26, v26 row_ror:4 row_mask:0xf bank_mask:0xf bound_ctrl:1
v_cvt_pk_bf16_f32 v50, v132, v133
v_cvt_pk_bf16_f32 v51, v134, v135
v_add_f32_dpp v26, v26, v26 row_ror:8 row_mask:0xf bank_mask:0xf bound_ctrl:1
v_mov_b32 v27, v26
s_nop 1
v_permlane16_swap_b32 v26, v27
v_add_f32 v26, v26, v27
v_mov_b32 v27, v26
s_nop 1
v_permlane32_swap_b32 v26, v27
v_add_f32 v26, v26, v27
v_mov_b32 v23, v26
buffer_load_dwordx4 a[224:227], v1, s[16:19], s29 offen nt
buffer_load_dwordx4 a[228:231], v1, s[16:19], s29 offen offset:128 nt
buffer_load_dwordx4 a[232:235], v1, s[16:19], s29 offen offset:2048 nt
buffer_load_dwordx4 a[236:239], v1, s[16:19], s29 offen offset:2176 nt
s_waitcnt vmcnt(19)
v_mov_b32_dpp v80, v48 row_shl:12 row_mask:0xf bank_mask:0x1
v_mov_b32_dpp v81, v49 row_shl:12 row_mask:0xf bank_mask:0x1
v_mov_b32_dpp v82, v50 quad_perm:[0,1,2,3] row_mask:0xf bank_mask:0x1
v_mov_b32_dpp v83, v51 quad_perm:[0,1,2,3] row_mask:0xf bank_mask:0x1
v_perm_b32 v96, v228, v224, s40
v_perm_b32 v97, v236, v232, s40
v_perm_b32 v98, v244, v240, s40
v_perm_b32 v99, v252, v248, s40
v_perm_b32 v112, v230, v226, s40
v_perm_b32 v113, v238, v234, s40
v_perm_b32 v114, v246, v242, s40
v_perm_b32 v115, v254, v250, s40
v_mfma_f32_16x16x32_bf16 v[72:75], v[80:83], v[96:99], 0
v_mov_b32_dpp v84, v48 row_shl:8 row_mask:0xf bank_mask:0x2
v_mov_b32_dpp v85, v49 row_shl:8 row_mask:0xf bank_mask:0x2
v_mfma_f32_16x16x32_bf16 v[76:79], v[80:83], v[112:115], 0
v_mov_b32_dpp v86, v50 row_shr:4 row_mask:0xf bank_mask:0x2
v_mov_b32_dpp v87, v51 row_shr:4 row_mask:0xf bank_mask:0x2
v_perm_b32 v100, v228, v224, s41
v_perm_b32 v101, v236, v232, s41
v_perm_b32 v102, v244, v240, s41
v_perm_b32 v103, v252, v248, s41
v_perm_b32 v116, v230, v226, s41
v_perm_b32 v117, v238, v234, s41
v_perm_b32 v118, v246, v242, s41
v_perm_b32 v119, v254, v250, s41
v_mfma_f32_16x16x32_bf16 v[72:75], v[84:87], v[100:103], v[72:75]
v_mov_b32_dpp v88, v48 row_shl:4 row_mask:0xf bank_mask:0x4
v_mov_b32_dpp v89, v49 row_shl:4 row_mask:0xf bank_mask:0x4
v_mfma_f32_16x16x32_bf16 v[76:79], v[84:87], v[116:119], v[76:79]
v_mov_b32_dpp v90, v50 row_shr:8 row_mask:0xf bank_mask:0x4
v_mov_b32_dpp v91, v51 row_shr:8 row_mask:0xf bank_mask:0x4
v_perm_b32 v104, v229, v225, s40
v_perm_b32 v105, v237, v233, s40
v_perm_b32 v106, v245, v241, s40
v_perm_b32 v107, v253, v249, s40
v_perm_b32 v120, v231, v227, s40
v_perm_b32 v121, v239, v235, s40
v_perm_b32 v122, v247, v243, s40
v_perm_b32 v123, v255, v251, s40
v_mfma_f32_16x16x32_bf16 v[72:75], v[88:91], v[104:107], v[72:75]
v_mov_b32_dpp v92, v48 quad_perm:[0,1,2,3] row_mask:0xf bank_mask:0x8
v_mov_b32_dpp v93, v49 quad_perm:[0,1,2,3] row_mask:0xf bank_mask:0x8
v_mfma_f32_16x16x32_bf16 v[76:79], v[88:91], v[120:123], v[76:79]
v_mov_b32_dpp v94, v50 row_shr:12 row_mask:0xf bank_mask:0x8
v_mov_b32_dpp v95, v51 row_shr:12 row_mask:0xf bank_mask:0x8
v_perm_b32 v108, v229, v225, s41
v_perm_b32 v109, v237, v233, s41
v_perm_b32 v110, v245, v241, s41
v_perm_b32 v111, v253, v249, s41
v_perm_b32 v124, v231, v227, s41
v_perm_b32 v125, v239, v235, s41
v_perm_b32 v126, v247, v243, s41
v_perm_b32 v127, v255, v251, s41
v_mfma_f32_16x16x32_bf16 v[72:75], v[92:95], v[108:111], v[72:75]
s_nop 0
v_mfma_f32_16x16x32_bf16 v[76:79], v[92:95], v[124:127], v[76:79]
s_waitcnt vmcnt(10)
s_cmp_lg_u32 s13, 0
s_cbranch_scc1 Lqdo_55_%=
s_mov_b32 m0, 147456
s_nop 0
buffer_load_dwordx4 v52, s[24:27], s43 offen sc1 lds
buffer_load_dwordx4 v52, s[24:27], s43 offen offset:1024 sc1 lds
s_branch Lqdj_56_%=
Lqdo_55_%=:
buffer_load_dword v61, v0, s[24:27], 0 offen
buffer_load_dword v61, v0, s[24:27], 0 offen
Lqdj_56_%=:
ds_read_b64_tr_b16 v[96:97], v14 offset:24576
v_mov_b32_dpp v80, v48 quad_perm:[0,1,2,3] row_mask:0xf bank_mask:0x1
ds_read_b64_tr_b16 v[98:99], v14 offset:28672
v_mov_b32_dpp v81, v49 quad_perm:[0,1,2,3] row_mask:0xf bank_mask:0x1
ds_read_b64_tr_b16 v[100:101], v15 offset:24576
v_mov_b32_dpp v82, v48 row_shl:4 row_mask:0xf bank_mask:0x1
ds_read_b64_tr_b16 v[102:103], v15 offset:28672
v_mov_b32_dpp v83, v49 row_shl:4 row_mask:0xf bank_mask:0x1
ds_read_b64_tr_b16 v[104:105], v16 offset:24576
v_mov_b32_dpp v84, v48 row_shr:4 row_mask:0xf bank_mask:0x2
ds_read_b64_tr_b16 v[106:107], v16 offset:28672
v_mov_b32_dpp v85, v49 row_shr:4 row_mask:0xf bank_mask:0x2
ds_read_b64_tr_b16 v[108:109], v17 offset:24576
v_mov_b32_dpp v86, v48 quad_perm:[0,1,2,3] row_mask:0xf bank_mask:0x2
ds_read_b64_tr_b16 v[110:111], v17 offset:28672
v_mov_b32_dpp v87, v49 quad_perm:[0,1,2,3] row_mask:0xf bank_mask:0x2
ds_read_b64_tr_b16 v[112:113], v18 offset:24576
v_mov_b32_dpp v88, v48 row_shr:8 row_mask:0xf bank_mask:0x4
ds_read_b64_tr_b16 v[114:115], v18 offset:28672
v_mov_b32_dpp v89, v49 row_shr:8 row_mask:0xf bank_mask:0x4
ds_read_b64_tr_b16 v[116:117], v19 offset:24576
v_mov_b32_dpp v90, v48 row_shr:4 row_mask:0xf bank_mask:0x4
ds_read_b64_tr_b16 v[118:119], v19 offset:28672
v_mov_b32_dpp v91, v49 row_shr:4 row_mask:0xf bank_mask:0x4
ds_read_b64_tr_b16 v[120:121], v20 offset:24576
v_mov_b32_dpp v92, v48 row_shr:12 row_mask:0xf bank_mask:0x8
ds_read_b64_tr_b16 v[122:123], v20 offset:28672
v_mov_b32_dpp v93, v49 row_shr:12 row_mask:0xf bank_mask:0x8
ds_read_b64_tr_b16 v[124:125], v21 offset:24576
v_mov_b32_dpp v94, v48 row_shr:8 row_mask:0xf bank_mask:0x8
ds_read_b64_tr_b16 v[126:127], v21 offset:28672
v_mov_b32_dpp v95, v49 row_shr:8 row_mask:0xf bank_mask:0x8
ds_read_b64_tr_b16 v[128:129], v14 offset:32768
s_waitcnt lgkmcnt(14)
v_mfma_f32_16x16x32_bf16 v[64:67], v[80:83], v[96:99], 0
ds_read_b64_tr_b16 v[130:131], v15 offset:32768
v_add_u32 v29, s35, v11
ds_read_b64_tr_b16 v[132:133], v16 offset:32768
s_waitcnt lgkmcnt(14)
v_mfma_f32_16x16x32_bf16 v[64:67], v[84:87], v[100:103], v[64:67]
ds_read_b64_tr_b16 v[134:135], v17 offset:32768
ds_read_b64_tr_b16 v[136:137], v18 offset:32768
s_waitcnt lgkmcnt(14)
v_mfma_f32_16x16x32_bf16 v[64:67], v[88:91], v[104:107], v[64:67]
ds_read_b64_tr_b16 v[138:139], v19 offset:32768
ds_read_b64_tr_b16 v[140:141], v20 offset:32768
s_waitcnt lgkmcnt(13)
v_mfma_f32_16x16x32_bf16 v[68:71], v[80:83], v[112:115], 0
ds_read_b64_tr_b16 v[142:143], v21 offset:32768
v_mov_b32_dpp v80, v48 row_shl:8 row_mask:0xf bank_mask:0x1
s_waitcnt lgkmcnt(12)
v_mfma_f32_16x16x32_bf16 v[68:71], v[84:87], v[116:119], v[68:71]
v_mov_b32_dpp v81, v49 row_shl:8 row_mask:0xf bank_mask:0x1
v_mov_b32_dpp v84, v48 row_shl:4 row_mask:0xf bank_mask:0x2
s_waitcnt lgkmcnt(10)
v_mfma_f32_16x16x32_bf16 v[68:71], v[88:91], v[120:123], v[68:71]
v_mov_b32_dpp v85, v49 row_shl:4 row_mask:0xf bank_mask:0x2
v_mov_b32_dpp v88, v48 quad_perm:[0,1,2,3] row_mask:0xf bank_mask:0x4
v_mfma_f32_16x16x32_bf16 v[64:67], v[92:95], v[108:111], v[64:67]
v_mov_b32_dpp v89, v49 quad_perm:[0,1,2,3] row_mask:0xf bank_mask:0x4
s_waitcnt lgkmcnt(8)
v_mfma_f32_16x16x32_bf16 v[68:71], v[92:95], v[124:127], v[68:71]
v_mov_b32_dpp v92, v48 row_shr:4 row_mask:0xf bank_mask:0x8
v_mov_b32_dpp v93, v49 row_shr:4 row_mask:0xf bank_mask:0x8
s_waitcnt lgkmcnt(7)
v_mfma_f32_16x16x16_bf16 v[64:67], v[80:81], v[128:129], v[64:67]
s_waitcnt lgkmcnt(3)
v_mfma_f32_16x16x16_bf16 v[68:71], v[80:81], v[136:137], v[68:71]
v_mfma_f32_16x16x16_bf16 v[64:67], v[84:85], v[130:131], v[64:67]
s_waitcnt lgkmcnt(2)
v_mfma_f32_16x16x16_bf16 v[68:71], v[84:85], v[138:139], v[68:71]
v_mfma_f32_16x16x16_bf16 v[64:67], v[88:89], v[132:133], v[64:67]
v_cvt_pk_bf16_f32 v132, v72, v76
v_cvt_pk_bf16_f32 v133, v73, v77
s_waitcnt lgkmcnt(1)
v_mfma_f32_16x16x16_bf16 v[68:71], v[88:89], v[140:141], v[68:71]
v_mfma_f32_16x16x16_bf16 v[64:67], v[92:93], v[134:135], v[64:67]
v_cvt_pk_bf16_f32 v134, v74, v78
v_cvt_pk_bf16_f32 v135, v75, v79
s_waitcnt lgkmcnt(0)
v_mfma_f32_16x16x16_bf16 v[68:71], v[92:93], v[142:143], v[68:71]
s_nop 7
v_cvt_pk_bf16_f32 v128, v64, v68
v_cvt_pk_bf16_f32 v129, v65, v69
v_cvt_pk_bf16_f32 v130, v66, v70
v_cvt_pk_bf16_f32 v131, v67, v71
ds_write2st64_b32 v10, v128, v129 offset0:0 offset1:1
ds_write2st64_b32 v10, v132, v133 offset0:4 offset1:5
ds_write2st64_b32 v10, v130, v131 offset0:2 offset1:3
ds_write2st64_b32 v10, v134, v135 offset0:6 offset1:7
ds_write_b64 v29, v[22:23] offset:2048
s_waitcnt lgkmcnt(0)
s_barrier
v_add_u32 v130, 0x25800, v13
ds_read_b64 v[96:97], v130 offset:0
v_add_u32 v128, 0x25800, v12
ds_read_b64 v[98:99], v130 offset:2176
v_add_u32 v129, 0x25800, v24
ds_read_b64 v[100:101], v130 offset:4352
v_mov_b32 v137, s12
ds_read_b64 v[102:103], v130 offset:6528
v_mov_b32 v139, s12
ds_read_b32 v104, v128 offset:0
ds_read2_b32 v[108:109], v129 offset0:0 offset1:8
ds_read_b32 v105, v128 offset:2176
ds_read_b32 v110, v129 offset:2176
ds_read_b32 v111, v129 offset:2208
ds_read_b32 v106, v128 offset:4352
ds_read_b32 v112, v129 offset:4352
ds_read_b32 v113, v129 offset:4384
ds_read_b32 v107, v128 offset:6528
ds_read_b32 v114, v129 offset:6528
ds_read_b32 v115, v129 offset:6560
s_waitcnt lgkmcnt(12)
v_max3_f32 v26, v96, v98, v100
s_waitcnt lgkmcnt(11)
v_max_f32 v26, v26, v102
v_sub_f32 v96, v96, v26
v_sub_f32 v98, v98, v26
v_exp_f32 v96, v96
v_sub_f32 v100, v100, v26
v_exp_f32 v98, v98
s_waitcnt lgkmcnt(10)
v_lshlrev_b32 v116, 16, v104
v_and_b32 v117, 0xffff0000, v104
s_waitcnt lgkmcnt(9)
v_perm_b32 v118, v108, v108, v25
v_perm_b32 v119, v109, v109, v25
v_sub_f32 v102, v102, v26
v_exp_f32 v100, v100
v_pk_add_f32 v[116:117], v[116:117], v[118:119]
s_waitcnt lgkmcnt(8)
v_lshlrev_b32 v120, 16, v105
v_and_b32 v121, 0xffff0000, v105
s_waitcnt lgkmcnt(7)
v_perm_b32 v122, v110, v110, v25
s_waitcnt lgkmcnt(6)
v_perm_b32 v123, v111, v111, v25
v_exp_f32 v102, v102
v_pk_mul_f32 v[134:135], v[116:117], v[96:97] op_sel_hi:[1,0]
v_pk_add_f32 v[120:121], v[120:121], v[122:123]
s_waitcnt lgkmcnt(5)
v_lshlrev_b32 v116, 16, v106
v_and_b32 v117, 0xffff0000, v106
s_waitcnt lgkmcnt(4)
v_perm_b32 v118, v112, v112, v25
s_waitcnt lgkmcnt(3)
v_perm_b32 v119, v113, v113, v25
v_pk_fma_f32 v[134:135], v[120:121], v[98:99], v[134:135] op_sel_hi:[1,0,1]
v_pk_add_f32 v[116:117], v[116:117], v[118:119]
s_waitcnt lgkmcnt(2)
v_lshlrev_b32 v120, 16, v107
v_and_b32 v121, 0xffff0000, v107
s_waitcnt lgkmcnt(1)
v_perm_b32 v122, v114, v114, v25
s_waitcnt lgkmcnt(0)
v_perm_b32 v123, v115, v115, v25
v_pk_fma_f32 v[134:135], v[116:117], v[100:101], v[134:135] op_sel_hi:[1,0,1]
v_pk_add_f32 v[120:121], v[120:121], v[122:123]
v_pk_fma_f32 v[134:135], v[120:121], v[102:103], v[134:135] op_sel_hi:[1,0,1]
v_mul_f32 v27, v96, v97
v_cvt_pk_bf16_f32 v136, v134, v135
v_fma_f32 v27, v98, v99, v27
v_fma_f32 v27, v100, v101, v27
v_fma_f32 v27, v102, v103, v27
v_mov_b32_dpp v138, v136 quad_perm:[1,0,3,2] row_mask:0xf bank_mask:0xf bound_ctrl:1
v_cndmask_b32 v136, v136, v26, s[56:57]
v_cndmask_b32 v138, v138, v27, s[56:57]
s_mov_b32 exec_lo, 0x55555557
s_mov_b32 exec_hi, 0x55555555
buffer_store_dwordx4 v[136:139], v6, s[24:27], 0 offen sc1
s_mov_b64 exec, -1
s_nop 0
v_add_u32 v6, v6, v8
s_waitcnt vmcnt(1)
s_barrier
s_mov_b32 s39, 0
Lqchk_57_%=:
ds_read_b128 v[96:99], v60 offset:0
ds_read_b128 v[100:103], v60 offset:512
ds_read_b128 v[104:107], v60 offset:1024
ds_read_b128 v[108:111], v60 offset:1536
s_waitcnt lgkmcnt(0)
v_bitop3_b32 v26, v97, v99, s12 bitop3:0x7e
v_bitop3_b32 v27, v101, v103, s12 bitop3:0x7e
v_bitop3_b32 v28, v105, v107, s12 bitop3:0x7e
v_bitop3_b32 v29, v109, v111, s12 bitop3:0x7e
v_or3_b32 v26, v26, v27, v28
v_or_b32 v26, v26, v29
v_cmp_ne_u32 vcc, 0, v26
v_mov_b32 v120, v96
v_mov_b32 v121, v98
v_mov_b32 v122, v100
v_mov_b32 v123, v102
v_mov_b32 v124, v104
v_mov_b32 v125, v106
v_mov_b32 v126, v108
v_mov_b32 v127, v110
s_cbranch_vccnz Lqslow_58_%=
Lqcont_59_%=:
s_add_u32 s43, s43, 0x4000
v_mfma_f32_16x16x32_bf16 v[128:131], a[240:243], v[120:123], 0
buffer_load_dwordx4 a[240:243], v1, s[16:19], s30 offen nt
v_perm_b32 v104, v165, v161, s40
v_mfma_f32_16x16x32_bf16 v[132:135], a[248:251], v[120:123], 0
v_perm_b32 v105, v173, v169, s40
v_perm_b32 v106, v181, v177, s40
v_mfma_f32_16x16x32_bf16 v[136:139], v[144:147], v[120:123], 0
v_perm_b32 v107, v189, v185, s40
v_perm_b32 v108, v165, v161, s41
v_mfma_f32_16x16x32_bf16 v[140:143], v[152:155], v[120:123], 0
v_perm_b32 v109, v173, v169, s41
v_perm_b32 v110, v181, v177, s41
v_mfma_f32_16x16x32_bf16 v[128:131], a[244:247], v[124:127], v[128:131]
buffer_load_dwordx4 a[244:247], v1, s[16:19], s30 offen offset:128 nt
buffer_load_dwordx4 a[248:251], v1, s[16:19], s30 offen offset:2048 nt
v_mfma_f32_16x16x32_bf16 v[132:135], a[252:255], v[124:127], v[132:135]
buffer_load_dwordx4 a[252:255], v1, s[16:19], s30 offen offset:2176 nt
buffer_load_dwordx4 v[144:147], v1, s[16:19], s31 offen nt
v_mfma_f32_16x16x32_bf16 v[136:139], v[148:151], v[124:127], v[136:139]
buffer_load_dwordx4 v[148:151], v1, s[16:19], s31 offen offset:128 nt
buffer_load_dwordx4 v[152:155], v1, s[16:19], s31 offen offset:2048 nt
v_mfma_f32_16x16x32_bf16 v[140:143], v[156:159], v[124:127], v[140:143]
buffer_load_dwordx4 v[156:159], v1, s[16:19], s31 offen offset:2176 nt
v_perm_b32 v111, v189, v185, s41
v_mfma_f32_16x16x32_bf16 v[96:99], a[0:3], v[120:123], 0
v_permlane32_swap_b32 v128, v132
v_permlane32_swap_b32 v129, v133
v_permlane32_swap_b32 v130, v134
v_permlane32_swap_b32 v131, v135
v_mfma_f32_16x16x32_bf16 v[100:103], a[8:11], v[120:123], 0
v_add_f32_dpp v40, v132, v128 row_shl:4 row_mask:0xf bank_mask:0x1
v_add_f32_dpp v41, v133, v129 row_shl:4 row_mask:0xf bank_mask:0x1
v_add_f32_dpp v42, v134, v130 row_shl:4 row_mask:0xf bank_mask:0x1
v_add_f32_dpp v43, v135, v131 row_shl:4 row_mask:0xf bank_mask:0x1
v_permlane32_swap_b32 v136, v140
v_permlane32_swap_b32 v137, v141
v_permlane32_swap_b32 v138, v142
v_permlane32_swap_b32 v139, v143
v_mfma_f32_16x16x32_bf16 v[128:131], a[16:19], v[120:123], v[56:59]
v_add_f32_dpp v40, v136, v140 row_shr:4 row_mask:0xf bank_mask:0x2
v_add_f32_dpp v41, v137, v141 row_shr:4 row_mask:0xf bank_mask:0x2
v_mfma_f32_16x16x32_bf16 v[132:135], a[24:27], v[120:123], v[56:59]
v_add_f32_dpp v42, v138, v142 row_shr:4 row_mask:0xf bank_mask:0x2
v_add_f32_dpp v43, v139, v143 row_shr:4 row_mask:0xf bank_mask:0x2
v_mfma_f32_16x16x32_bf16 v[96:99], a[4:7], v[124:127], v[96:99]
v_perm_b32 v112, v166, v162, s40
v_perm_b32 v113, v174, v170, s40
v_mfma_f32_16x16x32_bf16 v[100:103], a[12:15], v[124:127], v[100:103]
v_perm_b32 v114, v182, v178, s40
v_perm_b32 v115, v190, v186, s40
v_mfma_f32_16x16x32_bf16 v[136:139], a[32:35], v[120:123], v[56:59]
v_perm_b32 v116, v166, v162, s41
v_perm_b32 v117, v174, v170, s41
v_mfma_f32_16x16x32_bf16 v[140:143], a[40:43], v[120:123], v[56:59]
v_perm_b32 v118, v182, v178, s41
v_perm_b32 v119, v190, v186, s41
v_mfma_f32_16x16x32_bf16 v[128:131], a[20:23], v[124:127], v[128:131]
v_permlane32_swap_b32 v96, v100
v_permlane32_swap_b32 v97, v101
v_mfma_f32_16x16x32_bf16 v[132:135], a[28:31], v[124:127], v[132:135]
v_permlane32_swap_b32 v98, v102
v_permlane32_swap_b32 v99, v103
v_mfma_f32_16x16x32_bf16 v[136:139], a[36:39], v[124:127], v[136:139]
v_add_f32_dpp v40, v100, v96 row_shl:4 row_mask:0xf bank_mask:0x4
v_add_f32_dpp v41, v101, v97 row_shl:4 row_mask:0xf bank_mask:0x4
v_mfma_f32_16x16x32_bf16 v[140:143], a[44:47], v[124:127], v[140:143]
v_add_f32_dpp v42, v102, v98 row_shl:4 row_mask:0xf bank_mask:0x4
v_add_f32_dpp v43, v103, v99 row_shl:4 row_mask:0xf bank_mask:0x4
v_perm_b32 v96, v164, v160, s40
v_permlane32_swap_b32 v128, v132
v_permlane32_swap_b32 v129, v133
v_permlane32_swap_b32 v130, v134
v_permlane32_swap_b32 v131, v135
v_add_f32_dpp v40, v128, v132 row_shr:4 row_mask:0xf bank_mask:0x8
v_add_f32_dpp v41, v129, v133 row_shr:4 row_mask:0xf bank_mask:0x8
v_add_f32_dpp v42, v130, v134 row_shr:4 row_mask:0xf bank_mask:0x8
v_add_f32_dpp v43, v131, v135 row_shr:4 row_mask:0xf bank_mask:0x8
v_permlane32_swap_b32 v136, v140
v_permlane32_swap_b32 v137, v141
v_permlane32_swap_b32 v138, v142
v_permlane32_swap_b32 v139, v143
v_add_f32_dpp v44, v140, v136 row_shl:4 row_mask:0xf bank_mask:0x1
v_add_f32_dpp v45, v141, v137 row_shl:4 row_mask:0xf bank_mask:0x1
v_add_f32_dpp v46, v142, v138 row_shl:4 row_mask:0xf bank_mask:0x1
v_add_f32_dpp v47, v143, v139 row_shl:4 row_mask:0xf bank_mask:0x1
v_max3_f32 v26, v40, v41, v42
v_max3_f32 v27, v43, v44, v45
v_max3_f32 v26, v26, v46, v47
v_max_f32 v26, v26, v27
v_perm_b32 v97, v172, v168, s40
v_perm_b32 v98, v180, v176, s40
v_max_f32_dpp v26, v26, v26 row_ror:4 row_mask:0xf bank_mask:0xf bound_ctrl:1
v_perm_b32 v99, v188, v184, s40
v_perm_b32 v100, v164, v160, s41
v_max_f32_dpp v26, v26, v26 row_ror:8 row_mask:0xf bank_mask:0xf bound_ctrl:1
v_mov_b32 v27, v26
v_perm_b32 v101, v172, v168, s41
v_perm_b32 v102, v180, v176, s41
v_permlane16_swap_b32 v26, v27
v_max_f32 v26, v26, v27
v_mov_b32 v27, v26
v_perm_b32 v103, v188, v184, s41
v_perm_b32 v120, v167, v163, s40
v_permlane32_swap_b32 v26, v27
v_max_f32 v26, v26, v27
v_mul_f32 v22, s34, v26
v_fma_f32 v128, v40, s34, -v22
v_fma_f32 v129, v41, s34, -v22
v_fma_f32 v130, v42, s34, -v22
v_fma_f32 v131, v43, s34, -v22
v_fma_f32 v132, v44, s34, -v22
v_fma_f32 v133, v45, s34, -v22
v_fma_f32 v134, v46, s34, -v22
v_fma_f32 v135, v47, s34, -v22
v_exp_f32 v128, v128
v_exp_f32 v129, v129
v_exp_f32 v130, v130
v_exp_f32 v131, v131
v_exp_f32 v132, v132
v_exp_f32 v133, v133
v_exp_f32 v134, v134
v_exp_f32 v135, v135
v_cvt_pk_bf16_f32 v48, v128, v129
v_cvt_pk_bf16_f32 v49, v130, v131
v_cvt_pk_bf16_f32 v50, v132, v133
v_cvt_pk_bf16_f32 v51, v134, v135
v_mov_b32_dpp v80, v48 row_shl:12 row_mask:0xf bank_mask:0x1
v_mov_b32_dpp v81, v49 row_shl:12 row_mask:0xf bank_mask:0x1
v_mov_b32_dpp v82, v50 quad_perm:[0,1,2,3] row_mask:0xf bank_mask:0x1
v_mov_b32_dpp v83, v51 quad_perm:[0,1,2,3] row_mask:0xf bank_mask:0x1
v_mov_b32_dpp v84, v48 row_shl:8 row_mask:0xf bank_mask:0x2
v_mov_b32_dpp v85, v49 row_shl:8 row_mask:0xf bank_mask:0x2
v_mfma_f32_16x16x32_bf16 v[72:75], v[80:83], v[96:99], 0
v_mov_b32_dpp v86, v50 row_shr:4 row_mask:0xf bank_mask:0x2
v_mov_b32_dpp v87, v51 row_shr:4 row_mask:0xf bank_mask:0x2
v_mov_b32_dpp v88, v48 row_shl:4 row_mask:0xf bank_mask:0x4
v_mov_b32_dpp v89, v49 row_shl:4 row_mask:0xf bank_mask:0x4
v_mfma_f32_16x16x32_bf16 v[72:75], v[84:87], v[100:103], v[72:75]
v_mov_b32_dpp v90, v50 row_shr:8 row_mask:0xf bank_mask:0x4
v_mov_b32_dpp v91, v51 row_shr:8 row_mask:0xf bank_mask:0x4
v_mov_b32_dpp v92, v48 quad_perm:[0,1,2,3] row_mask:0xf bank_mask:0x8
v_mov_b32_dpp v93, v49 quad_perm:[0,1,2,3] row_mask:0xf bank_mask:0x8
v_mfma_f32_16x16x32_bf16 v[72:75], v[88:91], v[104:107], v[72:75]
v_mov_b32_dpp v94, v50 row_shr:12 row_mask:0xf bank_mask:0x8
v_mov_b32_dpp v95, v51 row_shr:12 row_mask:0xf bank_mask:0x8
v_mfma_f32_16x16x32_bf16 v[76:79], v[80:83], v[112:115], 0
ds_read_b64_tr_b16 v[96:97], v14 offset:0
v_perm_b32 v121, v175, v171, s40
v_mfma_f32_16x16x32_bf16 v[72:75], v[92:95], v[108:111], v[72:75]
ds_read_b64_tr_b16 v[98:99], v14 offset:4096
v_perm_b32 v122, v183, v179, s40
ds_read_b64_tr_b16 v[100:101], v15 offset:0
v_mfma_f32_16x16x32_bf16 v[76:79], v[84:87], v[116:119], v[76:79]
ds_read_b64_tr_b16 v[102:103], v15 offset:4096
v_perm_b32 v123, v191, v187, s40
ds_read_b64_tr_b16 v[104:105], v16 offset:0
v_perm_b32 v124, v167, v163, s41
ds_read_b64_tr_b16 v[106:107], v16 offset:4096
v_mfma_f32_16x16x32_bf16 v[76:79], v[88:91], v[120:123], v[76:79]
ds_read_b64_tr_b16 v[108:109], v17 offset:0
v_perm_b32 v125, v175, v171, s41
ds_read_b64_tr_b16 v[110:111], v17 offset:4096
v_perm_b32 v126, v183, v179, s41
ds_read_b64_tr_b16 v[112:113], v18 offset:0
v_perm_b32 v127, v191, v187, s41
ds_read_b64_tr_b16 v[114:115], v18 offset:4096
v_add_f32 v26, v128, v129
ds_read_b64_tr_b16 v[116:117], v19 offset:0
v_mfma_f32_16x16x32_bf16 v[76:79], v[92:95], v[124:127], v[76:79]
ds_read_b64_tr_b16 v[118:119], v19 offset:4096
v_add_f32 v27, v130, v131
ds_read_b64_tr_b16 v[120:121], v20 offset:0
v_add_f32 v28, v132, v133
ds_read_b64_tr_b16 v[122:123], v20 offset:4096
v_add_f32 v29, v134, v135
ds_read_b64_tr_b16 v[124:125], v21 offset:0
v_mov_b32_dpp v80, v48 quad_perm:[0,1,2,3] row_mask:0xf bank_mask:0x1
ds_read_b64_tr_b16 v[126:127], v21 offset:4096
v_mov_b32_dpp v81, v49 quad_perm:[0,1,2,3] row_mask:0xf bank_mask:0x1
ds_read_b64_tr_b16 v[128:129], v14 offset:8192
v_mov_b32_dpp v82, v48 row_shl:4 row_mask:0xf bank_mask:0x1
ds_read_b64_tr_b16 v[130:131], v15 offset:8192
v_mov_b32_dpp v83, v49 row_shl:4 row_mask:0xf bank_mask:0x1
ds_read_b64_tr_b16 v[132:133], v16 offset:8192
v_mov_b32_dpp v84, v48 row_shr:4 row_mask:0xf bank_mask:0x2
ds_read_b64_tr_b16 v[134:135], v17 offset:8192
s_waitcnt lgkmcnt(14)
v_mfma_f32_16x16x32_bf16 v[64:67], v[80:83], v[96:99], 0
ds_read_b64_tr_b16 v[136:137], v18 offset:8192
v_mov_b32_dpp v85, v49 row_shr:4 row_mask:0xf bank_mask:0x2
ds_read_b64_tr_b16 v[138:139], v19 offset:8192
v_mov_b32_dpp v86, v48 quad_perm:[0,1,2,3] row_mask:0xf bank_mask:0x2
ds_read_b64_tr_b16 v[140:141], v20 offset:8192
v_mov_b32_dpp v87, v49 quad_perm:[0,1,2,3] row_mask:0xf bank_mask:0x2
ds_read_b64_tr_b16 v[142:143], v21 offset:8192
s_waitcnt lgkmcnt(14)
v_mfma_f32_16x16x32_bf16 v[68:71], v[80:83], v[112:115], 0
v_mov_b32_dpp v88, v48 row_shr:8 row_mask:0xf bank_mask:0x4
v_mov_b32_dpp v89, v49 row_shr:8 row_mask:0xf bank_mask:0x4
v_mfma_f32_16x16x32_bf16 v[64:67], v[84:87], v[100:103], v[64:67]
v_mov_b32_dpp v90, v48 row_shr:4 row_mask:0xf bank_mask:0x4
v_mov_b32_dpp v91, v49 row_shr:4 row_mask:0xf bank_mask:0x4
s_waitcnt lgkmcnt(12)
v_mfma_f32_16x16x32_bf16 v[68:71], v[84:87], v[116:119], v[68:71]
v_mov_b32_dpp v92, v48 row_shr:12 row_mask:0xf bank_mask:0x8
v_mov_b32_dpp v93, v49 row_shr:12 row_mask:0xf bank_mask:0x8
v_mfma_f32_16x16x32_bf16 v[64:67], v[88:91], v[104:107], v[64:67]
v_mov_b32_dpp v94, v48 row_shr:8 row_mask:0xf bank_mask:0x8
v_mov_b32_dpp v95, v49 row_shr:8 row_mask:0xf bank_mask:0x8
s_waitcnt lgkmcnt(10)
v_mfma_f32_16x16x32_bf16 v[68:71], v[88:91], v[120:123], v[68:71]
v_mov_b32_dpp v80, v48 row_shl:8 row_mask:0xf bank_mask:0x1
v_mov_b32_dpp v81, v49 row_shl:8 row_mask:0xf bank_mask:0x1
v_mfma_f32_16x16x32_bf16 v[64:67], v[92:95], v[108:111], v[64:67]
v_mov_b32_dpp v84, v48 row_shl:4 row_mask:0xf bank_mask:0x2
v_mov_b32_dpp v85, v49 row_shl:4 row_mask:0xf bank_mask:0x2
s_waitcnt lgkmcnt(8)
v_mfma_f32_16x16x32_bf16 v[68:71], v[92:95], v[124:127], v[68:71]
v_mov_b32_dpp v88, v48 quad_perm:[0,1,2,3] row_mask:0xf bank_mask:0x4
v_mov_b32_dpp v89, v49 quad_perm:[0,1,2,3] row_mask:0xf bank_mask:0x4
s_waitcnt lgkmcnt(7)
v_mfma_f32_16x16x16_bf16 v[64:67], v[80:81], v[128:129], v[64:67]
v_add_f32 v26, v26, v27
v_add_f32 v28, v28, v29
s_waitcnt lgkmcnt(3)
v_mfma_f32_16x16x16_bf16 v[68:71], v[80:81], v[136:137], v[68:71]
v_add_f32 v26, v26, v28
v_mov_b32_dpp v92, v48 row_shr:4 row_mask:0xf bank_mask:0x8
v_mfma_f32_16x16x16_bf16 v[64:67], v[84:85], v[130:131], v[64:67]
v_mov_b32_dpp v93, v49 row_shr:4 row_mask:0xf bank_mask:0x8
v_add_f32_dpp v26, v26, v26 row_ror:4 row_mask:0xf bank_mask:0xf bound_ctrl:1
s_waitcnt lgkmcnt(2)
v_mfma_f32_16x16x16_bf16 v[68:71], v[84:85], v[138:139], v[68:71]
v_add_f32_dpp v26, v26, v26 row_ror:8 row_mask:0xf bank_mask:0xf bound_ctrl:1
v_mov_b32 v27, v26
v_mfma_f32_16x16x16_bf16 v[64:67], v[88:89], v[132:133], v[64:67]
s_nop 0
v_permlane16_swap_b32 v26, v27
v_add_f32 v26, v26, v27
s_waitcnt lgkmcnt(1)
v_mfma_f32_16x16x16_bf16 v[68:71], v[88:89], v[140:141], v[68:71]
v_mov_b32 v27, v26
v_cvt_pk_bf16_f32 v132, v72, v76
v_mfma_f32_16x16x16_bf16 v[64:67], v[92:93], v[134:135], v[64:67]
v_permlane32_swap_b32 v26, v27
v_add_f32 v26, v26, v27
s_waitcnt lgkmcnt(0)
v_mfma_f32_16x16x16_bf16 v[68:71], v[92:93], v[142:143], v[68:71]
v_mov_b32 v23, v26
v_cvt_pk_bf16_f32 v133, v73, v77
v_cvt_pk_bf16_f32 v134, v74, v78
v_cvt_pk_bf16_f32 v135, v75, v79
s_nop 3
v_cvt_pk_bf16_f32 v128, v64, v68
v_cvt_pk_bf16_f32 v129, v65, v69
v_cvt_pk_bf16_f32 v130, v66, v70
v_cvt_pk_bf16_f32 v131, v67, v71
ds_write2st64_b32 v9, v128, v129 offset0:16 offset1:17
ds_write2st64_b32 v9, v132, v133 offset0:20 offset1:21
ds_write2st64_b32 v9, v130, v131 offset0:18 offset1:19
ds_write2st64_b32 v9, v134, v135 offset0:22 offset1:23
ds_write_b64 v11, v[22:23] offset:6144
buffer_load_dwordx4 a[0:3], v1, s[16:19], s32 offen nt
buffer_load_dwordx4 a[4:7], v1, s[16:19], s32 offen offset:128 nt
buffer_load_dwordx4 a[8:11], v1, s[16:19], s32 offen offset:2048 nt
buffer_load_dwordx4 a[12:15], v1, s[16:19], s32 offen offset:2176 nt
s_add_u32 s20, s20, 0x1000000
s_addc_u32 s21, s21, 0
s_add_u32 s38, s37, 0
s_mov_b32 m0, s38
s_nop 0
buffer_load_dwordx4 v3, s[20:23], s28 offen  lds
buffer_load_dwordx4 v4, s[20:23], s28 offen offset:1024  lds
buffer_load_dwordx4 v3, s[20:23], s28 offen offset:2048  lds
buffer_load_dwordx4 v4, s[20:23], s28 offen offset:3072  lds
s_waitcnt lgkmcnt(0)
s_barrier
v_add_u32 v130, 0x1000, v13
v_add_u32 v133, 0x13000, v13
ds_read_b64 v[96:97], v130 offset:0
v_add_u32 v128, 0x1000, v12
ds_read_b64 v[98:99], v130 offset:36864
v_add_u32 v129, 0x1000, v24
ds_read_b64 v[100:101], v133 offset:0
v_add_u32 v131, 0x13000, v12
ds_read_b64 v[102:103], v133 offset:36864
v_add_u32 v132, 0x13000, v24
ds_read_b32 v104, v128 offset:0
v_mov_b32 v137, s12
ds_read2_b32 v[108:109], v129 offset0:0 offset1:8
v_mov_b32 v139, s12
ds_read_b32 v105, v128 offset:36864
ds_read_b32 v110, v129 offset:36864
ds_read_b32 v111, v129 offset:36896
ds_read_b32 v106, v131 offset:0
ds_read2_b32 v[112:113], v132 offset0:0 offset1:8
ds_read_b32 v107, v131 offset:36864
ds_read_b32 v114, v132 offset:36864
ds_read_b32 v115, v132 offset:36896
s_waitcnt lgkmcnt(11)
v_max3_f32 v26, v96, v98, v100
s_waitcnt lgkmcnt(10)
v_max_f32 v26, v26, v102
v_sub_f32 v96, v96, v26
v_sub_f32 v98, v98, v26
v_exp_f32 v96, v96
v_sub_f32 v100, v100, v26
v_exp_f32 v98, v98
s_waitcnt lgkmcnt(9)
v_lshlrev_b32 v116, 16, v104
v_and_b32 v117, 0xffff0000, v104
s_waitcnt lgkmcnt(8)
v_perm_b32 v118, v108, v108, v25
v_perm_b32 v119, v109, v109, v25
v_sub_f32 v102, v102, v26
v_exp_f32 v100, v100
v_pk_add_f32 v[116:117], v[116:117], v[118:119]
s_waitcnt lgkmcnt(7)
v_lshlrev_b32 v120, 16, v105
v_and_b32 v121, 0xffff0000, v105
s_waitcnt lgkmcnt(6)
v_perm_b32 v122, v110, v110, v25
s_waitcnt lgkmcnt(5)
v_perm_b32 v123, v111, v111, v25
v_exp_f32 v102, v102
v_pk_mul_f32 v[134:135], v[116:117], v[96:97] op_sel_hi:[1,0]
v_pk_add_f32 v[120:121], v[120:121], v[122:123]
s_waitcnt lgkmcnt(4)
v_lshlrev_b32 v116, 16, v106
v_and_b32 v117, 0xffff0000, v106
s_waitcnt lgkmcnt(3)
v_perm_b32 v118, v112, v112, v25
v_perm_b32 v119, v113, v113, v25
v_pk_fma_f32 v[134:135], v[120:121], v[98:99], v[134:135] op_sel_hi:[1,0,1]
v_pk_add_f32 v[116:117], v[116:117], v[118:119]
s_waitcnt lgkmcnt(2)
v_lshlrev_b32 v120, 16, v107
v_and_b32 v121, 0xffff0000, v107
s_waitcnt lgkmcnt(1)
v_perm_b32 v122, v114, v114, v25
s_waitcnt lgkmcnt(0)
v_perm_b32 v123, v115, v115, v25
v_pk_fma_f32 v[134:135], v[116:117], v[100:101], v[134:135] op_sel_hi:[1,0,1]
v_pk_add_f32 v[120:121], v[120:121], v[122:123]
v_pk_fma_f32 v[134:135], v[120:121], v[102:103], v[134:135] op_sel_hi:[1,0,1]
v_mul_f32 v27, v96, v97
v_cvt_pk_bf16_f32 v136, v134, v135
v_fma_f32 v27, v98, v99, v27
v_fma_f32 v27, v100, v101, v27
v_fma_f32 v27, v102, v103, v27
v_mov_b32_dpp v138, v136 quad_perm:[1,0,3,2] row_mask:0xf bank_mask:0xf bound_ctrl:1
v_cndmask_b32 v136, v136, v26, s[56:57]
v_cndmask_b32 v138, v138, v27, s[56:57]
s_mov_b32 exec_lo, 0x55555557
s_mov_b32 exec_hi, 0x55555555
buffer_store_dwordx4 v[136:139], v7, s[24:27], 0 offen sc1
s_mov_b64 exec, -1
s_nop 0
v_add_u32 v7, v7, v8
s_mov_b32 s39, 0
Lqchk_63_%=:
ds_read_b128 v[96:99], v60 offset:2048
ds_read_b128 v[100:103], v60 offset:2560
ds_read_b128 v[104:107], v60 offset:3072
ds_read_b128 v[108:111], v60 offset:3584
s_waitcnt lgkmcnt(0)
v_bitop3_b32 v26, v97, v99, s12 bitop3:0x7e
v_bitop3_b32 v27, v101, v103, s12 bitop3:0x7e
v_bitop3_b32 v28, v105, v107, s12 bitop3:0x7e
v_bitop3_b32 v29, v109, v111, s12 bitop3:0x7e
v_or3_b32 v26, v26, v27, v28
v_or_b32 v26, v26, v29
v_cmp_ne_u32 vcc, 0, v26
v_mov_b32 v32, v96
v_mov_b32 v33, v98
v_mov_b32 v34, v100
v_mov_b32 v35, v102
v_mov_b32 v36, v104
v_mov_b32 v37, v106
v_mov_b32 v38, v108
v_mov_b32 v39, v110
s_cbranch_vccnz Lqslow_64_%=
Lqcont_65_%=:
buffer_load_dwordx4 v[160:163], v2, s[20:23], s31 offen 
buffer_load_dwordx4 v[164:167], v2, s[20:23], s31 offen offset:256 
buffer_load_dwordx4 v[168:171], v2, s[20:23], s31 offen offset:512 
buffer_load_dwordx4 v[172:175], v2, s[20:23], s31 offen offset:768 
buffer_load_dwordx4 v[176:179], v2, s[20:23], s32 offen 
buffer_load_dwordx4 v[180:183], v2, s[20:23], s32 offen offset:256 
buffer_load_dwordx4 v[184:187], v2, s[20:23], s32 offen offset:512 
buffer_load_dwordx4 v[188:191], v2, s[20:23], s32 offen offset:768 
Lit6_%=:
v_mfma_f32_16x16x32_bf16 v[128:131], a[208:211], v[32:35], 0
v_mfma_f32_16x16x32_bf16 v[132:135], a[216:219], v[32:35], 0
v_mfma_f32_16x16x32_bf16 v[128:131], a[212:215], v[36:39], v[128:131]
v_mfma_f32_16x16x32_bf16 v[132:135], a[220:223], v[36:39], v[132:135]
s_nop 7
v_permlane32_swap_b32 v128, v132
v_permlane32_swap_b32 v129, v133
v_permlane32_swap_b32 v130, v134
v_permlane32_swap_b32 v131, v135
v_add_f32_dpp v40, v132, v128 row_shl:4 row_mask:0xf bank_mask:0x1
v_add_f32_dpp v41, v133, v129 row_shl:4 row_mask:0xf bank_mask:0x1
v_add_f32_dpp v42, v134, v130 row_shl:4 row_mask:0xf bank_mask:0x1
v_add_f32_dpp v43, v135, v131 row_shl:4 row_mask:0xf bank_mask:0x1
s_barrier
s_add_u32 s38, s37, 0x1000
s_mov_b32 m0, s38
s_nop 0
buffer_load_dwordx4 v3, s[20:23], s29 offen  lds
buffer_load_dwordx4 v4, s[20:23], s29 offen offset:1024  lds
buffer_load_dwordx4 v3, s[20:23], s29 offen offset:2048  lds
buffer_load_dwordx4 v4, s[20:23], s29 offen offset:3072  lds
s_add_u32 s68, s64, Lit7_%=-Lcode0_%=
s_addc_u32 s69, s65, 0
s_and_b32 s69, s69, 0xffff
s_mov_b32 s70, Lit8_%=-Lit7_%=
s_mov_b32 s71, 0x20000
buffer_load_dword v61, v62, s[68:71], 0 offen
v_mfma_f32_16x16x32_bf16 v[128:131], a[224:227], v[32:35], 0
v_mfma_f32_16x16x32_bf16 v[132:135], a[232:235], v[32:35], 0
v_mfma_f32_16x16x32_bf16 v[128:131], a[228:231], v[36:39], v[128:131]
v_mfma_f32_16x16x32_bf16 v[132:135], a[236:239], v[36:39], v[132:135]
s_nop 7
v_permlane32_swap_b32 v128, v132
v_permlane32_swap_b32 v129, v133
v_permlane32_swap_b32 v130, v134
v_permlane32_swap_b32 v131, v135
v_add_f32_dpp v40, v128, v132 row_shr:4 row_mask:0xf bank_mask:0x2
v_add_f32_dpp v41, v129, v133 row_shr:4 row_mask:0xf bank_mask:0x2
v_add_f32_dpp v42, v130, v134 row_shr:4 row_mask:0xf bank_mask:0x2
v_add_f32_dpp v43, v131, v135 row_shr:4 row_mask:0xf bank_mask:0x2
s_add_u32 s38, s37, 0x2000
s_mov_b32 m0, s38
s_nop 0
buffer_load_dwordx4 v3, s[20:23], s30 offen  lds
buffer_load_dwordx4 v4, s[20:23], s30 offen offset:1024  lds
buffer_load_dwordx4 v3, s[20:23], s30 offen offset:2048  lds
buffer_load_dwordx4 v4, s[20:23], s30 offen offset:3072  lds
s_add_u32 s42, s42, 0x4000
s_cmp_lg_u32 s13, 0
s_cbranch_scc1 Lqdo_69_%=
s_mov_b32 m0, 149504
s_nop 0
buffer_load_dwordx4 v52, s[24:27], s42 offen  lds
buffer_load_dwordx4 v52, s[24:27], s42 offen offset:1024  lds
s_branch Lqdj_70_%=
Lqdo_69_%=:
buffer_load_dword v61, v0, s[24:27], 0 offen
buffer_load_dword v61, v0, s[24:27], 0 offen
Lqdj_70_%=:
s_waitcnt vmcnt(32)
v_mfma_f32_16x16x32_bf16 v[128:131], a[240:243], v[32:35], 0
v_mfma_f32_16x16x32_bf16 v[132:135], a[248:251], v[32:35], 0
v_mfma_f32_16x16x32_bf16 v[128:131], a[244:247], v[36:39], v[128:131]
v_mfma_f32_16x16x32_bf16 v[132:135], a[252:255], v[36:39], v[132:135]
s_nop 7
v_permlane32_swap_b32 v128, v132
v_permlane32_swap_b32 v129, v133
v_permlane32_swap_b32 v130, v134
v_permlane32_swap_b32 v131, v135
v_add_f32_dpp v40, v132, v128 row_shl:4 row_mask:0xf bank_mask:0x4
v_add_f32_dpp v41, v133, v129 row_shl:4 row_mask:0xf bank_mask:0x4
v_add_f32_dpp v42, v134, v130 row_shl:4 row_mask:0xf bank_mask:0x4
v_add_f32_dpp v43, v135, v131 row_shl:4 row_mask:0xf bank_mask:0x4
s_waitcnt vmcnt(28)
v_mfma_f32_16x16x32_bf16 v[128:131], v[144:147], v[32:35], v[56:59]
v_mfma_f32_16x16x32_bf16 v[132:135], v[152:155], v[32:35], v[56:59]
v_mfma_f32_16x16x32_bf16 v[128:131], v[148:151], v[36:39], v[128:131]
v_mfma_f32_16x16x32_bf16 v[132:135], v[156:159], v[36:39], v[132:135]
s_nop 7
v_permlane32_swap_b32 v128, v132
v_permlane32_swap_b32 v129, v133
v_permlane32_swap_b32 v130, v134
v_permlane32_swap_b32 v131, v135
v_add_f32_dpp v40, v128, v132 row_shr:4 row_mask:0xf bank_mask:0x8
v_add_f32_dpp v41, v129, v133 row_shr:4 row_mask:0xf bank_mask:0x8
v_add_f32_dpp v42, v130, v134 row_shr:4 row_mask:0xf bank_mask:0x8
v_add_f32_dpp v43, v131, v135 row_shr:4 row_mask:0xf bank_mask:0x8
s_add_u32 s16, s16, 0x1000000
s_addc_u32 s17, s17, 0
buffer_load_dwordx4 a[16:19], v1, s[16:19], s28 offen nt
buffer_load_dwordx4 a[20:23], v1, s[16:19], s28 offen offset:128 nt
buffer_load_dwordx4 a[24:27], v1, s[16:19], s28 offen offset:2048 nt
buffer_load_dwordx4 a[28:31], v1, s[16:19], s28 offen offset:2176 nt
s_waitcnt vmcnt(28)
v_mfma_f32_16x16x32_bf16 v[128:131], a[0:3], v[32:35], v[56:59]
v_max3_f32 v26, v40, v41, v42
v_mfma_f32_16x16x32_bf16 v[132:135], a[8:11], v[32:35], v[56:59]
v_mfma_f32_16x16x32_bf16 v[128:131], a[4:7], v[36:39], v[128:131]
v_mfma_f32_16x16x32_bf16 v[132:135], a[12:15], v[36:39], v[132:135]
s_nop 7
v_permlane32_swap_b32 v128, v132
v_permlane32_swap_b32 v129, v133
v_permlane32_swap_b32 v130, v134
v_permlane32_swap_b32 v131, v135
v_add_f32_dpp v44, v132, v128 row_shl:4 row_mask:0xf bank_mask:0x1
v_add_f32_dpp v45, v133, v129 row_shl:4 row_mask:0xf bank_mask:0x1
v_add_f32_dpp v46, v134, v130 row_shl:4 row_mask:0xf bank_mask:0x1
v_add_f32_dpp v47, v135, v131 row_shl:4 row_mask:0xf bank_mask:0x1
v_max3_f32 v27, v43, v44, v45
v_max3_f32 v26, v26, v46, v47
v_max_f32 v26, v26, v27
s_nop 1
v_max_f32_dpp v26, v26, v26 row_ror:4 row_mask:0xf bank_mask:0xf bound_ctrl:1
s_nop 1
v_max_f32_dpp v26, v26, v26 row_ror:8 row_mask:0xf bank_mask:0xf bound_ctrl:1
v_mov_b32 v27, v26
s_nop 1
v_permlane16_swap_b32 v26, v27
v_max_f32 v26, v26, v27
v_mov_b32 v27, v26
s_nop 1
v_permlane32_swap_b32 v26, v27
v_max_f32 v26, v26, v27
v_mul_f32 v22, s34, v26
v_fma_f32 v128, v40, s34, -v22
v_fma_f32 v129, v41, s34, -v22
v_fma_f32 v130, v42, s34, -v22
v_fma_f32 v131, v43, s34, -v22
v_fma_f32 v132, v44, s34, -v22
v_fma_f32 v133, v45, s34, -v22
v_fma_f32 v134, v46, s34, -v22
v_fma_f32 v135, v47, s34, -v22
v_exp_f32 v128, v128
v_exp_f32 v129, v129
v_exp_f32 v130, v130
v_exp_f32 v131, v131
v_exp_f32 v132, v132
v_exp_f32 v133, v133
v_exp_f32 v134, v134
v_exp_f32 v135, v135
v_add_f32 v26, v128, v129
v_add_f32 v27, v130, v131
v_add_f32 v28, v132, v133
v_add_f32 v29, v134, v135
v_add_f32 v26, v26, v27
v_add_f32 v28, v28, v29
v_add_f32 v26, v26, v28
v_cvt_pk_bf16_f32 v48, v128, v129
v_cvt_pk_bf16_f32 v49, v130, v131
v_add_f32_dpp v26, v26, v26 row_ror:4 row_mask:0xf bank_mask:0xf bound_ctrl:1
v_cvt_pk_bf16_f32 v50, v132, v133
v_cvt_pk_bf16_f32 v51, v134, v135
v_add_f32_dpp v26, v26, v26 row_ror:8 row_mask:0xf bank_mask:0xf bound_ctrl:1
v_mov_b32 v27, v26
s_nop 1
v_permlane16_swap_b32 v26, v27
v_add_f32 v26, v26, v27
v_mov_b32 v27, v26
s_nop 1
v_permlane32_swap_b32 v26, v27
v_add_f32 v26, v26, v27
v_mov_b32 v23, v26
buffer_load_dwordx4 a[32:35], v1, s[16:19], s29 offen nt
buffer_load_dwordx4 a[36:39], v1, s[16:19], s29 offen offset:128 nt
buffer_load_dwordx4 a[40:43], v1, s[16:19], s29 offen offset:2048 nt
buffer_load_dwordx4 a[44:47], v1, s[16:19], s29 offen offset:2176 nt
s_waitcnt vmcnt(19)
v_mov_b32_dpp v80, v48 row_shl:12 row_mask:0xf bank_mask:0x1
v_mov_b32_dpp v81, v49 row_shl:12 row_mask:0xf bank_mask:0x1
v_mov_b32_dpp v82, v50 quad_perm:[0,1,2,3] row_mask:0xf bank_mask:0x1
v_mov_b32_dpp v83, v51 quad_perm:[0,1,2,3] row_mask:0xf bank_mask:0x1
v_perm_b32 v96, v164, v160, s40
v_perm_b32 v97, v172, v168, s40
v_perm_b32 v98, v180, v176, s40
v_perm_b32 v99, v188, v184, s40
v_perm_b32 v112, v166, v162, s40
v_perm_b32 v113, v174, v170, s40
v_perm_b32 v114, v182, v178, s40
v_perm_b32 v115, v190, v186, s40
v_mfma_f32_16x16x32_bf16 v[72:75], v[80:83], v[96:99], 0
v_mov_b32_dpp v84, v48 row_shl:8 row_mask:0xf bank_mask:0x2
v_mov_b32_dpp v85, v49 row_shl:8 row_mask:0xf bank_mask:0x2
v_mfma_f32_16x16x32_bf16 v[76:79], v[80:83], v[112:115], 0
v_mov_b32_dpp v86, v50 row_shr:4 row_mask:0xf bank_mask:0x2
v_mov_b32_dpp v87, v51 row_shr:4 row_mask:0xf bank_mask:0x2
v_perm_b32 v100, v164, v160, s41
v_perm_b32 v101, v172, v168, s41
v_perm_b32 v102, v180, v176, s41
v_perm_b32 v103, v188, v184, s41
v_perm_b32 v116, v166, v162, s41
v_perm_b32 v117, v174, v170, s41
v_perm_b32 v118, v182, v178, s41
v_perm_b32 v119, v190, v186, s41
v_mfma_f32_16x16x32_bf16 v[72:75], v[84:87], v[100:103], v[72:75]
v_mov_b32_dpp v88, v48 row_shl:4 row_mask:0xf bank_mask:0x4
v_mov_b32_dpp v89, v49 row_shl:4 row_mask:0xf bank_mask:0x4
v_mfma_f32_16x16x32_bf16 v[76:79], v[84:87], v[116:119], v[76:79]
v_mov_b32_dpp v90, v50 row_shr:8 row_mask:0xf bank_mask:0x4
v_mov_b32_dpp v91, v51 row_shr:8 row_mask:0xf bank_mask:0x4
v_perm_b32 v104, v165, v161, s40
v_perm_b32 v105, v173, v169, s40
v_perm_b32 v106, v181, v177, s40
v_perm_b32 v107, v189, v185, s40
v_perm_b32 v120, v167, v163, s40
v_perm_b32 v121, v175, v171, s40
v_perm_b32 v122, v183, v179, s40
v_perm_b32 v123, v191, v187, s40
v_mfma_f32_16x16x32_bf16 v[72:75], v[88:91], v[104:107], v[72:75]
v_mov_b32_dpp v92, v48 quad_perm:[0,1,2,3] row_mask:0xf bank_mask:0x8
v_mov_b32_dpp v93, v49 quad_perm:[0,1,2,3] row_mask:0xf bank_mask:0x8
v_mfma_f32_16x16x32_bf16 v[76:79], v[88:91], v[120:123], v[76:79]
v_mov_b32_dpp v94, v50 row_shr:12 row_mask:0xf bank_mask:0x8
v_mov_b32_dpp v95, v51 row_shr:12 row_mask:0xf bank_mask:0x8
v_perm_b32 v108, v165, v161, s41
v_perm_b32 v109, v173, v169, s41
v_perm_b32 v110, v181, v177, s41
v_perm_b32 v111, v189, v185, s41
v_perm_b32 v124, v167, v163, s41
v_perm_b32 v125, v175, v171, s41
v_perm_b32 v126, v183, v179, s41
v_perm_b32 v127, v191, v187, s41
v_mfma_f32_16x16x32_bf16 v[72:75], v[92:95], v[108:111], v[72:75]
s_nop 0
v_mfma_f32_16x16x32_bf16 v[76:79], v[92:95], v[124:127], v[76:79]
s_waitcnt vmcnt(10)
s_cmp_lg_u32 s13, 0
s_cbranch_scc1 Lqdo_71_%=
s_mov_b32 m0, 147456
s_nop 0
buffer_load_dwordx4 v52, s[24:27], s43 offen sc1 lds
buffer_load_dwordx4 v52, s[24:27], s43 offen offset:1024 sc1 lds
s_branch Lqdj_72_%=
Lqdo_71_%=:
buffer_load_dword v61, v0, s[24:27], 0 offen
buffer_load_dword v61, v0, s[24:27], 0 offen
Lqdj_72_%=:
ds_read_b64_tr_b16 v[96:97], v14 offset:0
v_mov_b32_dpp v80, v48 quad_perm:[0,1,2,3] row_mask:0xf bank_mask:0x1
ds_read_b64_tr_b16 v[98:99], v14 offset:4096
v_mov_b32_dpp v81, v49 quad_perm:[0,1,2,3] row_mask:0xf bank_mask:0x1
ds_read_b64_tr_b16 v[100:101], v15 offset:0
v_mov_b32_dpp v82, v48 row_shl:4 row_mask:0xf bank_mask:0x1
ds_read_b64_tr_b16 v[102:103], v15 offset:4096
v_mov_b32_dpp v83, v49 row_shl:4 row_mask:0xf bank_mask:0x1
ds_read_b64_tr_b16 v[104:105], v16 offset:0
v_mov_b32_dpp v84, v48 row_shr:4 row_mask:0xf bank_mask:0x2
ds_read_b64_tr_b16 v[106:107], v16 offset:4096
v_mov_b32_dpp v85, v49 row_shr:4 row_mask:0xf bank_mask:0x2
ds_read_b64_tr_b16 v[108:109], v17 offset:0
v_mov_b32_dpp v86, v48 quad_perm:[0,1,2,3] row_mask:0xf bank_mask:0x2
ds_read_b64_tr_b16 v[110:111], v17 offset:4096
v_mov_b32_dpp v87, v49 quad_perm:[0,1,2,3] row_mask:0xf bank_mask:0x2
ds_read_b64_tr_b16 v[112:113], v18 offset:0
v_mov_b32_dpp v88, v48 row_shr:8 row_mask:0xf bank_mask:0x4
ds_read_b64_tr_b16 v[114:115], v18 offset:4096
v_mov_b32_dpp v89, v49 row_shr:8 row_mask:0xf bank_mask:0x4
ds_read_b64_tr_b16 v[116:117], v19 offset:0
v_mov_b32_dpp v90, v48 row_shr:4 row_mask:0xf bank_mask:0x4
ds_read_b64_tr_b16 v[118:119], v19 offset:4096
v_mov_b32_dpp v91, v49 row_shr:4 row_mask:0xf bank_mask:0x4
ds_read_b64_tr_b16 v[120:121], v20 offset:0
v_mov_b32_dpp v92, v48 row_shr:12 row_mask:0xf bank_mask:0x8
ds_read_b64_tr_b16 v[122:123], v20 offset:4096
v_mov_b32_dpp v93, v49 row_shr:12 row_mask:0xf bank_mask:0x8
ds_read_b64_tr_b16 v[124:125], v21 offset:0
v_mov_b32_dpp v94, v48 row_shr:8 row_mask:0xf bank_mask:0x8
ds_read_b64_tr_b16 v[126:127], v21 offset:4096
v_mov_b32_dpp v95, v49 row_shr:8 row_mask:0xf bank_mask:0x8
ds_read_b64_tr_b16 v[128:129], v14 offset:8192
s_waitcnt lgkmcnt(14)
v_mfma_f32_16x16x32_bf16 v[64:67], v[80:83], v[96:99], 0
ds_read_b64_tr_b16 v[130:131], v15 offset:8192
v_add_u32 v29, s35, v11
ds_read_b64_tr_b16 v[132:133], v16 offset:8192
s_waitcnt lgkmcnt(14)
v_mfma_f32_16x16x32_bf16 v[64:67], v[84:87], v[100:103], v[64:67]
ds_read_b64_tr_b16 v[134:135], v17 offset:8192
ds_read_b64_tr_b16 v[136:137], v18 offset:8192
s_waitcnt lgkmcnt(14)
v_mfma_f32_16x16x32_bf16 v[64:67], v[88:91], v[104:107], v[64:67]
ds_read_b64_tr_b16 v[138:139], v19 offset:8192
ds_read_b64_tr_b16 v[140:141], v20 offset:8192
s_waitcnt lgkmcnt(13)
v_mfma_f32_16x16x32_bf16 v[68:71], v[80:83], v[112:115], 0
ds_read_b64_tr_b16 v[142:143], v21 offset:8192
v_mov_b32_dpp v80, v48 row_shl:8 row_mask:0xf bank_mask:0x1
s_waitcnt lgkmcnt(12)
v_mfma_f32_16x16x32_bf16 v[68:71], v[84:87], v[116:119], v[68:71]
v_mov_b32_dpp v81, v49 row_shl:8 row_mask:0xf bank_mask:0x1
v_mov_b32_dpp v84, v48 row_shl:4 row_mask:0xf bank_mask:0x2
s_waitcnt lgkmcnt(10)
v_mfma_f32_16x16x32_bf16 v[68:71], v[88:91], v[120:123], v[68:71]
v_mov_b32_dpp v85, v49 row_shl:4 row_mask:0xf bank_mask:0x2
v_mov_b32_dpp v88, v48 quad_perm:[0,1,2,3] row_mask:0xf bank_mask:0x4
v_mfma_f32_16x16x32_bf16 v[64:67], v[92:95], v[108:111], v[64:67]
v_mov_b32_dpp v89, v49 quad_perm:[0,1,2,3] row_mask:0xf bank_mask:0x4
s_waitcnt lgkmcnt(8)
v_mfma_f32_16x16x32_bf16 v[68:71], v[92:95], v[124:127], v[68:71]
v_mov_b32_dpp v92, v48 row_shr:4 row_mask:0xf bank_mask:0x8
v_mov_b32_dpp v93, v49 row_shr:4 row_mask:0xf bank_mask:0x8
s_waitcnt lgkmcnt(7)
v_mfma_f32_16x16x16_bf16 v[64:67], v[80:81], v[128:129], v[64:67]
s_waitcnt lgkmcnt(3)
v_mfma_f32_16x16x16_bf16 v[68:71], v[80:81], v[136:137], v[68:71]
v_mfma_f32_16x16x16_bf16 v[64:67], v[84:85], v[130:131], v[64:67]
s_waitcnt lgkmcnt(2)
v_mfma_f32_16x16x16_bf16 v[68:71], v[84:85], v[138:139], v[68:71]
v_mfma_f32_16x16x16_bf16 v[64:67], v[88:89], v[132:133], v[64:67]
v_cvt_pk_bf16_f32 v132, v72, v76
v_cvt_pk_bf16_f32 v133, v73, v77
s_waitcnt lgkmcnt(1)
v_mfma_f32_16x16x16_bf16 v[68:71], v[88:89], v[140:141], v[68:71]
v_mfma_f32_16x16x16_bf16 v[64:67], v[92:93], v[134:135], v[64:67]
v_cvt_pk_bf16_f32 v134, v74, v78
v_cvt_pk_bf16_f32 v135, v75, v79
s_waitcnt lgkmcnt(0)
v_mfma_f32_16x16x16_bf16 v[68:71], v[92:93], v[142:143], v[68:71]
s_nop 7
v_cvt_pk_bf16_f32 v128, v64, v68
v_cvt_pk_bf16_f32 v129, v65, v69
v_cvt_pk_bf16_f32 v130, v66, v70
v_cvt_pk_bf16_f32 v131, v67, v71
ds_write2st64_b32 v10, v128, v129 offset0:0 offset1:1
ds_write2st64_b32 v10, v132, v133 offset0:4 offset1:5
ds_write2st64_b32 v10, v130, v131 offset0:2 offset1:3
ds_write2st64_b32 v10, v134, v135 offset0:6 offset1:7
ds_write_b64 v29, v[22:23] offset:2048
s_waitcnt lgkmcnt(0)
s_barrier
v_add_u32 v130, 0x25800, v13
ds_read_b64 v[96:97], v130 offset:0
v_add_u32 v128, 0x25800, v12
ds_read_b64 v[98:99], v130 offset:2176
v_add_u32 v129, 0x25800, v24
ds_read_b64 v[100:101], v130 offset:4352
v_mov_b32 v137, s12
ds_read_b64 v[102:103], v130 offset:6528
v_mov_b32 v139, s12
ds_read_b32 v104, v128 offset:0
ds_read2_b32 v[108:109], v129 offset0:0 offset1:8
ds_read_b32 v105, v128 offset:2176
ds_read_b32 v110, v129 offset:2176
ds_read_b32 v111, v129 offset:2208
ds_read_b32 v106, v128 offset:4352
ds_read_b32 v112, v129 offset:4352
ds_read_b32 v113, v129 offset:4384
ds_read_b32 v107, v128 offset:6528
ds_read_b32 v114, v129 offset:6528
ds_read_b32 v115, v129 offset:6560
s_waitcnt lgkmcnt(12)
v_max3_f32 v26, v96, v98, v100
s_waitcnt lgkmcnt(11)
v_max_f32 v26, v26, v102
v_sub_f32 v96, v96, v26
v_sub_f32 v98, v98, v26
v_exp_f32 v96, v96
v_sub_f32 v100, v100, v26
v_exp_f32 v98, v98
s_waitcnt lgkmcnt(10)
v_lshlrev_b32 v116, 16, v104
v_and_b32 v117, 0xffff0000, v104
s_waitcnt lgkmcnt(9)
v_perm_b32 v118, v108, v108, v25
v_perm_b32 v119, v109, v109, v25
v_sub_f32 v102, v102, v26
v_exp_f32 v100, v100
v_pk_add_f32 v[116:117], v[116:117], v[118:119]
s_waitcnt lgkmcnt(8)
v_lshlrev_b32 v120, 16, v105
v_and_b32 v121, 0xffff0000, v105
s_waitcnt lgkmcnt(7)
v_perm_b32 v122, v110, v110, v25
s_waitcnt lgkmcnt(6)
v_perm_b32 v123, v111, v111, v25
v_exp_f32 v102, v102
v_pk_mul_f32 v[134:135], v[116:117], v[96:97] op_sel_hi:[1,0]
v_pk_add_f32 v[120:121], v[120:121], v[122:123]
s_waitcnt lgkmcnt(5)
v_lshlrev_b32 v116, 16, v106
v_and_b32 v117, 0xffff0000, v106
s_waitcnt lgkmcnt(4)
v_perm_b32 v118, v112, v112, v25
s_waitcnt lgkmcnt(3)
v_perm_b32 v119, v113, v113, v25
v_pk_fma_f32 v[134:135], v[120:121], v[98:99], v[134:135] op_sel_hi:[1,0,1]
v_pk_add_f32 v[116:117], v[116:117], v[118:119]
s_waitcnt lgkmcnt(2)
v_lshlrev_b32 v120, 16, v107
v_and_b32 v121, 0xffff0000, v107
s_waitcnt lgkmcnt(1)
v_perm_b32 v122, v114, v114, v25
s_waitcnt lgkmcnt(0)
v_perm_b32 v123, v115, v115, v25
v_pk_fma_f32 v[134:135], v[116:117], v[100:101], v[134:135] op_sel_hi:[1,0,1]
v_pk_add_f32 v[120:121], v[120:121], v[122:123]
v_pk_fma_f32 v[134:135], v[120:121], v[102:103], v[134:135] op_sel_hi:[1,0,1]
v_mul_f32 v27, v96, v97
v_cvt_pk_bf16_f32 v136, v134, v135
v_fma_f32 v27, v98, v99, v27
v_fma_f32 v27, v100, v101, v27
v_fma_f32 v27, v102, v103, v27
v_mov_b32_dpp v138, v136 quad_perm:[1,0,3,2] row_mask:0xf bank_mask:0xf bound_ctrl:1
v_cndmask_b32 v136, v136, v26, s[56:57]
v_cndmask_b32 v138, v138, v27, s[56:57]
s_mov_b32 exec_lo, 0x55555557
s_mov_b32 exec_hi, 0x55555555
buffer_store_dwordx4 v[136:139], v6, s[24:27], 0 offen sc1
s_mov_b64 exec, -1
s_nop 0
v_add_u32 v6, v6, v8
s_waitcnt vmcnt(1)
s_barrier
s_mov_b32 s39, 0
Lqchk_73_%=:
ds_read_b128 v[96:99], v60 offset:0
ds_read_b128 v[100:103], v60 offset:512
ds_read_b128 v[104:107], v60 offset:1024
ds_read_b128 v[108:111], v60 offset:1536
s_waitcnt lgkmcnt(0)
v_bitop3_b32 v26, v97, v99, s12 bitop3:0x7e
v_bitop3_b32 v27, v101, v103, s12 bitop3:0x7e
v_bitop3_b32 v28, v105, v107, s12 bitop3:0x7e
v_bitop3_b32 v29, v109, v111, s12 bitop3:0x7e
v_or3_b32 v26, v26, v27, v28
v_or_b32 v26, v26, v29
v_cmp_ne_u32 vcc, 0, v26
v_mov_b32 v120, v96
v_mov_b32 v121, v98
v_mov_b32 v122, v100
v_mov_b32 v123, v102
v_mov_b32 v124, v104
v_mov_b32 v125, v106
v_mov_b32 v126, v108
v_mov_b32 v127, v110
s_cbranch_vccnz Lqslow_74_%=
Lqcont_75_%=:
s_add_u32 s43, s43, 0x4000
v_mfma_f32_16x16x32_bf16 v[128:131], a[48:51], v[120:123], 0
buffer_load_dwordx4 a[48:51], v1, s[16:19], s30 offen nt
v_perm_b32 v104, v197, v193, s40
v_mfma_f32_16x16x32_bf16 v[132:135], a[56:59], v[120:123], 0
v_perm_b32 v105, v205, v201, s40
v_perm_b32 v106, v213, v209, s40
v_mfma_f32_16x16x32_bf16 v[136:139], a[64:67], v[120:123], 0
v_perm_b32 v107, v221, v217, s40
v_perm_b32 v108, v197, v193, s41
v_mfma_f32_16x16x32_bf16 v[140:143], a[72:75], v[120:123], 0
v_perm_b32 v109, v205, v201, s41
v_perm_b32 v110, v213, v209, s41
v_mfma_f32_16x16x32_bf16 v[128:131], a[52:55], v[124:127], v[128:131]
buffer_load_dwordx4 a[52:55], v1, s[16:19], s30 offen offset:128 nt
buffer_load_dwordx4 a[56:59], v1, s[16:19], s30 offen offset:2048 nt
v_mfma_f32_16x16x32_bf16 v[132:135], a[60:63], v[124:127], v[132:135]
buffer_load_dwordx4 a[60:63], v1, s[16:19], s30 offen offset:2176 nt
buffer_load_dwordx4 a[64:67], v1, s[16:19], s31 offen nt
v_mfma_f32_16x16x32_bf16 v[136:139], a[68:71], v[124:127], v[136:139]
buffer_load_dwordx4 a[68:71], v1, s[16:19], s31 offen offset:128 nt
buffer_load_dwordx4 a[72:75], v1, s[16:19], s31 offen offset:2048 nt
v_mfma_f32_16x16x32_bf16 v[140:143], a[76:79], v[124:127], v[140:143]
buffer_load_dwordx4 a[76:79], v1, s[16:19], s31 offen offset:2176 nt
v_perm_b32 v111, v221, v217, s41
v_mfma_f32_16x16x32_bf16 v[96:99], a[80:83], v[120:123], 0
v_permlane32_swap_b32 v128, v132
v_permlane32_swap_b32 v129, v133
v_permlane32_swap_b32 v130, v134
v_permlane32_swap_b32 v131, v135
v_mfma_f32_16x16x32_bf16 v[100:103], a[88:91], v[120:123], 0
v_add_f32_dpp v40, v132, v128 row_shl:4 row_mask:0xf bank_mask:0x1
v_add_f32_dpp v41, v133, v129 row_shl:4 row_mask:0xf bank_mask:0x1
v_add_f32_dpp v42, v134, v130 row_shl:4 row_mask:0xf bank_mask:0x1
v_add_f32_dpp v43, v135, v131 row_shl:4 row_mask:0xf bank_mask:0x1
v_permlane32_swap_b32 v136, v140
v_permlane32_swap_b32 v137, v141
v_permlane32_swap_b32 v138, v142
v_permlane32_swap_b32 v139, v143
v_mfma_f32_16x16x32_bf16 v[128:131], a[96:99], v[120:123], v[56:59]
v_add_f32_dpp v40, v136, v140 row_shr:4 row_mask:0xf bank_mask:0x2
v_add_f32_dpp v41, v137, v141 row_shr:4 row_mask:0xf bank_mask:0x2
v_mfma_f32_16x16x32_bf16 v[132:135], a[104:107], v[120:123], v[56:59]
v_add_f32_dpp v42, v138, v142 row_shr:4 row_mask:0xf bank_mask:0x2
v_add_f32_dpp v43, v139, v143 row_shr:4 row_mask:0xf bank_mask:0x2
v_mfma_f32_16x16x32_bf16 v[96:99], a[84:87], v[124:127], v[96:99]
v_perm_b32 v112, v198, v194, s40
v_perm_b32 v113, v206, v202, s40
v_mfma_f32_16x16x32_bf16 v[100:103], a[92:95], v[124:127], v[100:103]
v_perm_b32 v114, v214, v210, s40
v_perm_b32 v115, v222, v218, s40
v_mfma_f32_16x16x32_bf16 v[136:139], a[112:115], v[120:123], v[56:59]
v_perm_b32 v116, v198, v194, s41
v_perm_b32 v117, v206, v202, s41
v_mfma_f32_16x16x32_bf16 v[140:143], a[120:123], v[120:123], v[56:59]
v_perm_b32 v118, v214, v210, s41
v_perm_b32 v119, v222, v218, s41
v_mfma_f32_16x16x32_bf16 v[128:131], a[100:103], v[124:127], v[128:131]
v_permlane32_swap_b32 v96, v100
v_permlane32_swap_b32 v97, v101
v_mfma_f32_16x16x32_bf16 v[132:135], a[108:111], v[124:127], v[132:135]
v_permlane32_swap_b32 v98, v102
v_permlane32_swap_b32 v99, v103
v_mfma_f32_16x16x32_bf16 v[136:139], a[116:119], v[124:127], v[136:139]
v_add_f32_dpp v40, v100, v96 row_shl:4 row_mask:0xf bank_mask:0x4
v_add_f32_dpp v41, v101, v97 row_shl:4 row_mask:0xf bank_mask:0x4
v_mfma_f32_16x16x32_bf16 v[140:143], a[124:127], v[124:127], v[140:143]
v_add_f32_dpp v42, v102, v98 row_shl:4 row_mask:0xf bank_mask:0x4
v_add_f32_dpp v43, v103, v99 row_shl:4 row_mask:0xf bank_mask:0x4
v_perm_b32 v96, v196, v192, s40
v_permlane32_swap_b32 v128, v132
v_permlane32_swap_b32 v129, v133
v_permlane32_swap_b32 v130, v134
v_permlane32_swap_b32 v131, v135
v_add_f32_dpp v40, v128, v132 row_shr:4 row_mask:0xf bank_mask:0x8
v_add_f32_dpp v41, v129, v133 row_shr:4 row_mask:0xf bank_mask:0x8
v_add_f32_dpp v42, v130, v134 row_shr:4 row_mask:0xf bank_mask:0x8
v_add_f32_dpp v43, v131, v135 row_shr:4 row_mask:0xf bank_mask:0x8
v_permlane32_swap_b32 v136, v140
v_permlane32_swap_b32 v137, v141
v_permlane32_swap_b32 v138, v142
v_permlane32_swap_b32 v139, v143
v_add_f32_dpp v44, v140, v136 row_shl:4 row_mask:0xf bank_mask:0x1
v_add_f32_dpp v45, v141, v137 row_shl:4 row_mask:0xf bank_mask:0x1
v_add_f32_dpp v46, v142, v138 row_shl:4 row_mask:0xf bank_mask:0x1
v_add_f32_dpp v47, v143, v139 row_shl:4 row_mask:0xf bank_mask:0x1
v_max3_f32 v26, v40, v41, v42
v_max3_f32 v27, v43, v44, v45
v_max3_f32 v26, v26, v46, v47
v_max_f32 v26, v26, v27
v_perm_b32 v97, v204, v200, s40
v_perm_b32 v98, v212, v208, s40
v_max_f32_dpp v26, v26, v26 row_ror:4 row_mask:0xf bank_mask:0xf bound_ctrl:1
v_perm_b32 v99, v220, v216, s40
v_perm_b32 v100, v196, v192, s41
v_max_f32_dpp v26, v26, v26 row_ror:8 row_mask:0xf bank_mask:0xf bound_ctrl:1
v_mov_b32 v27, v26
v_perm_b32 v101, v204, v200, s41
v_perm_b32 v102, v212, v208, s41
v_permlane16_swap_b32 v26, v27
v_max_f32 v26, v26, v27
v_mov_b32 v27, v26
v_perm_b32 v103, v220, v216, s41
v_perm_b32 v120, v199, v195, s40
v_permlane32_swap_b32 v26, v27
v_max_f32 v26, v26, v27
v_mul_f32 v22, s34, v26
v_fma_f32 v128, v40, s34, -v22
v_fma_f32 v129, v41, s34, -v22
v_fma_f32 v130, v42, s34, -v22
v_fma_f32 v131, v43, s34, -v22
v_fma_f32 v132, v44, s34, -v22
v_fma_f32 v133, v45, s34, -v22
v_fma_f32 v134, v46, s34, -v22
v_fma_f32 v135, v47, s34, -v22
v_exp_f32 v128, v128
v_exp_f32 v129, v129
v_exp_f32 v130, v130
v_exp_f32 v131, v131
v_exp_f32 v132, v132
v_exp_f32 v133, v133
v_exp_f32 v134, v134
v_exp_f32 v135, v135
v_cvt_pk_bf16_f32 v48, v128, v129
v_cvt_pk_bf16_f32 v49, v130, v131
v_cvt_pk_bf16_f32 v50, v132, v133
v_cvt_pk_bf16_f32 v51, v134, v135
v_mov_b32_dpp v80, v48 row_shl:12 row_mask:0xf bank_mask:0x1
v_mov_b32_dpp v81, v49 row_shl:12 row_mask:0xf bank_mask:0x1
v_mov_b32_dpp v82, v50 quad_perm:[0,1,2,3] row_mask:0xf bank_mask:0x1
v_mov_b32_dpp v83, v51 quad_perm:[0,1,2,3] row_mask:0xf bank_mask:0x1
v_mov_b32_dpp v84, v48 row_shl:8 row_mask:0xf bank_mask:0x2
v_mov_b32_dpp v85, v49 row_shl:8 row_mask:0xf bank_mask:0x2
v_mfma_f32_16x16x32_bf16 v[72:75], v[80:83], v[96:99], 0
v_mov_b32_dpp v86, v50 row_shr:4 row_mask:0xf bank_mask:0x2
v_mov_b32_dpp v87, v51 row_shr:4 row_mask:0xf bank_mask:0x2
v_mov_b32_dpp v88, v48 row_shl:4 row_mask:0xf bank_mask:0x4
v_mov_b32_dpp v89, v49 row_shl:4 row_mask:0xf bank_mask:0x4
v_mfma_f32_16x16x32_bf16 v[72:75], v[84:87], v[100:103], v[72:75]
v_mov_b32_dpp v90, v50 row_shr:8 row_mask:0xf bank_mask:0x4
v_mov_b32_dpp v91, v51 row_shr:8 row_mask:0xf bank_mask:0x4
v_mov_b32_dpp v92, v48 quad_perm:[0,1,2,3] row_mask:0xf bank_mask:0x8
v_mov_b32_dpp v93, v49 quad_perm:[0,1,2,3] row_mask:0xf bank_mask:0x8
v_mfma_f32_16x16x32_bf16 v[72:75], v[88:91], v[104:107], v[72:75]
v_mov_b32_dpp v94, v50 row_shr:12 row_mask:0xf bank_mask:0x8
v_mov_b32_dpp v95, v51 row_shr:12 row_mask:0xf bank_mask:0x8
v_mfma_f32_16x16x32_bf16 v[76:79], v[80:83], v[112:115], 0
ds_read_b64_tr_b16 v[96:97], v14 offset:12288
v_perm_b32 v121, v207, v203, s40
v_mfma_f32_16x16x32_bf16 v[72:75], v[92:95], v[108:111], v[72:75]
ds_read_b64_tr_b16 v[98:99], v14 offset:16384
v_perm_b32 v122, v215, v211, s40
ds_read_b64_tr_b16 v[100:101], v15 offset:12288
v_mfma_f32_16x16x32_bf16 v[76:79], v[84:87], v[116:119], v[76:79]
ds_read_b64_tr_b16 v[102:103], v15 offset:16384
v_perm_b32 v123, v223, v219, s40
ds_read_b64_tr_b16 v[104:105], v16 offset:12288
v_perm_b32 v124, v199, v195, s41
ds_read_b64_tr_b16 v[106:107], v16 offset:16384
v_mfma_f32_16x16x32_bf16 v[76:79], v[88:91], v[120:123], v[76:79]
ds_read_b64_tr_b16 v[108:109], v17 offset:12288
v_perm_b32 v125, v207, v203, s41
ds_read_b64_tr_b16 v[110:111], v17 offset:16384
v_perm_b32 v126, v215, v211, s41
ds_read_b64_tr_b16 v[112:113], v18 offset:12288
v_perm_b32 v127, v223, v219, s41
ds_read_b64_tr_b16 v[114:115], v18 offset:16384
v_add_f32 v26, v128, v129
ds_read_b64_tr_b16 v[116:117], v19 offset:12288
v_mfma_f32_16x16x32_bf16 v[76:79], v[92:95], v[124:127], v[76:79]
ds_read_b64_tr_b16 v[118:119], v19 offset:16384
v_add_f32 v27, v130, v131
ds_read_b64_tr_b16 v[120:121], v20 offset:12288
v_add_f32 v28, v132, v133
ds_read_b64_tr_b16 v[122:123], v20 offset:16384
v_add_f32 v29, v134, v135
ds_read_b64_tr_b16 v[124:125], v21 offset:12288
v_mov_b32_dpp v80, v48 quad_perm:[0,1,2,3] row_mask:0xf bank_mask:0x1
ds_read_b64_tr_b16 v[126:127], v21 offset:16384
v_mov_b32_dpp v81, v49 quad_perm:[0,1,2,3] row_mask:0xf bank_mask:0x1
ds_read_b64_tr_b16 v[128:129], v14 offset:20480
v_mov_b32_dpp v82, v48 row_shl:4 row_mask:0xf bank_mask:0x1
ds_read_b64_tr_b16 v[130:131], v15 offset:20480
v_mov_b32_dpp v83, v49 row_shl:4 row_mask:0xf bank_mask:0x1
ds_read_b64_tr_b16 v[132:133], v16 offset:20480
v_mov_b32_dpp v84, v48 row_shr:4 row_mask:0xf bank_mask:0x2
ds_read_b64_tr_b16 v[134:135], v17 offset:20480
s_waitcnt lgkmcnt(14)
v_mfma_f32_16x16x32_bf16 v[64:67], v[80:83], v[96:99], 0
ds_read_b64_tr_b16 v[136:137], v18 offset:20480
v_mov_b32_dpp v85, v49 row_shr:4 row_mask:0xf bank_mask:0x2
ds_read_b64_tr_b16 v[138:139], v19 offset:20480
v_mov_b32_dpp v86, v48 quad_perm:[0,1,2,3] row_mask:0xf bank_mask:0x2
ds_read_b64_tr_b16 v[140:141], v20 offset:20480
v_mov_b32_dpp v87, v49 quad_perm:[0,1,2,3] row_mask:0xf bank_mask:0x2
ds_read_b64_tr_b16 v[142:143], v21 offset:20480
s_waitcnt lgkmcnt(14)
v_mfma_f32_16x16x32_bf16 v[68:71], v[80:83], v[112:115], 0
v_mov_b32_dpp v88, v48 row_shr:8 row_mask:0xf bank_mask:0x4
v_mov_b32_dpp v89, v49 row_shr:8 row_mask:0xf bank_mask:0x4
v_mfma_f32_16x16x32_bf16 v[64:67], v[84:87], v[100:103], v[64:67]
v_mov_b32_dpp v90, v48 row_shr:4 row_mask:0xf bank_mask:0x4
v_mov_b32_dpp v91, v49 row_shr:4 row_mask:0xf bank_mask:0x4
s_waitcnt lgkmcnt(12)
v_mfma_f32_16x16x32_bf16 v[68:71], v[84:87], v[116:119], v[68:71]
v_mov_b32_dpp v92, v48 row_shr:12 row_mask:0xf bank_mask:0x8
v_mov_b32_dpp v93, v49 row_shr:12 row_mask:0xf bank_mask:0x8
v_mfma_f32_16x16x32_bf16 v[64:67], v[88:91], v[104:107], v[64:67]
v_mov_b32_dpp v94, v48 row_shr:8 row_mask:0xf bank_mask:0x8
v_mov_b32_dpp v95, v49 row_shr:8 row_mask:0xf bank_mask:0x8
s_waitcnt lgkmcnt(10)
v_mfma_f32_16x16x32_bf16 v[68:71], v[88:91], v[120:123], v[68:71]
v_mov_b32_dpp v80, v48 row_shl:8 row_mask:0xf bank_mask:0x1
v_mov_b32_dpp v81, v49 row_shl:8 row_mask:0xf bank_mask:0x1
v_mfma_f32_16x16x32_bf16 v[64:67], v[92:95], v[108:111], v[64:67]
v_mov_b32_dpp v84, v48 row_shl:4 row_mask:0xf bank_mask:0x2
v_mov_b32_dpp v85, v49 row_shl:4 row_mask:0xf bank_mask:0x2
s_waitcnt lgkmcnt(8)
v_mfma_f32_16x16x32_bf16 v[68:71], v[92:95], v[124:127], v[68:71]
v_mov_b32_dpp v88, v48 quad_perm:[0,1,2,3] row_mask:0xf bank_mask:0x4
v_mov_b32_dpp v89, v49 quad_perm:[0,1,2,3] row_mask:0xf bank_mask:0x4
s_waitcnt lgkmcnt(7)
v_mfma_f32_16x16x16_bf16 v[64:67], v[80:81], v[128:129], v[64:67]
v_add_f32 v26, v26, v27
v_add_f32 v28, v28, v29
s_waitcnt lgkmcnt(3)
v_mfma_f32_16x16x16_bf16 v[68:71], v[80:81], v[136:137], v[68:71]
v_add_f32 v26, v26, v28
v_mov_b32_dpp v92, v48 row_shr:4 row_mask:0xf bank_mask:0x8
v_mfma_f32_16x16x16_bf16 v[64:67], v[84:85], v[130:131], v[64:67]
v_mov_b32_dpp v93, v49 row_shr:4 row_mask:0xf bank_mask:0x8
v_add_f32_dpp v26, v26, v26 row_ror:4 row_mask:0xf bank_mask:0xf bound_ctrl:1
s_waitcnt lgkmcnt(2)
v_mfma_f32_16x16x16_bf16 v[68:71], v[84:85], v[138:139], v[68:71]
v_add_f32_dpp v26, v26, v26 row_ror:8 row_mask:0xf bank_mask:0xf bound_ctrl:1
v_mov_b32 v27, v26
v_mfma_f32_16x16x16_bf16 v[64:67], v[88:89], v[132:133], v[64:67]
s_nop 0
v_permlane16_swap_b32 v26, v27
v_add_f32 v26, v26, v27
s_waitcnt lgkmcnt(1)
v_mfma_f32_16x16x16_bf16 v[68:71], v[88:89], v[140:141], v[68:71]
v_mov_b32 v27, v26
v_cvt_pk_bf16_f32 v132, v72, v76
v_mfma_f32_16x16x16_bf16 v[64:67], v[92:93], v[134:135], v[64:67]
v_permlane32_swap_b32 v26, v27
v_add_f32 v26, v26, v27
s_waitcnt lgkmcnt(0)
v_mfma_f32_16x16x16_bf16 v[68:71], v[92:93], v[142:143], v[68:71]
v_mov_b32 v23, v26
v_cvt_pk_bf16_f32 v133, v73, v77
v_cvt_pk_bf16_f32 v134, v74, v78
v_cvt_pk_bf16_f32 v135, v75, v79
s_nop 3
v_cvt_pk_bf16_f32 v128, v64, v68
v_cvt_pk_bf16_f32 v129, v65, v69
v_cvt_pk_bf16_f32 v130, v66, v70
v_cvt_pk_bf16_f32 v131, v67, v71
ds_write2st64_b32 v9, v128, v129 offset0:64 offset1:65
ds_write2st64_b32 v9, v132, v133 offset0:68 offset1:69
ds_write2st64_b32 v9, v130, v131 offset0:66 offset1:67
ds_write2st64_b32 v9, v134, v135 offset0:70 offset1:71
ds_write_b64 v11, v[22:23] offset:18432
buffer_load_dwordx4 a[80:83], v1, s[16:19], s32 offen nt
buffer_load_dwordx4 a[84:87], v1, s[16:19], s32 offen offset:128 nt
buffer_load_dwordx4 a[88:91], v1, s[16:19], s32 offen offset:2048 nt
buffer_load_dwordx4 a[92:95], v1, s[16:19], s32 offen offset:2176 nt
s_add_u32 s20, s20, 0x1000000
s_addc_u32 s21, s21, 0
s_add_u32 s38, s37, 0x3000
s_mov_b32 m0, s38
s_nop 0
buffer_load_dwordx4 v3, s[20:23], s28 offen  lds
buffer_load_dwordx4 v4, s[20:23], s28 offen offset:1024  lds
buffer_load_dwordx4 v3, s[20:23], s28 offen offset:2048  lds
buffer_load_dwordx4 v4, s[20:23], s28 offen offset:3072  lds
s_waitcnt lgkmcnt(0)
s_barrier
v_add_u32 v130, 0x4000, v13
v_add_u32 v133, 0x16000, v13
ds_read_b64 v[96:97], v130 offset:0
v_add_u32 v128, 0x4000, v12
ds_read_b64 v[98:99], v130 offset:36864
v_add_u32 v129, 0x4000, v24
ds_read_b64 v[100:101], v133 offset:0
v_add_u32 v131, 0x16000, v12
ds_read_b64 v[102:103], v133 offset:36864
v_add_u32 v132, 0x16000, v24
ds_read_b32 v104, v128 offset:0
v_mov_b32 v137, s12
ds_read2_b32 v[108:109], v129 offset0:0 offset1:8
v_mov_b32 v139, s12
ds_read_b32 v105, v128 offset:36864
ds_read_b32 v110, v129 offset:36864
ds_read_b32 v111, v129 offset:36896
ds_read_b32 v106, v131 offset:0
ds_read2_b32 v[112:113], v132 offset0:0 offset1:8
ds_read_b32 v107, v131 offset:36864
ds_read_b32 v114, v132 offset:36864
ds_read_b32 v115, v132 offset:36896
s_waitcnt lgkmcnt(11)
v_max3_f32 v26, v96, v98, v100
s_waitcnt lgkmcnt(10)
v_max_f32 v26, v26, v102
v_sub_f32 v96, v96, v26
v_sub_f32 v98, v98, v26
v_exp_f32 v96, v96
v_sub_f32 v100, v100, v26
v_exp_f32 v98, v98
s_waitcnt lgkmcnt(9)
v_lshlrev_b32 v116, 16, v104
v_and_b32 v117, 0xffff0000, v104
s_waitcnt lgkmcnt(8)
v_perm_b32 v118, v108, v108, v25
v_perm_b32 v119, v109, v109, v25
v_sub_f32 v102, v102, v26
v_exp_f32 v100, v100
v_pk_add_f32 v[116:117], v[116:117], v[118:119]
s_waitcnt lgkmcnt(7)
v_lshlrev_b32 v120, 16, v105
v_and_b32 v121, 0xffff0000, v105
s_waitcnt lgkmcnt(6)
v_perm_b32 v122, v110, v110, v25
s_waitcnt lgkmcnt(5)
v_perm_b32 v123, v111, v111, v25
v_exp_f32 v102, v102
v_pk_mul_f32 v[134:135], v[116:117], v[96:97] op_sel_hi:[1,0]
v_pk_add_f32 v[120:121], v[120:121], v[122:123]
s_waitcnt lgkmcnt(4)
v_lshlrev_b32 v116, 16, v106
v_and_b32 v117, 0xffff0000, v106
s_waitcnt lgkmcnt(3)
v_perm_b32 v118, v112, v112, v25
v_perm_b32 v119, v113, v113, v25
v_pk_fma_f32 v[134:135], v[120:121], v[98:99], v[134:135] op_sel_hi:[1,0,1]
v_pk_add_f32 v[116:117], v[116:117], v[118:119]
s_waitcnt lgkmcnt(2)
v_lshlrev_b32 v120, 16, v107
v_and_b32 v121, 0xffff0000, v107
s_waitcnt lgkmcnt(1)
v_perm_b32 v122, v114, v114, v25
s_waitcnt lgkmcnt(0)
v_perm_b32 v123, v115, v115, v25
v_pk_fma_f32 v[134:135], v[116:117], v[100:101], v[134:135] op_sel_hi:[1,0,1]
v_pk_add_f32 v[120:121], v[120:121], v[122:123]
v_pk_fma_f32 v[134:135], v[120:121], v[102:103], v[134:135] op_sel_hi:[1,0,1]
v_mul_f32 v27, v96, v97
v_cvt_pk_bf16_f32 v136, v134, v135
v_fma_f32 v27, v98, v99, v27
v_fma_f32 v27, v100, v101, v27
v_fma_f32 v27, v102, v103, v27
v_mov_b32_dpp v138, v136 quad_perm:[1,0,3,2] row_mask:0xf bank_mask:0xf bound_ctrl:1
v_cndmask_b32 v136, v136, v26, s[56:57]
v_cndmask_b32 v138, v138, v27, s[56:57]
s_mov_b32 exec_lo, 0x55555557
s_mov_b32 exec_hi, 0x55555555
buffer_store_dwordx4 v[136:139], v7, s[24:27], 0 offen sc1
s_mov_b64 exec, -1
s_nop 0
v_add_u32 v7, v7, v8
s_mov_b32 s39, 0
Lqchk_79_%=:
ds_read_b128 v[96:99], v60 offset:2048
ds_read_b128 v[100:103], v60 offset:2560
ds_read_b128 v[104:107], v60 offset:3072
ds_read_b128 v[108:111], v60 offset:3584
s_waitcnt lgkmcnt(0)
v_bitop3_b32 v26, v97, v99, s12 bitop3:0x7e
v_bitop3_b32 v27, v101, v103, s12 bitop3:0x7e
v_bitop3_b32 v28, v105, v107, s12 bitop3:0x7e
v_bitop3_b32 v29, v109, v111, s12 bitop3:0x7e
v_or3_b32 v26, v26, v27, v28
v_or_b32 v26, v26, v29
v_cmp_ne_u32 vcc, 0, v26
v_mov_b32 v32, v96
v_mov_b32 v33, v98
v_mov_b32 v34, v100
v_mov_b32 v35, v102
v_mov_b32 v36, v104
v_mov_b32 v37, v106
v_mov_b32 v38, v108
v_mov_b32 v39, v110
s_cbranch_vccnz Lqslow_80_%=
Lqcont_81_%=:
buffer_load_dwordx4 v[192:195], v2, s[20:23], s31 offen 
buffer_load_dwordx4 v[196:199], v2, s[20:23], s31 offen offset:256 
buffer_load_dwordx4 v[200:203], v2, s[20:23], s31 offen offset:512 
buffer_load_dwordx4 v[204:207], v2, s[20:23], s31 offen offset:768 
buffer_load_dwordx4 v[208:211], v2, s[20:23], s32 offen 
buffer_load_dwordx4 v[212:215], v2, s[20:23], s32 offen offset:256 
buffer_load_dwordx4 v[216:219], v2, s[20:23], s32 offen offset:512 
buffer_load_dwordx4 v[220:223], v2, s[20:23], s32 offen offset:768 
Lit7_%=:
v_mfma_f32_16x16x32_bf16 v[128:131], a[16:19], v[32:35], 0
v_mfma_f32_16x16x32_bf16 v[132:135], a[24:27], v[32:35], 0
v_mfma_f32_16x16x32_bf16 v[128:131], a[20:23], v[36:39], v[128:131]
v_mfma_f32_16x16x32_bf16 v[132:135], a[28:31], v[36:39], v[132:135]
s_nop 7
v_permlane32_swap_b32 v128, v132
v_permlane32_swap_b32 v129, v133
v_permlane32_swap_b32 v130, v134
v_permlane32_swap_b32 v131, v135
v_add_f32_dpp v40, v132, v128 row_shl:4 row_mask:0xf bank_mask:0x1
v_add_f32_dpp v41, v133, v129 row_shl:4 row_mask:0xf bank_mask:0x1
v_add_f32_dpp v42, v134, v130 row_shl:4 row_mask:0xf bank_mask:0x1
v_add_f32_dpp v43, v135, v131 row_shl:4 row_mask:0xf bank_mask:0x1
s_barrier
s_add_u32 s38, s37, 0x4000
s_mov_b32 m0, s38
s_nop 0
buffer_load_dwordx4 v3, s[20:23], s29 offen  lds
buffer_load_dwordx4 v4, s[20:23], s29 offen offset:1024  lds
buffer_load_dwordx4 v3, s[20:23], s29 offen offset:2048  lds
buffer_load_dwordx4 v4, s[20:23], s29 offen offset:3072  lds
s_add_u32 s68, s64, Lit8_%=-Lcode0_%=
s_addc_u32 s69, s65, 0
s_and_b32 s69, s69, 0xffff
s_mov_b32 s70, Lit9_%=-Lit8_%=
s_mov_b32 s71, 0x20000
buffer_load_dword v61, v62, s[68:71], 0 offen
v_mfma_f32_16x16x32_bf16 v[128:131], a[32:35], v[32:35], 0
v_mfma_f32_16x16x32_bf16 v[132:135], a[40:43], v[32:35], 0
v_mfma_f32_16x16x32_bf16 v[128:131], a[36:39], v[36:39], v[128:131]
v_mfma_f32_16x16x32_bf16 v[132:135], a[44:47], v[36:39], v[132:135]
s_nop 7
v_permlane32_swap_b32 v128, v132
v_permlane32_swap_b32 v129, v133
v_permlane32_swap_b32 v130, v134
v_permlane32_swap_b32 v131, v135
v_add_f32_dpp v40, v128, v132 row_shr:4 row_mask:0xf bank_mask:0x2
v_add_f32_dpp v41, v129, v133 row_shr:4 row_mask:0xf bank_mask:0x2
v_add_f32_dpp v42, v130, v134 row_shr:4 row_mask:0xf bank_mask:0x2
v_add_f32_dpp v43, v131, v135 row_shr:4 row_mask:0xf bank_mask:0x2
s_add_u32 s38, s37, 0x5000
s_mov_b32 m0, s38
s_nop 0
buffer_load_dwordx4 v3, s[20:23], s30 offen  lds
buffer_load_dwordx4 v4, s[20:23], s30 offen offset:1024  lds
buffer_load_dwordx4 v3, s[20:23], s30 offen offset:2048  lds
buffer_load_dwordx4 v4, s[20:23], s30 offen offset:3072  lds
s_waitcnt vmcnt(30)
v_mfma_f32_16x16x32_bf16 v[128:131], a[48:51], v[32:35], 0
v_mfma_f32_16x16x32_bf16 v[132:135], a[56:59], v[32:35], 0
v_mfma_f32_16x16x32_bf16 v[128:131], a[52:55], v[36:39], v[128:131]
v_mfma_f32_16x16x32_bf16 v[132:135], a[60:63], v[36:39], v[132:135]
s_nop 7
v_permlane32_swap_b32 v128, v132
v_permlane32_swap_b32 v129, v133
v_permlane32_swap_b32 v130, v134
v_permlane32_swap_b32 v131, v135
v_add_f32_dpp v40, v132, v128 row_shl:4 row_mask:0xf bank_mask:0x4
v_add_f32_dpp v41, v133, v129 row_shl:4 row_mask:0xf bank_mask:0x4
v_add_f32_dpp v42, v134, v130 row_shl:4 row_mask:0xf bank_mask:0x4
v_add_f32_dpp v43, v135, v131 row_shl:4 row_mask:0xf bank_mask:0x4
s_waitcnt vmcnt(26)
v_mfma_f32_16x16x32_bf16 v[128:131], a[64:67], v[32:35], v[56:59]
v_mfma_f32_16x16x32_bf16 v[132:135], a[72:75], v[32:35], v[56:59]
v_mfma_f32_16x16x32_bf16 v[128:131], a[68:71], v[36:39], v[128:131]
v_mfma_f32_16x16x32_bf16 v[132:135], a[76:79], v[36:39], v[132:135]
s_nop 7
v_permlane32_swap_b32 v128, v132
v_permlane32_swap_b32 v129, v133
v_permlane32_swap_b32 v130, v134
v_permlane32_swap_b32 v131, v135
v_add_f32_dpp v40, v128, v132 row_shr:4 row_mask:0xf bank_mask:0x8
v_add_f32_dpp v41, v129, v133 row_shr:4 row_mask:0xf bank_mask:0x8
v_add_f32_dpp v42, v130, v134 row_shr:4 row_mask:0xf bank_mask:0x8
v_add_f32_dpp v43, v131, v135 row_shr:4 row_mask:0xf bank_mask:0x8
s_sub_u32 s16, s16, 0x7000000
s_subb_u32 s17, s17, 0
buffer_load_dwordx4 a[96:99], v1, s[16:19], s28 offen nt
buffer_load_dwordx4 a[100:103], v1, s[16:19], s28 offen offset:128 nt
buffer_load_dwordx4 a[104:107], v1, s[16:19], s28 offen offset:2048 nt
buffer_load_dwordx4 a[108:111], v1, s[16:19], s28 offen offset:2176 nt
s_waitcnt vmcnt(26)
v_mfma_f32_16x16x32_bf16 v[128:131], a[80:83], v[32:35], v[56:59]
v_max3_f32 v26, v40, v41, v42
v_mfma_f32_16x16x32_bf16 v[132:135], a[88:91], v[32:35], v[56:59]
v_mfma_f32_16x16x32_bf16 v[128:131], a[84:87], v[36:39], v[128:131]
v_mfma_f32_16x16x32_bf16 v[132:135], a[92:95], v[36:39], v[132:135]
s_nop 7
v_permlane32_swap_b32 v128, v132
v_permlane32_swap_b32 v129, v133
v_permlane32_swap_b32 v130, v134
v_permlane32_swap_b32 v131, v135
v_add_f32_dpp v44, v132, v128 row_shl:4 row_mask:0xf bank_mask:0x1
v_add_f32_dpp v45, v133, v129 row_shl:4 row_mask:0xf bank_mask:0x1
v_add_f32_dpp v46, v134, v130 row_shl:4 row_mask:0xf bank_mask:0x1
v_add_f32_dpp v47, v135, v131 row_shl:4 row_mask:0xf bank_mask:0x1
v_max3_f32 v27, v43, v44, v45
v_max3_f32 v26, v26, v46, v47
v_max_f32 v26, v26, v27
s_nop 1
v_max_f32_dpp v26, v26, v26 row_ror:4 row_mask:0xf bank_mask:0xf bound_ctrl:1
s_nop 1
v_max_f32_dpp v26, v26, v26 row_ror:8 row_mask:0xf bank_mask:0xf bound_ctrl:1
v_mov_b32 v27, v26
s_nop 1
v_permlane16_swap_b32 v26, v27
v_max_f32 v26, v26, v27
v_mov_b32 v27, v26
s_nop 1
v_permlane32_swap_b32 v26, v27
v_max_f32 v26, v26, v27
v_mul_f32 v22, s34, v26
v_fma_f32 v128, v40, s34, -v22
v_fma_f32 v129, v41, s34, -v22
v_fma_f32 v130, v42, s34, -v22
v_fma_f32 v131, v43, s34, -v22
v_fma_f32 v132, v44, s34, -v22
v_fma_f32 v133, v45, s34, -v22
v_fma_f32 v134, v46, s34, -v22
v_fma_f32 v135, v47, s34, -v22
v_exp_f32 v128, v128
v_exp_f32 v129, v129
v_exp_f32 v130, v130
v_exp_f32 v131, v131
v_exp_f32 v132, v132
v_exp_f32 v133, v133
v_exp_f32 v134, v134
v_exp_f32 v135, v135
v_add_f32 v26, v128, v129
v_add_f32 v27, v130, v131
v_add_f32 v28, v132, v133
v_add_f32 v29, v134, v135
v_add_f32 v26, v26, v27
v_add_f32 v28, v28, v29
v_add_f32 v26, v26, v28
v_cvt_pk_bf16_f32 v48, v128, v129
v_cvt_pk_bf16_f32 v49, v130, v131
v_add_f32_dpp v26, v26, v26 row_ror:4 row_mask:0xf bank_mask:0xf bound_ctrl:1
v_cvt_pk_bf16_f32 v50, v132, v133
v_cvt_pk_bf16_f32 v51, v134, v135
v_add_f32_dpp v26, v26, v26 row_ror:8 row_mask:0xf bank_mask:0xf bound_ctrl:1
v_mov_b32 v27, v26
s_nop 1
v_permlane16_swap_b32 v26, v27
v_add_f32 v26, v26, v27
v_mov_b32 v27, v26
s_nop 1
v_permlane32_swap_b32 v26, v27
v_add_f32 v26, v26, v27
v_mov_b32 v23, v26
buffer_load_dwordx4 a[112:115], v1, s[16:19], s29 offen nt
buffer_load_dwordx4 a[116:119], v1, s[16:19], s29 offen offset:128 nt
buffer_load_dwordx4 a[120:123], v1, s[16:19], s29 offen offset:2048 nt
buffer_load_dwordx4 a[124:127], v1, s[16:19], s29 offen offset:2176 nt
s_waitcnt vmcnt(17)
v_mov_b32_dpp v80, v48 row_shl:12 row_mask:0xf bank_mask:0x1
v_mov_b32_dpp v81, v49 row_shl:12 row_mask:0xf bank_mask:0x1
v_mov_b32_dpp v82, v50 quad_perm:[0,1,2,3] row_mask:0xf bank_mask:0x1
v_mov_b32_dpp v83, v51 quad_perm:[0,1,2,3] row_mask:0xf bank_mask:0x1
v_perm_b32 v96, v196, v192, s40
v_perm_b32 v97, v204, v200, s40
v_perm_b32 v98, v212, v208, s40
v_perm_b32 v99, v220, v216, s40
v_perm_b32 v112, v198, v194, s40
v_perm_b32 v113, v206, v202, s40
v_perm_b32 v114, v214, v210, s40
v_perm_b32 v115, v222, v218, s40
v_mfma_f32_16x16x32_bf16 v[72:75], v[80:83], v[96:99], 0
v_mov_b32_dpp v84, v48 row_shl:8 row_mask:0xf bank_mask:0x2
v_mov_b32_dpp v85, v49 row_shl:8 row_mask:0xf bank_mask:0x2
v_mfma_f32_16x16x32_bf16 v[76:79], v[80:83], v[112:115], 0
v_mov_b32_dpp v86, v50 row_shr:4 row_mask:0xf bank_mask:0x2
v_mov_b32_dpp v87, v51 row_shr:4 row_mask:0xf bank_mask:0x2
v_perm_b32 v100, v196, v192, s41
v_perm_b32 v101, v204, v200, s41
v_perm_b32 v102, v212, v208, s41
v_perm_b32 v103, v220, v216, s41
v_perm_b32 v116, v198, v194, s41
v_perm_b32 v117, v206, v202, s41
v_perm_b32 v118, v214, v210, s41
v_perm_b32 v119, v222, v218, s41
v_mfma_f32_16x16x32_bf16 v[72:75], v[84:87], v[100:103], v[72:75]
v_mov_b32_dpp v88, v48 row_shl:4 row_mask:0xf bank_mask:0x4
v_mov_b32_dpp v89, v49 row_shl:4 row_mask:0xf bank_mask:0x4
v_mfma_f32_16x16x32_bf16 v[76:79], v[84:87], v[116:119], v[76:79]
v_mov_b32_dpp v90, v50 row_shr:8 row_mask:0xf bank_mask:0x4
v_mov_b32_dpp v91, v51 row_shr:8 row_mask:0xf bank_mask:0x4
v_perm_b32 v104, v197, v193, s40
v_perm_b32 v105, v205, v201, s40
v_perm_b32 v106, v213, v209, s40
v_perm_b32 v107, v221, v217, s40
v_perm_b32 v120, v199, v195, s40
v_perm_b32 v121, v207, v203, s40
v_perm_b32 v122, v215, v211, s40
v_perm_b32 v123, v223, v219, s40
v_mfma_f32_16x16x32_bf16 v[72:75], v[88:91], v[104:107], v[72:75]
v_mov_b32_dpp v92, v48 quad_perm:[0,1,2,3] row_mask:0xf bank_mask:0x8
v_mov_b32_dpp v93, v49 quad_perm:[0,1,2,3] row_mask:0xf bank_mask:0x8
v_mfma_f32_16x16x32_bf16 v[76:79], v[88:91], v[120:123], v[76:79]
v_mov_b32_dpp v94, v50 row_shr:12 row_mask:0xf bank_mask:0x8
v_mov_b32_dpp v95, v51 row_shr:12 row_mask:0xf bank_mask:0x8
v_perm_b32 v108, v197, v193, s41
v_perm_b32 v109, v205, v201, s41
v_perm_b32 v110, v213, v209, s41
v_perm_b32 v111, v221, v217, s41
v_perm_b32 v124, v199, v195, s41
v_perm_b32 v125, v207, v203, s41
v_perm_b32 v126, v215, v211, s41
v_perm_b32 v127, v223, v219, s41
v_mfma_f32_16x16x32_bf16 v[72:75], v[92:95], v[108:111], v[72:75]
s_nop 0
v_mfma_f32_16x16x32_bf16 v[76:79], v[92:95], v[124:127], v[76:79]
s_waitcnt vmcnt(8)
s_cmp_lg_u32 s13, 0
s_cbranch_scc1 Lqdo_85_%=
s_mov_b32 m0, 147456
s_nop 0
buffer_load_dwordx4 v52, s[24:27], s43 offen sc1 lds
buffer_load_dwordx4 v52, s[24:27], s43 offen offset:1024 sc1 lds
s_branch Lqdj_86_%=
Lqdo_85_%=:
buffer_load_dword v61, v0, s[24:27], 0 offen
buffer_load_dword v61, v0, s[24:27], 0 offen
Lqdj_86_%=:
ds_read_b64_tr_b16 v[96:97], v14 offset:12288
v_mov_b32_dpp v80, v48 quad_perm:[0,1,2,3] row_mask:0xf bank_mask:0x1
ds_read_b64_tr_b16 v[98:99], v14 offset:16384
v_mov_b32_dpp v81, v49 quad_perm:[0,1,2,3] row_mask:0xf bank_mask:0x1
ds_read_b64_tr_b16 v[100:101], v15 offset:12288
v_mov_b32_dpp v82, v48 row_shl:4 row_mask:0xf bank_mask:0x1
ds_read_b64_tr_b16 v[102:103], v15 offset:16384
v_mov_b32_dpp v83, v49 row_shl:4 row_mask:0xf bank_mask:0x1
ds_read_b64_tr_b16 v[104:105], v16 offset:12288
v_mov_b32_dpp v84, v48 row_shr:4 row_mask:0xf bank_mask:0x2
ds_read_b64_tr_b16 v[106:107], v16 offset:16384
v_mov_b32_dpp v85, v49 row_shr:4 row_mask:0xf bank_mask:0x2
ds_read_b64_tr_b16 v[108:109], v17 offset:12288
v_mov_b32_dpp v86, v48 quad_perm:[0,1,2,3] row_mask:0xf bank_mask:0x2
ds_read_b64_tr_b16 v[110:111], v17 offset:16384
v_mov_b32_dpp v87, v49 quad_perm:[0,1,2,3] row_mask:0xf bank_mask:0x2
ds_read_b64_tr_b16 v[112:113], v18 offset:12288
v_mov_b32_dpp v88, v48 row_shr:8 row_mask:0xf bank_mask:0x4
ds_read_b64_tr_b16 v[114:115], v18 offset:16384
v_mov_b32_dpp v89, v49 row_shr:8 row_mask:0xf bank_mask:0x4
ds_read_b64_tr_b16 v[116:117], v19 offset:12288
v_mov_b32_dpp v90, v48 row_shr:4 row_mask:0xf bank_mask:0x4
ds_read_b64_tr_b16 v[118:119], v19 offset:16384
v_mov_b32_dpp v91, v49 row_shr:4 row_mask:0xf bank_mask:0x4
ds_read_b64_tr_b16 v[120:121], v20 offset:12288
v_mov_b32_dpp v92, v48 row_shr:12 row_mask:0xf bank_mask:0x8
ds_read_b64_tr_b16 v[122:123], v20 offset:16384
v_mov_b32_dpp v93, v49 row_shr:12 row_mask:0xf bank_mask:0x8
ds_read_b64_tr_b16 v[124:125], v21 offset:12288
v_mov_b32_dpp v94, v48 row_shr:8 row_mask:0xf bank_mask:0x8
ds_read_b64_tr_b16 v[126:127], v21 offset:16384
v_mov_b32_dpp v95, v49 row_shr:8 row_mask:0xf bank_mask:0x8
ds_read_b64_tr_b16 v[128:129], v14 offset:20480
s_waitcnt lgkmcnt(14)
v_mfma_f32_16x16x32_bf16 v[64:67], v[80:83], v[96:99], 0
ds_read_b64_tr_b16 v[130:131], v15 offset:20480
v_add_u32 v29, s35, v11
ds_read_b64_tr_b16 v[132:133], v16 offset:20480
s_waitcnt lgkmcnt(14)
v_mfma_f32_16x16x32_bf16 v[64:67], v[84:87], v[100:103], v[64:67]
ds_read_b64_tr_b16 v[134:135], v17 offset:20480
ds_read_b64_tr_b16 v[136:137], v18 offset:20480
s_waitcnt lgkmcnt(14)
v_mfma_f32_16x16x32_bf16 v[64:67], v[88:91], v[104:107], v[64:67]
ds_read_b64_tr_b16 v[138:139], v19 offset:20480
ds_read_b64_tr_b16 v[140:141], v20 offset:20480
s_waitcnt lgkmcnt(13)
v_mfma_f32_16x16x32_bf16 v[68:71], v[80:83], v[112:115], 0
ds_read_b64_tr_b16 v[142:143], v21 offset:20480
v_mov_b32_dpp v80, v48 row_shl:8 row_mask:0xf bank_mask:0x1
s_waitcnt lgkmcnt(12)
v_mfma_f32_16x16x32_bf16 v[68:71], v[84:87], v[116:119], v[68:71]
v_mov_b32_dpp v81, v49 row_shl:8 row_mask:0xf bank_mask:0x1
v_mov_b32_dpp v84, v48 row_shl:4 row_mask:0xf bank_mask:0x2
s_waitcnt lgkmcnt(10)
v_mfma_f32_16x16x32_bf16 v[68:71], v[88:91], v[120:123], v[68:71]
v_mov_b32_dpp v85, v49 row_shl:4 row_mask:0xf bank_mask:0x2
v_mov_b32_dpp v88, v48 quad_perm:[0,1,2,3] row_mask:0xf bank_mask:0x4
v_mfma_f32_16x16x32_bf16 v[64:67], v[92:95], v[108:111], v[64:67]
v_mov_b32_dpp v89, v49 quad_perm:[0,1,2,3] row_mask:0xf bank_mask:0x4
s_waitcnt lgkmcnt(8)
v_mfma_f32_16x16x32_bf16 v[68:71], v[92:95], v[124:127], v[68:71]
v_mov_b32_dpp v92, v48 row_shr:4 row_mask:0xf bank_mask:0x8
v_mov_b32_dpp v93, v49 row_shr:4 row_mask:0xf bank_mask:0x8
s_waitcnt lgkmcnt(7)
v_mfma_f32_16x16x16_bf16 v[64:67], v[80:81], v[128:129], v[64:67]
s_waitcnt lgkmcnt(3)
v_mfma_f32_16x16x16_bf16 v[68:71], v[80:81], v[136:137], v[68:71]
v_mfma_f32_16x16x16_bf16 v[64:67], v[84:85], v[130:131], v[64:67]
s_waitcnt lgkmcnt(2)
v_mfma_f32_16x16x16_bf16 v[68:71], v[84:85], v[138:139], v[68:71]
v_mfma_f32_16x16x16_bf16 v[64:67], v[88:89], v[132:133], v[64:67]
v_cvt_pk_bf16_f32 v132, v72, v76
v_cvt_pk_bf16_f32 v133, v73, v77
s_waitcnt lgkmcnt(1)
v_mfma_f32_16x16x16_bf16 v[68:71], v[88:89], v[140:141], v[68:71]
v_mfma_f32_16x16x16_bf16 v[64:67], v[92:93], v[134:135], v[64:67]
v_cvt_pk_bf16_f32 v134, v74, v78
v_cvt_pk_bf16_f32 v135, v75, v79
s_waitcnt lgkmcnt(0)
v_mfma_f32_16x16x16_bf16 v[68:71], v[92:93], v[142:143], v[68:71]
s_nop 7
v_cvt_pk_bf16_f32 v128, v64, v68
v_cvt_pk_bf16_f32 v129, v65, v69
v_cvt_pk_bf16_f32 v130, v66, v70
v_cvt_pk_bf16_f32 v131, v67, v71
ds_write2st64_b32 v10, v128, v129 offset0:0 offset1:1
ds_write2st64_b32 v10, v132, v133 offset0:4 offset1:5
ds_write2st64_b32 v10, v130, v131 offset0:2 offset1:3
ds_write2st64_b32 v10, v134, v135 offset0:6 offset1:7
ds_write_b64 v29, v[22:23] offset:2048
s_waitcnt lgkmcnt(0)
s_barrier
v_add_u32 v130, 0x25800, v13
ds_read_b64 v[96:97], v130 offset:0
v_add_u32 v128, 0x25800, v12
ds_read_b64 v[98:99], v130 offset:2176
v_add_u32 v129, 0x25800, v24
ds_read_b64 v[100:101], v130 offset:4352
v_mov_b32 v137, s12
ds_read_b64 v[102:103], v130 offset:6528
v_mov_b32 v139, s12
ds_read_b32 v104, v128 offset:0
ds_read2_b32 v[108:109], v129 offset0:0 offset1:8
ds_read_b32 v105, v128 offset:2176
ds_read_b32 v110, v129 offset:2176
ds_read_b32 v111, v129 offset:2208
ds_read_b32 v106, v128 offset:4352
ds_read_b32 v112, v129 offset:4352
ds_read_b32 v113, v129 offset:4384
ds_read_b32 v107, v128 offset:6528
ds_read_b32 v114, v129 offset:6528
ds_read_b32 v115, v129 offset:6560
s_waitcnt lgkmcnt(12)
v_max3_f32 v26, v96, v98, v100
s_waitcnt lgkmcnt(11)
v_max_f32 v26, v26, v102
v_sub_f32 v96, v96, v26
v_sub_f32 v98, v98, v26
v_exp_f32 v96, v96
v_sub_f32 v100, v100, v26
v_exp_f32 v98, v98
s_waitcnt lgkmcnt(10)
v_lshlrev_b32 v116, 16, v104
v_and_b32 v117, 0xffff0000, v104
s_waitcnt lgkmcnt(9)
v_perm_b32 v118, v108, v108, v25
v_perm_b32 v119, v109, v109, v25
v_sub_f32 v102, v102, v26
v_exp_f32 v100, v100
v_pk_add_f32 v[116:117], v[116:117], v[118:119]
s_waitcnt lgkmcnt(8)
v_lshlrev_b32 v120, 16, v105
v_and_b32 v121, 0xffff0000, v105
s_waitcnt lgkmcnt(7)
v_perm_b32 v122, v110, v110, v25
s_waitcnt lgkmcnt(6)
v_perm_b32 v123, v111, v111, v25
v_exp_f32 v102, v102
v_pk_mul_f32 v[134:135], v[116:117], v[96:97] op_sel_hi:[1,0]
v_pk_add_f32 v[120:121], v[120:121], v[122:123]
s_waitcnt lgkmcnt(5)
v_lshlrev_b32 v116, 16, v106
v_and_b32 v117, 0xffff0000, v106
s_waitcnt lgkmcnt(4)
v_perm_b32 v118, v112, v112, v25
s_waitcnt lgkmcnt(3)
v_perm_b32 v119, v113, v113, v25
v_pk_fma_f32 v[134:135], v[120:121], v[98:99], v[134:135] op_sel_hi:[1,0,1]
v_pk_add_f32 v[116:117], v[116:117], v[118:119]
s_waitcnt lgkmcnt(2)
v_lshlrev_b32 v120, 16, v107
v_and_b32 v121, 0xffff0000, v107
s_waitcnt lgkmcnt(1)
v_perm_b32 v122, v114, v114, v25
s_waitcnt lgkmcnt(0)
v_perm_b32 v123, v115, v115, v25
v_pk_fma_f32 v[134:135], v[116:117], v[100:101], v[134:135] op_sel_hi:[1,0,1]
v_pk_add_f32 v[120:121], v[120:121], v[122:123]
v_pk_fma_f32 v[134:135], v[120:121], v[102:103], v[134:135] op_sel_hi:[1,0,1]
v_mul_f32 v27, v96, v97
v_cvt_pk_bf16_f32 v136, v134, v135
v_fma_f32 v27, v98, v99, v27
v_fma_f32 v27, v100, v101, v27
v_fma_f32 v27, v102, v103, v27
v_mov_b32_dpp v138, v136 quad_perm:[1,0,3,2] row_mask:0xf bank_mask:0xf bound_ctrl:1
v_cndmask_b32 v136, v136, v26, s[56:57]
v_cndmask_b32 v138, v138, v27, s[56:57]
s_mov_b32 exec_lo, 0x55555557
s_mov_b32 exec_hi, 0x55555555
buffer_store_dwordx4 v[136:139], v6, s[24:27], 0 offen sc1
s_mov_b64 exec, -1
s_nop 0
v_add_u32 v6, v6, v8
s_waitcnt vmcnt(1)
s_barrier
s_mov_b32 s39, 0
Lqchk_87_%=:
ds_read_b128 v[96:99], v60 offset:0
ds_read_b128 v[100:103], v60 offset:512
ds_read_b128 v[104:107], v60 offset:1024
ds_read_b128 v[108:111], v60 offset:1536
s_waitcnt lgkmcnt(0)
v_bitop3_b32 v26, v97, v99, s12 bitop3:0x7e
v_bitop3_b32 v27, v101, v103, s12 bitop3:0x7e
v_bitop3_b32 v28, v105, v107, s12 bitop3:0x7e
v_bitop3_b32 v29, v109, v111, s12 bitop3:0x7e
v_or3_b32 v26, v26, v27, v28
v_or_b32 v26, v26, v29
v_cmp_ne_u32 vcc, 0, v26
v_mov_b32 v120, v96
v_mov_b32 v121, v98
v_mov_b32 v122, v100
v_mov_b32 v123, v102
v_mov_b32 v124, v104
v_mov_b32 v125, v106
v_mov_b32 v126, v108
v_mov_b32 v127, v110
s_cbranch_vccnz Lqslow_88_%=
Lqcont_89_%=:
s_add_u32 s43, s43, 0x4000
v_mfma_f32_16x16x32_bf16 v[128:131], a[128:131], v[120:123], 0
buffer_load_dwordx4 a[128:131], v1, s[16:19], s30 offen nt
v_perm_b32 v104, v229, v225, s40
v_mfma_f32_16x16x32_bf16 v[132:135], a[136:139], v[120:123], 0
v_perm_b32 v105, v237, v233, s40
v_perm_b32 v106, v245, v241, s40
v_mfma_f32_16x16x32_bf16 v[136:139], a[144:147], v[120:123], 0
v_perm_b32 v107, v253, v249, s40
v_perm_b32 v108, v229, v225, s41
v_mfma_f32_16x16x32_bf16 v[140:143], a[152:155], v[120:123], 0
v_perm_b32 v109, v237, v233, s41
v_perm_b32 v110, v245, v241, s41
v_mfma_f32_16x16x32_bf16 v[128:131], a[132:135], v[124:127], v[128:131]
buffer_load_dwordx4 a[132:135], v1, s[16:19], s30 offen offset:128 nt
buffer_load_dwordx4 a[136:139], v1, s[16:19], s30 offen offset:2048 nt
v_mfma_f32_16x16x32_bf16 v[132:135], a[140:143], v[124:127], v[132:135]
buffer_load_dwordx4 a[140:143], v1, s[16:19], s30 offen offset:2176 nt
buffer_load_dwordx4 a[144:147], v1, s[16:19], s31 offen nt
v_mfma_f32_16x16x32_bf16 v[136:139], a[148:151], v[124:127], v[136:139]
buffer_load_dwordx4 a[148:151], v1, s[16:19], s31 offen offset:128 nt
buffer_load_dwordx4 a[152:155], v1, s[16:19], s31 offen offset:2048 nt
v_mfma_f32_16x16x32_bf16 v[140:143], a[156:159], v[124:127], v[140:143]
buffer_load_dwordx4 a[156:159], v1, s[16:19], s31 offen offset:2176 nt
v_perm_b32 v111, v253, v249, s41
v_mfma_f32_16x16x32_bf16 v[96:99], a[160:163], v[120:123], 0
v_permlane32_swap_b32 v128, v132
v_permlane32_swap_b32 v129, v133
v_permlane32_swap_b32 v130, v134
v_permlane32_swap_b32 v131, v135
v_mfma_f32_16x16x32_bf16 v[100:103], a[168:171], v[120:123], 0
v_add_f32_dpp v40, v132, v128 row_shl:4 row_mask:0xf bank_mask:0x1
v_add_f32_dpp v41, v133, v129 row_shl:4 row_mask:0xf bank_mask:0x1
v_add_f32_dpp v42, v134, v130 row_shl:4 row_mask:0xf bank_mask:0x1
v_add_f32_dpp v43, v135, v131 row_shl:4 row_mask:0xf bank_mask:0x1
v_permlane32_swap_b32 v136, v140
v_permlane32_swap_b32 v137, v141
v_permlane32_swap_b32 v138, v142
v_permlane32_swap_b32 v139, v143
v_mfma_f32_16x16x32_bf16 v[128:131], a[176:179], v[120:123], v[56:59]
v_add_f32_dpp v40, v136, v140 row_shr:4 row_mask:0xf bank_mask:0x2
v_add_f32_dpp v41, v137, v141 row_shr:4 row_mask:0xf bank_mask:0x2
v_mfma_f32_16x16x32_bf16 v[132:135], a[184:187], v[120:123], v[56:59]
v_add_f32_dpp v42, v138, v142 row_shr:4 row_mask:0xf bank_mask:0x2
v_add_f32_dpp v43, v139, v143 row_shr:4 row_mask:0xf bank_mask:0x2
v_mfma_f32_16x16x32_bf16 v[96:99], a[164:167], v[124:127], v[96:99]
v_perm_b32 v112, v230, v226, s40
v_perm_b32 v113, v238, v234, s40
v_mfma_f32_16x16x32_bf16 v[100:103], a[172:175], v[124:127], v[100:103]
v_perm_b32 v114, v246, v242, s40
v_perm_b32 v115, v254, v250, s40
v_mfma_f32_16x16x32_bf16 v[136:139], a[192:195], v[120:123], v[56:59]
v_perm_b32 v116, v230, v226, s41
v_perm_b32 v117, v238, v234, s41
v_mfma_f32_16x16x32_bf16 v[140:143], a[200:203], v[120:123], v[56:59]
v_perm_b32 v118, v246, v242, s41
v_perm_b32 v119, v254, v250, s41
v_mfma_f32_16x16x32_bf16 v[128:131], a[180:183], v[124:127], v[128:131]
v_permlane32_swap_b32 v96, v100
v_permlane32_swap_b32 v97, v101
v_mfma_f32_16x16x32_bf16 v[132:135], a[188:191], v[124:127], v[132:135]
v_permlane32_swap_b32 v98, v102
v_permlane32_swap_b32 v99, v103
v_mfma_f32_16x16x32_bf16 v[136:139], a[196:199], v[124:127], v[136:139]
v_add_f32_dpp v40, v100, v96 row_shl:4 row_mask:0xf bank_mask:0x4
v_add_f32_dpp v41, v101, v97 row_shl:4 row_mask:0xf bank_mask:0x4
v_mfma_f32_16x16x32_bf16 v[140:143], a[204:207], v[124:127], v[140:143]
v_add_f32_dpp v42, v102, v98 row_shl:4 row_mask:0xf bank_mask:0x4
v_add_f32_dpp v43, v103, v99 row_shl:4 row_mask:0xf bank_mask:0x4
v_perm_b32 v96, v228, v224, s40
v_permlane32_swap_b32 v128, v132
v_permlane32_swap_b32 v129, v133
v_permlane32_swap_b32 v130, v134
v_permlane32_swap_b32 v131, v135
v_add_f32_dpp v40, v128, v132 row_shr:4 row_mask:0xf bank_mask:0x8
v_add_f32_dpp v41, v129, v133 row_shr:4 row_mask:0xf bank_mask:0x8
v_add_f32_dpp v42, v130, v134 row_shr:4 row_mask:0xf bank_mask:0x8
v_add_f32_dpp v43, v131, v135 row_shr:4 row_mask:0xf bank_mask:0x8
v_permlane32_swap_b32 v136, v140
v_permlane32_swap_b32 v137, v141
v_permlane32_swap_b32 v138, v142
v_permlane32_swap_b32 v139, v143
v_add_f32_dpp v44, v140, v136 row_shl:4 row_mask:0xf bank_mask:0x1
v_add_f32_dpp v45, v141, v137 row_shl:4 row_mask:0xf bank_mask:0x1
v_add_f32_dpp v46, v142, v138 row_shl:4 row_mask:0xf bank_mask:0x1
v_add_f32_dpp v47, v143, v139 row_shl:4 row_mask:0xf bank_mask:0x1
v_max3_f32 v26, v40, v41, v42
v_max3_f32 v27, v43, v44, v45
v_max3_f32 v26, v26, v46, v47
v_max_f32 v26, v26, v27
v_perm_b32 v97, v236, v232, s40
v_perm_b32 v98, v244, v240, s40
v_max_f32_dpp v26, v26, v26 row_ror:4 row_mask:0xf bank_mask:0xf bound_ctrl:1
v_perm_b32 v99, v252, v248, s40
v_perm_b32 v100, v228, v224, s41
v_max_f32_dpp v26, v26, v26 row_ror:8 row_mask:0xf bank_mask:0xf bound_ctrl:1
v_mov_b32 v27, v26
v_perm_b32 v101, v236, v232, s41
v_perm_b32 v102, v244, v240, s41
v_permlane16_swap_b32 v26, v27
v_max_f32 v26, v26, v27
v_mov_b32 v27, v26
v_perm_b32 v103, v252, v248, s41
v_perm_b32 v120, v231, v227, s40
v_permlane32_swap_b32 v26, v27
v_max_f32 v26, v26, v27
v_mul_f32 v22, s34, v26
v_fma_f32 v128, v40, s34, -v22
v_fma_f32 v129, v41, s34, -v22
v_fma_f32 v130, v42, s34, -v22
v_fma_f32 v131, v43, s34, -v22
v_fma_f32 v132, v44, s34, -v22
v_fma_f32 v133, v45, s34, -v22
v_fma_f32 v134, v46, s34, -v22
v_fma_f32 v135, v47, s34, -v22
v_exp_f32 v128, v128
v_exp_f32 v129, v129
v_exp_f32 v130, v130
v_exp_f32 v131, v131
v_exp_f32 v132, v132
v_exp_f32 v133, v133
v_exp_f32 v134, v134
v_exp_f32 v135, v135
v_cvt_pk_bf16_f32 v48, v128, v129
v_cvt_pk_bf16_f32 v49, v130, v131
v_cvt_pk_bf16_f32 v50, v132, v133
v_cvt_pk_bf16_f32 v51, v134, v135
v_mov_b32_dpp v80, v48 row_shl:12 row_mask:0xf bank_mask:0x1
v_mov_b32_dpp v81, v49 row_shl:12 row_mask:0xf bank_mask:0x1
v_mov_b32_dpp v82, v50 quad_perm:[0,1,2,3] row_mask:0xf bank_mask:0x1
v_mov_b32_dpp v83, v51 quad_perm:[0,1,2,3] row_mask:0xf bank_mask:0x1
v_mov_b32_dpp v84, v48 row_shl:8 row_mask:0xf bank_mask:0x2
v_mov_b32_dpp v85, v49 row_shl:8 row_mask:0xf bank_mask:0x2
v_mfma_f32_16x16x32_bf16 v[72:75], v[80:83], v[96:99], 0
v_mov_b32_dpp v86, v50 row_shr:4 row_mask:0xf bank_mask:0x2
v_mov_b32_dpp v87, v51 row_shr:4 row_mask:0xf bank_mask:0x2
v_mov_b32_dpp v88, v48 row_shl:4 row_mask:0xf bank_mask:0x4
v_mov_b32_dpp v89, v49 row_shl:4 row_mask:0xf bank_mask:0x4
v_mfma_f32_16x16x32_bf16 v[72:75], v[84:87], v[100:103], v[72:75]
v_mov_b32_dpp v90, v50 row_shr:8 row_mask:0xf bank_mask:0x4
v_mov_b32_dpp v91, v51 row_shr:8 row_mask:0xf bank_mask:0x4
v_mov_b32_dpp v92, v48 quad_perm:[0,1,2,3] row_mask:0xf bank_mask:0x8
v_mov_b32_dpp v93, v49 quad_perm:[0,1,2,3] row_mask:0xf bank_mask:0x8
v_mfma_f32_16x16x32_bf16 v[72:75], v[88:91], v[104:107], v[72:75]
v_mov_b32_dpp v94, v50 row_shr:12 row_mask:0xf bank_mask:0x8
v_mov_b32_dpp v95, v51 row_shr:12 row_mask:0xf bank_mask:0x8
v_mfma_f32_16x16x32_bf16 v[76:79], v[80:83], v[112:115], 0
ds_read_b64_tr_b16 v[96:97], v14 offset:24576
v_perm_b32 v121, v239, v235, s40
v_mfma_f32_16x16x32_bf16 v[72:75], v[92:95], v[108:111], v[72:75]
ds_read_b64_tr_b16 v[98:99], v14 offset:28672
v_perm_b32 v122, v247, v243, s40
ds_read_b64_tr_b16 v[100:101], v15 offset:24576
v_mfma_f32_16x16x32_bf16 v[76:79], v[84:87], v[116:119], v[76:79]
ds_read_b64_tr_b16 v[102:103], v15 offset:28672
v_perm_b32 v123, v255, v251, s40
ds_read_b64_tr_b16 v[104:105], v16 offset:24576
v_perm_b32 v124, v231, v227, s41
ds_read_b64_tr_b16 v[106:107], v16 offset:28672
v_mfma_f32_16x16x32_bf16 v[76:79], v[88:91], v[120:123], v[76:79]
ds_read_b64_tr_b16 v[108:109], v17 offset:24576
v_perm_b32 v125, v239, v235, s41
ds_read_b64_tr_b16 v[110:111], v17 offset:28672
v_perm_b32 v126, v247, v243, s41
ds_read_b64_tr_b16 v[112:113], v18 offset:24576
v_perm_b32 v127, v255, v251, s41
ds_read_b64_tr_b16 v[114:115], v18 offset:28672
v_add_f32 v26, v128, v129
ds_read_b64_tr_b16 v[116:117], v19 offset:24576
v_mfma_f32_16x16x32_bf16 v[76:79], v[92:95], v[124:127], v[76:79]
ds_read_b64_tr_b16 v[118:119], v19 offset:28672
v_add_f32 v27, v130, v131
ds_read_b64_tr_b16 v[120:121], v20 offset:24576
v_add_f32 v28, v132, v133
ds_read_b64_tr_b16 v[122:123], v20 offset:28672
v_add_f32 v29, v134, v135
ds_read_b64_tr_b16 v[124:125], v21 offset:24576
v_mov_b32_dpp v80, v48 quad_perm:[0,1,2,3] row_mask:0xf bank_mask:0x1
ds_read_b64_tr_b16 v[126:127], v21 offset:28672
v_mov_b32_dpp v81, v49 quad_perm:[0,1,2,3] row_mask:0xf bank_mask:0x1
ds_read_b64_tr_b16 v[128:129], v14 offset:32768
v_mov_b32_dpp v82, v48 row_shl:4 row_mask:0xf bank_mask:0x1
ds_read_b64_tr_b16 v[130:131], v15 offset:32768
v_mov_b32_dpp v83, v49 row_shl:4 row_mask:0xf bank_mask:0x1
ds_read_b64_tr_b16 v[132:133], v16 offset:32768
v_mov_b32_dpp v84, v48 row_shr:4 row_mask:0xf bank_mask:0x2
ds_read_b64_tr_b16 v[134:135], v17 offset:32768
s_waitcnt lgkmcnt(14)
v_mfma_f32_16x16x32_bf16 v[64:67], v[80:83], v[96:99], 0
ds_read_b64_tr_b16 v[136:137], v18 offset:32768
v_mov_b32_dpp v85, v49 row_shr:4 row_mask:0xf bank_mask:0x2
ds_read_b64_tr_b16 v[138:139], v19 offset:32768
v_mov_b32_dpp v86, v48 quad_perm:[0,1,2,3] row_mask:0xf bank_mask:0x2
ds_read_b64_tr_b16 v[140:141], v20 offset:32768
v_mov_b32_dpp v87, v49 quad_perm:[0,1,2,3] row_mask:0xf bank_mask:0x2
ds_read_b64_tr_b16 v[142:143], v21 offset:32768
s_waitcnt lgkmcnt(14)
v_mfma_f32_16x16x32_bf16 v[68:71], v[80:83], v[112:115], 0
v_mov_b32_dpp v88, v48 row_shr:8 row_mask:0xf bank_mask:0x4
v_mov_b32_dpp v89, v49 row_shr:8 row_mask:0xf bank_mask:0x4
v_mfma_f32_16x16x32_bf16 v[64:67], v[84:87], v[100:103], v[64:67]
v_mov_b32_dpp v90, v48 row_shr:4 row_mask:0xf bank_mask:0x4
v_mov_b32_dpp v91, v49 row_shr:4 row_mask:0xf bank_mask:0x4
s_waitcnt lgkmcnt(12)
v_mfma_f32_16x16x32_bf16 v[68:71], v[84:87], v[116:119], v[68:71]
v_mov_b32_dpp v92, v48 row_shr:12 row_mask:0xf bank_mask:0x8
v_mov_b32_dpp v93, v49 row_shr:12 row_mask:0xf bank_mask:0x8
v_mfma_f32_16x16x32_bf16 v[64:67], v[88:91], v[104:107], v[64:67]
v_mov_b32_dpp v94, v48 row_shr:8 row_mask:0xf bank_mask:0x8
v_mov_b32_dpp v95, v49 row_shr:8 row_mask:0xf bank_mask:0x8
s_waitcnt lgkmcnt(10)
v_mfma_f32_16x16x32_bf16 v[68:71], v[88:91], v[120:123], v[68:71]
v_mov_b32_dpp v80, v48 row_shl:8 row_mask:0xf bank_mask:0x1
v_mov_b32_dpp v81, v49 row_shl:8 row_mask:0xf bank_mask:0x1
v_mfma_f32_16x16x32_bf16 v[64:67], v[92:95], v[108:111], v[64:67]
v_mov_b32_dpp v84, v48 row_shl:4 row_mask:0xf bank_mask:0x2
v_mov_b32_dpp v85, v49 row_shl:4 row_mask:0xf bank_mask:0x2
s_waitcnt lgkmcnt(8)
v_mfma_f32_16x16x32_bf16 v[68:71], v[92:95], v[124:127], v[68:71]
v_mov_b32_dpp v88, v48 quad_perm:[0,1,2,3] row_mask:0xf bank_mask:0x4
v_mov_b32_dpp v89, v49 quad_perm:[0,1,2,3] row_mask:0xf bank_mask:0x4
s_waitcnt lgkmcnt(7)
v_mfma_f32_16x16x16_bf16 v[64:67], v[80:81], v[128:129], v[64:67]
v_add_f32 v26, v26, v27
v_add_f32 v28, v28, v29
s_waitcnt lgkmcnt(3)
v_mfma_f32_16x16x16_bf16 v[68:71], v[80:81], v[136:137], v[68:71]
v_add_f32 v26, v26, v28
v_mov_b32_dpp v92, v48 row_shr:4 row_mask:0xf bank_mask:0x8
v_mfma_f32_16x16x16_bf16 v[64:67], v[84:85], v[130:131], v[64:67]
v_mov_b32_dpp v93, v49 row_shr:4 row_mask:0xf bank_mask:0x8
v_add_f32_dpp v26, v26, v26 row_ror:4 row_mask:0xf bank_mask:0xf bound_ctrl:1
s_waitcnt lgkmcnt(2)
v_mfma_f32_16x16x16_bf16 v[68:71], v[84:85], v[138:139], v[68:71]
v_add_f32_dpp v26, v26, v26 row_ror:8 row_mask:0xf bank_mask:0xf bound_ctrl:1
v_mov_b32 v27, v26
v_mfma_f32_16x16x16_bf16 v[64:67], v[88:89], v[132:133], v[64:67]
s_nop 0
v_permlane16_swap_b32 v26, v27
v_add_f32 v26, v26, v27
s_waitcnt lgkmcnt(1)
v_mfma_f32_16x16x16_bf16 v[68:71], v[88:89], v[140:141], v[68:71]
v_mov_b32 v27, v26
v_cvt_pk_bf16_f32 v132, v72, v76
v_mfma_f32_16x16x16_bf16 v[64:67], v[92:93], v[134:135], v[64:67]
v_permlane32_swap_b32 v26, v27
v_add_f32 v26, v26, v27
s_waitcnt lgkmcnt(0)
v_mfma_f32_16x16x16_bf16 v[68:71], v[92:93], v[142:143], v[68:71]
v_mov_b32 v23, v26
v_cvt_pk_bf16_f32 v133, v73, v77
v_cvt_pk_bf16_f32 v134, v74, v78
v_cvt_pk_bf16_f32 v135, v75, v79
s_nop 3
v_cvt_pk_bf16_f32 v128, v64, v68
v_cvt_pk_bf16_f32 v129, v65, v69
v_cvt_pk_bf16_f32 v130, v66, v70
v_cvt_pk_bf16_f32 v131, v67, v71
ds_write2st64_b32 v9, v128, v129 offset0:112 offset1:113
ds_write2st64_b32 v9, v132, v133 offset0:116 offset1:117
ds_write2st64_b32 v9, v130, v131 offset0:114 offset1:115
ds_write2st64_b32 v9, v134, v135 offset0:118 offset1:119
ds_write_b64 v11, v[22:23] offset:30720
buffer_load_dwordx4 a[160:163], v1, s[16:19], s32 offen nt
buffer_load_dwordx4 a[164:167], v1, s[16:19], s32 offen offset:128 nt
buffer_load_dwordx4 a[168:171], v1, s[16:19], s32 offen offset:2048 nt
buffer_load_dwordx4 a[172:175], v1, s[16:19], s32 offen offset:2176 nt
s_sub_u32 s20, s20, 0x7000000
s_subb_u32 s21, s21, 0
s_add_u32 s38, s37, 0x6000
s_mov_b32 m0, s38
s_nop 0
buffer_load_dwordx4 v3, s[20:23], s28 offen  lds
buffer_load_dwordx4 v4, s[20:23], s28 offen offset:1024  lds
buffer_load_dwordx4 v3, s[20:23], s28 offen offset:2048  lds
buffer_load_dwordx4 v4, s[20:23], s28 offen offset:3072  lds
s_waitcnt lgkmcnt(0)
s_barrier
v_add_u32 v130, 0x7000, v13
v_add_u32 v133, 0x19000, v13
ds_read_b64 v[96:97], v130 offset:0
v_add_u32 v128, 0x7000, v12
ds_read_b64 v[98:99], v130 offset:36864
v_add_u32 v129, 0x7000, v24
ds_read_b64 v[100:101], v133 offset:0
v_add_u32 v131, 0x19000, v12
ds_read_b64 v[102:103], v133 offset:36864
v_add_u32 v132, 0x19000, v24
ds_read_b32 v104, v128 offset:0
v_mov_b32 v137, s12
ds_read2_b32 v[108:109], v129 offset0:0 offset1:8
v_mov_b32 v139, s12
ds_read_b32 v105, v128 offset:36864
ds_read_b32 v110, v129 offset:36864
ds_read_b32 v111, v129 offset:36896
ds_read_b32 v106, v131 offset:0
ds_read2_b32 v[112:113], v132 offset0:0 offset1:8
ds_read_b32 v107, v131 offset:36864
ds_read_b32 v114, v132 offset:36864
ds_read_b32 v115, v132 offset:36896
s_waitcnt lgkmcnt(11)
v_max3_f32 v26, v96, v98, v100
s_waitcnt lgkmcnt(10)
v_max_f32 v26, v26, v102
v_sub_f32 v96, v96, v26
v_sub_f32 v98, v98, v26
v_exp_f32 v96, v96
v_sub_f32 v100, v100, v26
v_exp_f32 v98, v98
s_waitcnt lgkmcnt(9)
v_lshlrev_b32 v116, 16, v104
v_and_b32 v117, 0xffff0000, v104
s_waitcnt lgkmcnt(8)
v_perm_b32 v118, v108, v108, v25
v_perm_b32 v119, v109, v109, v25
v_sub_f32 v102, v102, v26
v_exp_f32 v100, v100
v_pk_add_f32 v[116:117], v[116:117], v[118:119]
s_waitcnt lgkmcnt(7)
v_lshlrev_b32 v120, 16, v105
v_and_b32 v121, 0xffff0000, v105
s_waitcnt lgkmcnt(6)
v_perm_b32 v122, v110, v110, v25
s_waitcnt lgkmcnt(5)
v_perm_b32 v123, v111, v111, v25
v_exp_f32 v102, v102
v_pk_mul_f32 v[134:135], v[116:117], v[96:97] op_sel_hi:[1,0]
v_pk_add_f32 v[120:121], v[120:121], v[122:123]
s_waitcnt lgkmcnt(4)
v_lshlrev_b32 v116, 16, v106
v_and_b32 v117, 0xffff0000, v106
s_waitcnt lgkmcnt(3)
v_perm_b32 v118, v112, v112, v25
v_perm_b32 v119, v113, v113, v25
v_pk_fma_f32 v[134:135], v[120:121], v[98:99], v[134:135] op_sel_hi:[1,0,1]
v_pk_add_f32 v[116:117], v[116:117], v[118:119]
s_waitcnt lgkmcnt(2)
v_lshlrev_b32 v120, 16, v107
v_and_b32 v121, 0xffff0000, v107
s_waitcnt lgkmcnt(1)
v_perm_b32 v122, v114, v114, v25
s_waitcnt lgkmcnt(0)
v_perm_b32 v123, v115, v115, v25
v_pk_fma_f32 v[134:135], v[116:117], v[100:101], v[134:135] op_sel_hi:[1,0,1]
v_pk_add_f32 v[120:121], v[120:121], v[122:123]
v_pk_fma_f32 v[134:135], v[120:121], v[102:103], v[134:135] op_sel_hi:[1,0,1]
v_mul_f32 v27, v96, v97
v_cvt_pk_bf16_f32 v136, v134, v135
v_fma_f32 v27, v98, v99, v27
v_fma_f32 v27, v100, v101, v27
v_fma_f32 v27, v102, v103, v27
v_mov_b32_dpp v138, v136 quad_perm:[1,0,3,2] row_mask:0xf bank_mask:0xf bound_ctrl:1
v_cndmask_b32 v136, v136, v26, s[56:57]
v_cndmask_b32 v138, v138, v27, s[56:57]
s_mov_b32 exec_lo, 0x55555557
s_mov_b32 exec_hi, 0x55555555
buffer_store_dwordx4 v[136:139], v7, s[24:27], 0 offen sc1
s_mov_b64 exec, -1
s_nop 0
v_add_u32 v7, v7, v8
buffer_load_dwordx4 v[224:227], v2, s[20:23], s31 offen 
buffer_load_dwordx4 v[228:231], v2, s[20:23], s31 offen offset:256 
buffer_load_dwordx4 v[232:235], v2, s[20:23], s31 offen offset:512 
buffer_load_dwordx4 v[236:239], v2, s[20:23], s31 offen offset:768 
buffer_load_dwordx4 v[240:243], v2, s[20:23], s32 offen 
buffer_load_dwordx4 v[244:247], v2, s[20:23], s32 offen offset:256 
buffer_load_dwordx4 v[248:251], v2, s[20:23], s32 offen offset:512 
buffer_load_dwordx4 v[252:255], v2, s[20:23], s32 offen offset:768 
Lit8_%=:
s_barrier
s_add_u32 s38, s37, 0x7000
s_mov_b32 m0, s38
s_nop 0
buffer_load_dwordx4 v3, s[20:23], s29 offen  lds
buffer_load_dwordx4 v4, s[20:23], s29 offen offset:1024  lds
buffer_load_dwordx4 v3, s[20:23], s29 offen offset:2048  lds
buffer_load_dwordx4 v4, s[20:23], s29 offen offset:3072  lds
s_add_u32 s68, s64, Lit9_%=-Lcode0_%=
s_addc_u32 s69, s65, 0
s_and_b32 s69, s69, 0xffff
s_mov_b32 s70, Lend_%=-Lit9_%=
s_mov_b32 s71, 0x20000
buffer_load_dword v61, v62, s[68:71], 0 offen
s_add_u32 s38, s37, 0x8000
s_mov_b32 m0, s38
s_nop 0
buffer_load_dwordx4 v3, s[20:23], s30 offen  lds
buffer_load_dwordx4 v4, s[20:23], s30 offen offset:1024  lds
buffer_load_dwordx4 v3, s[20:23], s30 offen offset:2048  lds
buffer_load_dwordx4 v4, s[20:23], s30 offen offset:3072  lds
s_cmp_lg_u32 s13, 0
s_cbranch_scc1 Lqdo_93_%=
s_mov_b32 m0, 147456
s_nop 0
buffer_load_dwordx4 v52, s[24:27], s43 offen sc1 lds
buffer_load_dwordx4 v52, s[24:27], s43 offen offset:1024 sc1 lds
s_branch Lqdj_94_%=
Lqdo_93_%=:
buffer_load_dword v61, v0, s[24:27], 0 offen
buffer_load_dword v61, v0, s[24:27], 0 offen
Lqdj_94_%=:
s_waitcnt vmcnt(0)
s_barrier
s_mov_b32 s39, 0
Lqchk_95_%=:
ds_read_b128 v[96:99], v60 offset:0
ds_read_b128 v[100:103], v60 offset:512
ds_read_b128 v[104:107], v60 offset:1024
ds_read_b128 v[108:111], v60 offset:1536
s_waitcnt lgkmcnt(0)
v_bitop3_b32 v26, v97, v99, s12 bitop3:0x7e
v_bitop3_b32 v27, v101, v103, s12 bitop3:0x7e
v_bitop3_b32 v28, v105, v107, s12 bitop3:0x7e
v_bitop3_b32 v29, v109, v111, s12 bitop3:0x7e
v_or3_b32 v26, v26, v27, v28
v_or_b32 v26, v26, v29
v_cmp_ne_u32 vcc, 0, v26
v_mov_b32 v120, v96
v_mov_b32 v121, v98
v_mov_b32 v122, v100
v_mov_b32 v123, v102
v_mov_b32 v124, v104
v_mov_b32 v125, v106
v_mov_b32 v126, v108
v_mov_b32 v127, v110
s_cbranch_vccnz Lqslow_96_%=
Lqcont_97_%=:
s_add_u32 s43, s43, 0x4000
v_mfma_f32_16x16x32_bf16 v[128:131], a[208:211], v[120:123], 0
v_perm_b32 v104, v165, v161, s40
v_perm_b32 v105, v173, v169, s40
v_mfma_f32_16x16x32_bf16 v[132:135], a[216:219], v[120:123], 0
v_perm_b32 v106, v181, v177, s40
v_perm_b32 v107, v189, v185, s40
v_mfma_f32_16x16x32_bf16 v[136:139], a[224:227], v[120:123], 0
v_perm_b32 v108, v165, v161, s41
v_perm_b32 v109, v173, v169, s41
v_mfma_f32_16x16x32_bf16 v[140:143], a[232:235], v[120:123], 0
v_perm_b32 v110, v181, v177, s41
v_perm_b32 v111, v189, v185, s41
v_mfma_f32_16x16x32_bf16 v[128:131], a[212:215], v[124:127], v[128:131]
v_perm_b32 v112, v166, v162, s40
v_perm_b32 v113, v174, v170, s40
v_mfma_f32_16x16x32_bf16 v[132:135], a[220:223], v[124:127], v[132:135]
v_perm_b32 v114, v182, v178, s40
v_perm_b32 v115, v190, v186, s40
v_mfma_f32_16x16x32_bf16 v[136:139], a[228:231], v[124:127], v[136:139]
v_perm_b32 v116, v166, v162, s41
v_perm_b32 v117, v174, v170, s41
v_mfma_f32_16x16x32_bf16 v[140:143], a[236:239], v[124:127], v[140:143]
v_perm_b32 v118, v182, v178, s41
v_perm_b32 v119, v190, v186, s41
v_mfma_f32_16x16x32_bf16 v[96:99], a[240:243], v[120:123], 0
v_permlane32_swap_b32 v128, v132
v_permlane32_swap_b32 v129, v133
v_permlane32_swap_b32 v130, v134
v_permlane32_swap_b32 v131, v135
v_mfma_f32_16x16x32_bf16 v[100:103], a[248:251], v[120:123], 0
v_add_f32_dpp v40, v132, v128 row_shl:4 row_mask:0xf bank_mask:0x1
v_add_f32_dpp v41, v133, v129 row_shl:4 row_mask:0xf bank_mask:0x1
v_add_f32_dpp v42, v134, v130 row_shl:4 row_mask:0xf bank_mask:0x1
v_add_f32_dpp v43, v135, v131 row_shl:4 row_mask:0xf bank_mask:0x1
v_permlane32_swap_b32 v136, v140
v_permlane32_swap_b32 v137, v141
v_permlane32_swap_b32 v138, v142
v_permlane32_swap_b32 v139, v143
v_mfma_f32_16x16x32_bf16 v[128:131], v[144:147], v[120:123], v[56:59]
v_add_f32_dpp v40, v136, v140 row_shr:4 row_mask:0xf bank_mask:0x2
v_add_f32_dpp v41, v137, v141 row_shr:4 row_mask:0xf bank_mask:0x2
v_mfma_f32_16x16x32_bf16 v[132:135], v[152:155], v[120:123], v[56:59]
v_add_f32_dpp v42, v138, v142 row_shr:4 row_mask:0xf bank_mask:0x2
v_add_f32_dpp v43, v139, v143 row_shr:4 row_mask:0xf bank_mask:0x2
v_mfma_f32_16x16x32_bf16 v[96:99], a[244:247], v[124:127], v[96:99]
v_mfma_f32_16x16x32_bf16 v[100:103], a[252:255], v[124:127], v[100:103]
v_mfma_f32_16x16x32_bf16 v[136:139], a[0:3], v[120:123], v[56:59]
v_mfma_f32_16x16x32_bf16 v[140:143], a[8:11], v[120:123], v[56:59]
v_perm_b32 v120, v167, v163, s40
v_perm_b32 v121, v175, v171, s40
v_mfma_f32_16x16x32_bf16 v[128:131], v[148:151], v[124:127], v[128:131]
s_nop 2
v_permlane32_swap_b32 v96, v100
v_permlane32_swap_b32 v97, v101
v_mfma_f32_16x16x32_bf16 v[132:135], v[156:159], v[124:127], v[132:135]
v_permlane32_swap_b32 v98, v102
v_permlane32_swap_b32 v99, v103
v_mfma_f32_16x16x32_bf16 v[136:139], a[4:7], v[124:127], v[136:139]
v_add_f32_dpp v40, v100, v96 row_shl:4 row_mask:0xf bank_mask:0x4
v_add_f32_dpp v41, v101, v97 row_shl:4 row_mask:0xf bank_mask:0x4
v_mfma_f32_16x16x32_bf16 v[140:143], a[12:15], v[124:127], v[140:143]
v_add_f32_dpp v42, v102, v98 row_shl:4 row_mask:0xf bank_mask:0x4
v_add_f32_dpp v43, v103, v99 row_shl:4 row_mask:0xf bank_mask:0x4
v_perm_b32 v96, v164, v160, s40
v_permlane32_swap_b32 v128, v132
v_permlane32_swap_b32 v129, v133
v_permlane32_swap_b32 v130, v134
v_permlane32_swap_b32 v131, v135
v_add_f32_dpp v40, v128, v132 row_shr:4 row_mask:0xf bank_mask:0x8
v_add_f32_dpp v41, v129, v133 row_shr:4 row_mask:0xf bank_mask:0x8
v_add_f32_dpp v42, v130, v134 row_shr:4 row_mask:0xf bank_mask:0x8
v_add_f32_dpp v43, v131, v135 row_shr:4 row_mask:0xf bank_mask:0x8
v_permlane32_swap_b32 v136, v140
v_permlane32_swap_b32 v137, v141
v_permlane32_swap_b32 v138, v142
v_permlane32_swap_b32 v139, v143
v_add_f32_dpp v44, v140, v136 row_shl:4 row_mask:0xf bank_mask:0x1
v_add_f32_dpp v45, v141, v137 row_shl:4 row_mask:0xf bank_mask:0x1
v_add_f32_dpp v46, v142, v138 row_shl:4 row_mask:0xf bank_mask:0x1
v_add_f32_dpp v47, v143, v139 row_shl:4 row_mask:0xf bank_mask:0x1
v_max3_f32 v26, v40, v41, v42
v_max3_f32 v27, v43, v44, v45
v_max3_f32 v26, v26, v46, v47
v_max_f32 v26, v26, v27
v_perm_b32 v97, v172, v168, s40
v_perm_b32 v98, v180, v176, s40
v_max_f32_dpp v26, v26, v26 row_ror:4 row_mask:0xf bank_mask:0xf bound_ctrl:1
v_perm_b32 v99, v188, v184, s40
v_perm_b32 v100, v164, v160, s41
v_max_f32_dpp v26, v26, v26 row_ror:8 row_mask:0xf bank_mask:0xf bound_ctrl:1
v_mov_b32 v27, v26
v_perm_b32 v101, v172, v168, s41
v_perm_b32 v102, v180, v176, s41
v_permlane16_swap_b32 v26, v27
v_max_f32 v26, v26, v27
v_mov_b32 v27, v26
v_perm_b32 v103, v188, v184, s41
v_perm_b32 v122, v183, v179, s40
v_permlane32_swap_b32 v26, v27
v_max_f32 v26, v26, v27
v_mul_f32 v22, s34, v26
v_fma_f32 v128, v40, s34, -v22
v_fma_f32 v129, v41, s34, -v22
v_fma_f32 v130, v42, s34, -v22
v_fma_f32 v131, v43, s34, -v22
v_fma_f32 v132, v44, s34, -v22
v_fma_f32 v133, v45, s34, -v22
v_fma_f32 v134, v46, s34, -v22
v_fma_f32 v135, v47, s34, -v22
v_exp_f32 v128, v128
v_exp_f32 v129, v129
v_exp_f32 v130, v130
v_exp_f32 v131, v131
v_exp_f32 v132, v132
v_exp_f32 v133, v133
v_exp_f32 v134, v134
v_exp_f32 v135, v135
v_cvt_pk_bf16_f32 v48, v128, v129
v_cvt_pk_bf16_f32 v49, v130, v131
v_cvt_pk_bf16_f32 v50, v132, v133
v_cvt_pk_bf16_f32 v51, v134, v135
v_mov_b32_dpp v80, v48 row_shl:12 row_mask:0xf bank_mask:0x1
v_mov_b32_dpp v81, v49 row_shl:12 row_mask:0xf bank_mask:0x1
v_mov_b32_dpp v82, v50 quad_perm:[0,1,2,3] row_mask:0xf bank_mask:0x1
v_mov_b32_dpp v83, v51 quad_perm:[0,1,2,3] row_mask:0xf bank_mask:0x1
v_mov_b32_dpp v84, v48 row_shl:8 row_mask:0xf bank_mask:0x2
v_mov_b32_dpp v85, v49 row_shl:8 row_mask:0xf bank_mask:0x2
v_mfma_f32_16x16x32_bf16 v[72:75], v[80:83], v[96:99], 0
v_mov_b32_dpp v86, v50 row_shr:4 row_mask:0xf bank_mask:0x2
v_mov_b32_dpp v87, v51 row_shr:4 row_mask:0xf bank_mask:0x2
v_mov_b32_dpp v88, v48 row_shl:4 row_mask:0xf bank_mask:0x4
v_mov_b32_dpp v89, v49 row_shl:4 row_mask:0xf bank_mask:0x4
v_mfma_f32_16x16x32_bf16 v[72:75], v[84:87], v[100:103], v[72:75]
v_mov_b32_dpp v90, v50 row_shr:8 row_mask:0xf bank_mask:0x4
v_mov_b32_dpp v91, v51 row_shr:8 row_mask:0xf bank_mask:0x4
v_mov_b32_dpp v92, v48 quad_perm:[0,1,2,3] row_mask:0xf bank_mask:0x8
v_mov_b32_dpp v93, v49 quad_perm:[0,1,2,3] row_mask:0xf bank_mask:0x8
v_mfma_f32_16x16x32_bf16 v[72:75], v[88:91], v[104:107], v[72:75]
v_mov_b32_dpp v94, v50 row_shr:12 row_mask:0xf bank_mask:0x8
v_mov_b32_dpp v95, v51 row_shr:12 row_mask:0xf bank_mask:0x8
v_mfma_f32_16x16x32_bf16 v[76:79], v[80:83], v[112:115], 0
ds_read_b64_tr_b16 v[96:97], v14 offset:0
v_perm_b32 v123, v191, v187, s40
v_mfma_f32_16x16x32_bf16 v[72:75], v[92:95], v[108:111], v[72:75]
ds_read_b64_tr_b16 v[98:99], v14 offset:4096
v_perm_b32 v124, v167, v163, s41
ds_read_b64_tr_b16 v[100:101], v15 offset:0
v_mfma_f32_16x16x32_bf16 v[76:79], v[84:87], v[116:119], v[76:79]
ds_read_b64_tr_b16 v[102:103], v15 offset:4096
v_perm_b32 v125, v175, v171, s41
ds_read_b64_tr_b16 v[104:105], v16 offset:0
v_mfma_f32_16x16x32_bf16 v[76:79], v[88:91], v[120:123], v[76:79]
ds_read_b64_tr_b16 v[106:107], v16 offset:4096
v_perm_b32 v126, v183, v179, s41
ds_read_b64_tr_b16 v[108:109], v17 offset:0
v_perm_b32 v127, v191, v187, s41
ds_read_b64_tr_b16 v[110:111], v17 offset:4096
v_add_f32 v26, v128, v129
ds_read_b64_tr_b16 v[112:113], v18 offset:0
v_mfma_f32_16x16x32_bf16 v[76:79], v[92:95], v[124:127], v[76:79]
ds_read_b64_tr_b16 v[114:115], v18 offset:4096
v_add_f32 v27, v130, v131
ds_read_b64_tr_b16 v[116:117], v19 offset:0
v_add_f32 v28, v132, v133
ds_read_b64_tr_b16 v[118:119], v19 offset:4096
v_add_f32 v29, v134, v135
ds_read_b64_tr_b16 v[120:121], v20 offset:0
v_mov_b32_dpp v80, v48 quad_perm:[0,1,2,3] row_mask:0xf bank_mask:0x1
ds_read_b64_tr_b16 v[122:123], v20 offset:4096
v_mov_b32_dpp v81, v49 quad_perm:[0,1,2,3] row_mask:0xf bank_mask:0x1
ds_read_b64_tr_b16 v[124:125], v21 offset:0
v_mov_b32_dpp v82, v48 row_shl:4 row_mask:0xf bank_mask:0x1
ds_read_b64_tr_b16 v[126:127], v21 offset:4096
v_mov_b32_dpp v83, v49 row_shl:4 row_mask:0xf bank_mask:0x1
ds_read_b64_tr_b16 v[128:129], v14 offset:8192
v_mov_b32_dpp v84, v48 row_shr:4 row_mask:0xf bank_mask:0x2
ds_read_b64_tr_b16 v[130:131], v15 offset:8192
s_waitcnt lgkmcnt(14)
v_mfma_f32_16x16x32_bf16 v[64:67], v[80:83], v[96:99], 0
ds_read_b64_tr_b16 v[132:133], v16 offset:8192
v_mov_b32_dpp v85, v49 row_shr:4 row_mask:0xf bank_mask:0x2
ds_read_b64_tr_b16 v[134:135], v17 offset:8192
v_mov_b32_dpp v86, v48 quad_perm:[0,1,2,3] row_mask:0xf bank_mask:0x2
ds_read_b64_tr_b16 v[136:137], v18 offset:8192
v_mov_b32_dpp v87, v49 quad_perm:[0,1,2,3] row_mask:0xf bank_mask:0x2
ds_read_b64_tr_b16 v[138:139], v19 offset:8192
v_mov_b32_dpp v88, v48 row_shr:8 row_mask:0xf bank_mask:0x4
ds_read_b64_tr_b16 v[140:141], v20 offset:8192
v_mfma_f32_16x16x32_bf16 v[64:67], v[84:87], v[100:103], v[64:67]
ds_read_b64_tr_b16 v[142:143], v21 offset:8192
v_mov_b32_dpp v89, v49 row_shr:8 row_mask:0xf bank_mask:0x4
s_waitcnt lgkmcnt(14)
v_mfma_f32_16x16x32_bf16 v[68:71], v[80:83], v[112:115], 0
v_mov_b32_dpp v90, v48 row_shr:4 row_mask:0xf bank_mask:0x4
v_mov_b32_dpp v91, v49 row_shr:4 row_mask:0xf bank_mask:0x4
s_waitcnt lgkmcnt(12)
v_mfma_f32_16x16x32_bf16 v[68:71], v[84:87], v[116:119], v[68:71]
v_mov_b32_dpp v92, v48 row_shr:12 row_mask:0xf bank_mask:0x8
v_mov_b32_dpp v93, v49 row_shr:12 row_mask:0xf bank_mask:0x8
v_mfma_f32_16x16x32_bf16 v[64:67], v[88:91], v[104:107], v[64:67]
v_mov_b32_dpp v94, v48 row_shr:8 row_mask:0xf bank_mask:0x8
v_mov_b32_dpp v95, v49 row_shr:8 row_mask:0xf bank_mask:0x8
s_waitcnt lgkmcnt(10)
v_mfma_f32_16x16x32_bf16 v[68:71], v[88:91], v[120:123], v[68:71]
v_mov_b32_dpp v80, v48 row_shl:8 row_mask:0xf bank_mask:0x1
v_mov_b32_dpp v81, v49 row_shl:8 row_mask:0xf bank_mask:0x1
v_mfma_f32_16x16x32_bf16 v[64:67], v[92:95], v[108:111], v[64:67]
v_mov_b32_dpp v84, v48 row_shl:4 row_mask:0xf bank_mask:0x2
v_mov_b32_dpp v85, v49 row_shl:4 row_mask:0xf bank_mask:0x2
s_waitcnt lgkmcnt(8)
v_mfma_f32_16x16x32_bf16 v[68:71], v[92:95], v[124:127], v[68:71]
v_mov_b32_dpp v88, v48 quad_perm:[0,1,2,3] row_mask:0xf bank_mask:0x4
v_mov_b32_dpp v89, v49 quad_perm:[0,1,2,3] row_mask:0xf bank_mask:0x4
s_waitcnt lgkmcnt(7)
v_mfma_f32_16x16x16_bf16 v[64:67], v[80:81], v[128:129], v[64:67]
v_add_f32 v26, v26, v27
v_add_f32 v28, v28, v29
s_waitcnt lgkmcnt(3)
v_mfma_f32_16x16x16_bf16 v[68:71], v[80:81], v[136:137], v[68:71]
v_add_f32 v26, v26, v28
v_mov_b32_dpp v92, v48 row_shr:4 row_mask:0xf bank_mask:0x8
v_mfma_f32_16x16x16_bf16 v[64:67], v[84:85], v[130:131], v[64:67]
v_mov_b32_dpp v93, v49 row_shr:4 row_mask:0xf bank_mask:0x8
v_add_f32_dpp v26, v26, v26 row_ror:4 row_mask:0xf bank_mask:0xf bound_ctrl:1
s_waitcnt lgkmcnt(2)
v_mfma_f32_16x16x16_bf16 v[68:71], v[84:85], v[138:139], v[68:71]
v_add_f32_dpp v26, v26, v26 row_ror:8 row_mask:0xf bank_mask:0xf bound_ctrl:1
v_mov_b32 v27, v26
v_mfma_f32_16x16x16_bf16 v[64:67], v[88:89], v[132:133], v[64:67]
s_nop 0
v_permlane16_swap_b32 v26, v27
v_add_f32 v26, v26, v27
s_waitcnt lgkmcnt(1)
v_mfma_f32_16x16x16_bf16 v[68:71], v[88:89], v[140:141], v[68:71]
v_mov_b32 v27, v26
v_cvt_pk_bf16_f32 v132, v72, v76
v_mfma_f32_16x16x16_bf16 v[64:67], v[92:93], v[134:135], v[64:67]
v_permlane32_swap_b32 v26, v27
v_add_f32 v26, v26, v27
s_waitcnt lgkmcnt(0)
v_mfma_f32_16x16x16_bf16 v[68:71], v[92:93], v[142:143], v[68:71]
v_mov_b32 v23, v26
v_cvt_pk_bf16_f32 v133, v73, v77
v_cvt_pk_bf16_f32 v134, v74, v78
v_cvt_pk_bf16_f32 v135, v75, v79
s_nop 3
v_cvt_pk_bf16_f32 v128, v64, v68
v_cvt_pk_bf16_f32 v129, v65, v69
v_cvt_pk_bf16_f32 v130, v66, v70
v_cvt_pk_bf16_f32 v131, v67, v71
ds_write2st64_b32 v9, v128, v129 offset0:16 offset1:17
ds_write2st64_b32 v9, v132, v133 offset0:20 offset1:21
ds_write2st64_b32 v9, v130, v131 offset0:18 offset1:19
ds_write2st64_b32 v9, v134, v135 offset0:22 offset1:23
ds_write_b64 v11, v[22:23] offset:6144
s_waitcnt lgkmcnt(0)
s_barrier
v_add_u32 v130, 0x1000, v13
v_add_u32 v133, 0x13000, v13
ds_read_b64 v[96:97], v130 offset:0
v_add_u32 v128, 0x1000, v12
ds_read_b64 v[98:99], v130 offset:36864
v_add_u32 v129, 0x1000, v24
ds_read_b64 v[100:101], v133 offset:0
v_add_u32 v131, 0x13000, v12
ds_read_b64 v[102:103], v133 offset:36864
v_add_u32 v132, 0x13000, v24
ds_read_b32 v104, v128 offset:0
v_mov_b32 v137, s12
ds_read2_b32 v[108:109], v129 offset0:0 offset1:8
v_mov_b32 v139, s12
ds_read_b32 v105, v128 offset:36864
ds_read_b32 v110, v129 offset:36864
ds_read_b32 v111, v129 offset:36896
ds_read_b32 v106, v131 offset:0
ds_read2_b32 v[112:113], v132 offset0:0 offset1:8
ds_read_b32 v107, v131 offset:36864
ds_read_b32 v114, v132 offset:36864
ds_read_b32 v115, v132 offset:36896
s_waitcnt lgkmcnt(11)
v_max3_f32 v26, v96, v98, v100
s_waitcnt lgkmcnt(10)
v_max_f32 v26, v26, v102
v_sub_f32 v96, v96, v26
v_sub_f32 v98, v98, v26
v_exp_f32 v96, v96
v_sub_f32 v100, v100, v26
v_exp_f32 v98, v98
s_waitcnt lgkmcnt(9)
v_lshlrev_b32 v116, 16, v104
v_and_b32 v117, 0xffff0000, v104
s_waitcnt lgkmcnt(8)
v_perm_b32 v118, v108, v108, v25
v_perm_b32 v119, v109, v109, v25
v_sub_f32 v102, v102, v26
v_exp_f32 v100, v100
v_pk_add_f32 v[116:117], v[116:117], v[118:119]
s_waitcnt lgkmcnt(7)
v_lshlrev_b32 v120, 16, v105
v_and_b32 v121, 0xffff0000, v105
s_waitcnt lgkmcnt(6)
v_perm_b32 v122, v110, v110, v25
s_waitcnt lgkmcnt(5)
v_perm_b32 v123, v111, v111, v25
v_exp_f32 v102, v102
v_pk_mul_f32 v[134:135], v[116:117], v[96:97] op_sel_hi:[1,0]
v_pk_add_f32 v[120:121], v[120:121], v[122:123]
s_waitcnt lgkmcnt(4)
v_lshlrev_b32 v116, 16, v106
v_and_b32 v117, 0xffff0000, v106
s_waitcnt lgkmcnt(3)
v_perm_b32 v118, v112, v112, v25
v_perm_b32 v119, v113, v113, v25
v_pk_fma_f32 v[134:135], v[120:121], v[98:99], v[134:135] op_sel_hi:[1,0,1]
v_pk_add_f32 v[116:117], v[116:117], v[118:119]
s_waitcnt lgkmcnt(2)
v_lshlrev_b32 v120, 16, v107
v_and_b32 v121, 0xffff0000, v107
s_waitcnt lgkmcnt(1)
v_perm_b32 v122, v114, v114, v25
s_waitcnt lgkmcnt(0)
v_perm_b32 v123, v115, v115, v25
v_pk_fma_f32 v[134:135], v[116:117], v[100:101], v[134:135] op_sel_hi:[1,0,1]
v_pk_add_f32 v[120:121], v[120:121], v[122:123]
v_pk_fma_f32 v[134:135], v[120:121], v[102:103], v[134:135] op_sel_hi:[1,0,1]
v_mul_f32 v27, v96, v97
v_cvt_pk_bf16_f32 v136, v134, v135
v_fma_f32 v27, v98, v99, v27
v_fma_f32 v27, v100, v101, v27
v_fma_f32 v27, v102, v103, v27
v_mov_b32_dpp v138, v136 quad_perm:[1,0,3,2] row_mask:0xf bank_mask:0xf bound_ctrl:1
v_cndmask_b32 v136, v136, v26, s[56:57]
v_cndmask_b32 v138, v138, v27, s[56:57]
s_mov_b32 exec_lo, 0x55555557
s_mov_b32 exec_hi, 0x55555555
buffer_store_dwordx4 v[136:139], v7, s[24:27], 0 offen sc1
s_mov_b64 exec, -1
s_nop 0
v_add_u32 v7, v7, v8
Lit9_%=:
s_sub_u32 s49, s43, 0x1c000
s_cmp_lg_u32 s13, 0
s_cbranch_scc1 Lqdo_101_%=
s_mov_b32 m0, 147456
s_nop 0
buffer_load_dwordx4 v52, s[24:27], s49 offen sc1 lds
buffer_load_dwordx4 v52, s[24:27], s49 offen offset:1024 sc1 lds
s_branch Lqdj_102_%=
Lqdo_101_%=:
buffer_load_dword v61, v0, s[24:27], 0 offen
buffer_load_dword v61, v0, s[24:27], 0 offen
Lqdj_102_%=:
s_waitcnt vmcnt(0)
s_barrier
s_mov_b32 s39, 0
Lqchk_103_%=:
ds_read_b128 v[96:99], v60 offset:0
ds_read_b128 v[100:103], v60 offset:512
ds_read_b128 v[104:107], v60 offset:1024
ds_read_b128 v[108:111], v60 offset:1536
s_waitcnt lgkmcnt(0)
v_bitop3_b32 v26, v97, v99, s12 bitop3:0x7e
v_bitop3_b32 v27, v101, v103, s12 bitop3:0x7e
v_bitop3_b32 v28, v105, v107, s12 bitop3:0x7e
v_bitop3_b32 v29, v109, v111, s12 bitop3:0x7e
v_or3_b32 v26, v26, v27, v28
v_or_b32 v26, v26, v29
v_cmp_ne_u32 vcc, 0, v26
v_mov_b32 v120, v96
v_mov_b32 v121, v98
v_mov_b32 v122, v100
v_mov_b32 v123, v102
v_mov_b32 v124, v104
v_mov_b32 v125, v106
v_mov_b32 v126, v108
v_mov_b32 v127, v110
s_cbranch_vccnz Lqslow_104_%=
Lqcont_105_%=:
v_sub_u32 v7, v7, v8
v_sub_u32 v7, v7, v8
v_sub_u32 v7, v7, v8
v_sub_u32 v7, v7, v8
v_sub_u32 v7, v7, v8
v_sub_u32 v7, v7, v8
v_sub_u32 v7, v7, v8
v_mfma_f32_16x16x32_bf16 v[128:131], a[96:99], v[120:123], 0
v_mfma_f32_16x16x32_bf16 v[132:135], a[104:107], v[120:123], 0
v_mfma_f32_16x16x32_bf16 v[136:139], a[112:115], v[120:123], 0
v_mfma_f32_16x16x32_bf16 v[140:143], a[120:123], v[120:123], 0
v_mfma_f32_16x16x32_bf16 v[128:131], a[100:103], v[124:127], v[128:131]
v_mfma_f32_16x16x32_bf16 v[132:135], a[108:111], v[124:127], v[132:135]
v_mfma_f32_16x16x32_bf16 v[136:139], a[116:119], v[124:127], v[136:139]
v_mfma_f32_16x16x32_bf16 v[140:143], a[124:127], v[124:127], v[140:143]
v_mfma_f32_16x16x32_bf16 v[96:99], a[128:131], v[120:123], 0
s_nop 4
v_permlane32_swap_b32 v128, v132
v_permlane32_swap_b32 v129, v133
v_permlane32_swap_b32 v130, v134
v_permlane32_swap_b32 v131, v135
v_mfma_f32_16x16x32_bf16 v[100:103], a[136:139], v[120:123], 0
v_add_f32_dpp v40, v132, v128 row_shl:4 row_mask:0xf bank_mask:0x1
v_add_f32_dpp v41, v133, v129 row_shl:4 row_mask:0xf bank_mask:0x1
v_add_f32_dpp v42, v134, v130 row_shl:4 row_mask:0xf bank_mask:0x1
v_add_f32_dpp v43, v135, v131 row_shl:4 row_mask:0xf bank_mask:0x1
v_permlane32_swap_b32 v136, v140
v_permlane32_swap_b32 v137, v141
v_permlane32_swap_b32 v138, v142
v_permlane32_swap_b32 v139, v143
v_mfma_f32_16x16x32_bf16 v[128:131], a[144:147], v[120:123], v[56:59]
v_add_f32_dpp v40, v136, v140 row_shr:4 row_mask:0xf bank_mask:0x2
v_add_f32_dpp v41, v137, v141 row_shr:4 row_mask:0xf bank_mask:0x2
v_mfma_f32_16x16x32_bf16 v[132:135], a[152:155], v[120:123], v[56:59]
v_add_f32_dpp v42, v138, v142 row_shr:4 row_mask:0xf bank_mask:0x2
v_add_f32_dpp v43, v139, v143 row_shr:4 row_mask:0xf bank_mask:0x2
v_mfma_f32_16x16x32_bf16 v[96:99], a[132:135], v[124:127], v[96:99]
v_mfma_f32_16x16x32_bf16 v[100:103], a[140:143], v[124:127], v[100:103]
v_mfma_f32_16x16x32_bf16 v[136:139], a[160:163], v[120:123], v[56:59]
v_mfma_f32_16x16x32_bf16 v[140:143], a[168:171], v[120:123], v[56:59]
v_mfma_f32_16x16x32_bf16 v[128:131], a[148:151], v[124:127], v[128:131]
s_nop 4
v_permlane32_swap_b32 v96, v100
v_permlane32_swap_b32 v97, v101
v_mfma_f32_16x16x32_bf16 v[132:135], a[156:159], v[124:127], v[132:135]
v_permlane32_swap_b32 v98, v102
v_permlane32_swap_b32 v99, v103
v_mfma_f32_16x16x32_bf16 v[136:139], a[164:167], v[124:127], v[136:139]
v_add_f32_dpp v40, v100, v96 row_shl:4 row_mask:0xf bank_mask:0x4
v_add_f32_dpp v41, v101, v97 row_shl:4 row_mask:0xf bank_mask:0x4
v_mfma_f32_16x16x32_bf16 v[140:143], a[172:175], v[124:127], v[140:143]
v_add_f32_dpp v42, v102, v98 row_shl:4 row_mask:0xf bank_mask:0x4
v_add_f32_dpp v43, v103, v99 row_shl:4 row_mask:0xf bank_mask:0x4
ds_read_b64_tr_b16 v[96:97], v14 offset:24576
v_permlane32_swap_b32 v128, v132
v_permlane32_swap_b32 v129, v133
v_permlane32_swap_b32 v130, v134
v_permlane32_swap_b32 v131, v135
v_add_f32_dpp v40, v128, v132 row_shr:4 row_mask:0xf bank_mask:0x8
v_add_f32_dpp v41, v129, v133 row_shr:4 row_mask:0xf bank_mask:0x8
v_add_f32_dpp v42, v130, v134 row_shr:4 row_mask:0xf bank_mask:0x8
v_add_f32_dpp v43, v131, v135 row_shr:4 row_mask:0xf bank_mask:0x8
v_permlane32_swap_b32 v136, v140
v_permlane32_swap_b32 v137, v141
v_permlane32_swap_b32 v138, v142
v_permlane32_swap_b32 v139, v143
v_add_f32_dpp v44, v140, v136 row_shl:4 row_mask:0xf bank_mask:0x1
v_add_f32_dpp v45, v141, v137 row_shl:4 row_mask:0xf bank_mask:0x1
v_add_f32_dpp v46, v142, v138 row_shl:4 row_mask:0xf bank_mask:0x1
v_add_f32_dpp v47, v143, v139 row_shl:4 row_mask:0xf bank_mask:0x1
v_max3_f32 v26, v40, v41, v42
v_max3_f32 v27, v43, v44, v45
v_max3_f32 v26, v26, v46, v47
v_max_f32 v26, v26, v27
ds_read_b64_tr_b16 v[98:99], v14 offset:28672
s_nop 0
v_max_f32_dpp v26, v26, v26 row_ror:4 row_mask:0xf bank_mask:0xf bound_ctrl:1
ds_read_b64_tr_b16 v[100:101], v15 offset:24576
s_nop 0
v_max_f32_dpp v26, v26, v26 row_ror:8 row_mask:0xf bank_mask:0xf bound_ctrl:1
ds_read_b64_tr_b16 v[102:103], v15 offset:28672
v_mov_b32 v27, v26
ds_read_b64_tr_b16 v[104:105], v16 offset:24576
ds_read_b64_tr_b16 v[106:107], v16 offset:28672
v_permlane16_swap_b32 v26, v27
ds_read_b64_tr_b16 v[108:109], v17 offset:24576
v_max_f32 v26, v26, v27
ds_read_b64_tr_b16 v[110:111], v17 offset:28672
v_mov_b32 v27, v26
ds_read_b64_tr_b16 v[112:113], v18 offset:24576
ds_read_b64_tr_b16 v[114:115], v18 offset:28672
v_permlane32_swap_b32 v26, v27
ds_read_b64_tr_b16 v[116:117], v19 offset:24576
v_max_f32 v26, v26, v27
ds_read_b64_tr_b16 v[118:119], v19 offset:28672
v_mul_f32 v22, s34, v26
ds_read_b64_tr_b16 v[120:121], v20 offset:24576
v_fma_f32 v128, v40, s34, -v22
ds_read_b64_tr_b16 v[122:123], v20 offset:28672
v_fma_f32 v129, v41, s34, -v22
v_fma_f32 v130, v42, s34, -v22
v_fma_f32 v131, v43, s34, -v22
v_fma_f32 v132, v44, s34, -v22
v_fma_f32 v133, v45, s34, -v22
v_fma_f32 v134, v46, s34, -v22
v_fma_f32 v135, v47, s34, -v22
ds_read_b64_tr_b16 v[124:125], v21 offset:24576
v_exp_f32 v128, v128
ds_read_b64_tr_b16 v[126:127], v21 offset:28672
v_exp_f32 v129, v129
v_exp_f32 v130, v130
v_exp_f32 v131, v131
v_exp_f32 v132, v132
v_exp_f32 v133, v133
v_exp_f32 v134, v134
v_exp_f32 v135, v135
v_add_f32 v26, v128, v129
v_add_f32 v27, v130, v131
v_cvt_pk_bf16_f32 v48, v128, v129
v_cvt_pk_bf16_f32 v49, v130, v131
v_add_f32 v28, v132, v133
v_add_f32 v29, v134, v135
v_cvt_pk_bf16_f32 v50, v132, v133
v_cvt_pk_bf16_f32 v51, v134, v135
ds_read_b64_tr_b16 v[128:129], v14 offset:32768
v_mov_b32_dpp v80, v48 quad_perm:[0,1,2,3] row_mask:0xf bank_mask:0x1
ds_read_b64_tr_b16 v[130:131], v15 offset:32768
v_mov_b32_dpp v81, v49 quad_perm:[0,1,2,3] row_mask:0xf bank_mask:0x1
ds_read_b64_tr_b16 v[132:133], v16 offset:32768
v_mov_b32_dpp v82, v48 row_shl:4 row_mask:0xf bank_mask:0x1
ds_read_b64_tr_b16 v[134:135], v17 offset:32768
v_mov_b32_dpp v83, v49 row_shl:4 row_mask:0xf bank_mask:0x1
ds_read_b64_tr_b16 v[136:137], v18 offset:32768
v_mov_b32_dpp v84, v48 row_shr:4 row_mask:0xf bank_mask:0x2
ds_read_b64_tr_b16 v[138:139], v19 offset:32768
s_waitcnt lgkmcnt(14)
v_mfma_f32_16x16x32_bf16 v[64:67], v[80:83], v[96:99], 0
ds_read_b64_tr_b16 v[140:141], v20 offset:32768
v_mov_b32_dpp v85, v49 row_shr:4 row_mask:0xf bank_mask:0x2
ds_read_b64_tr_b16 v[142:143], v21 offset:32768
s_waitcnt lgkmcnt(14)
v_mfma_f32_16x16x32_bf16 v[68:71], v[80:83], v[112:115], 0
v_mov_b32_dpp v86, v48 quad_perm:[0,1,2,3] row_mask:0xf bank_mask:0x2
v_mov_b32_dpp v87, v49 quad_perm:[0,1,2,3] row_mask:0xf bank_mask:0x2
v_mov_b32_dpp v88, v48 row_shr:8 row_mask:0xf bank_mask:0x4
v_mov_b32_dpp v89, v49 row_shr:8 row_mask:0xf bank_mask:0x4
v_mfma_f32_16x16x32_bf16 v[64:67], v[84:87], v[100:103], v[64:67]
v_mov_b32_dpp v90, v48 row_shr:4 row_mask:0xf bank_mask:0x4
v_mov_b32_dpp v91, v49 row_shr:4 row_mask:0xf bank_mask:0x4
s_waitcnt lgkmcnt(12)
v_mfma_f32_16x16x32_bf16 v[68:71], v[84:87], v[116:119], v[68:71]
v_mov_b32_dpp v92, v48 row_shr:12 row_mask:0xf bank_mask:0x8
v_mov_b32_dpp v93, v49 row_shr:12 row_mask:0xf bank_mask:0x8
v_mfma_f32_16x16x32_bf16 v[64:67], v[88:91], v[104:107], v[64:67]
v_mov_b32_dpp v94, v48 row_shr:8 row_mask:0xf bank_mask:0x8
v_mov_b32_dpp v95, v49 row_shr:8 row_mask:0xf bank_mask:0x8
s_waitcnt lgkmcnt(10)
v_mfma_f32_16x16x32_bf16 v[68:71], v[88:91], v[120:123], v[68:71]
v_mov_b32_dpp v80, v48 row_shl:8 row_mask:0xf bank_mask:0x1
v_mov_b32_dpp v81, v49 row_shl:8 row_mask:0xf bank_mask:0x1
v_mfma_f32_16x16x32_bf16 v[64:67], v[92:95], v[108:111], v[64:67]
v_mov_b32_dpp v82, v50 quad_perm:[0,1,2,3] row_mask:0xf bank_mask:0x1
v_mov_b32_dpp v83, v51 quad_perm:[0,1,2,3] row_mask:0xf bank_mask:0x1
s_waitcnt lgkmcnt(8)
v_mfma_f32_16x16x32_bf16 v[68:71], v[92:95], v[124:127], v[68:71]
v_perm_b32 v96, v228, v224, s40
v_perm_b32 v97, v236, v232, s40
s_waitcnt lgkmcnt(7)
v_mfma_f32_16x16x16_bf16 v[64:67], v[80:81], v[128:129], v[64:67]
v_perm_b32 v98, v244, v240, s40
v_perm_b32 v99, v252, v248, s40
s_waitcnt lgkmcnt(3)
v_mfma_f32_16x16x16_bf16 v[68:71], v[80:81], v[136:137], v[68:71]
v_mov_b32_dpp v80, v48 row_shl:12 row_mask:0xf bank_mask:0x1
v_mov_b32_dpp v81, v49 row_shl:12 row_mask:0xf bank_mask:0x1
v_perm_b32 v112, v230, v226, s40
v_perm_b32 v113, v238, v234, s40
v_perm_b32 v114, v246, v242, s40
v_perm_b32 v115, v254, v250, s40
v_mov_b32_dpp v84, v48 row_shl:4 row_mask:0xf bank_mask:0x2
v_mov_b32_dpp v85, v49 row_shl:4 row_mask:0xf bank_mask:0x2
v_mfma_f32_16x16x32_bf16 v[72:75], v[80:83], v[96:99], 0
v_mov_b32_dpp v86, v50 row_shr:4 row_mask:0xf bank_mask:0x2
v_mov_b32_dpp v87, v51 row_shr:4 row_mask:0xf bank_mask:0x2
v_mfma_f32_16x16x32_bf16 v[76:79], v[80:83], v[112:115], 0
v_perm_b32 v100, v228, v224, s41
v_perm_b32 v101, v236, v232, s41
v_mfma_f32_16x16x16_bf16 v[64:67], v[84:85], v[130:131], v[64:67]
v_perm_b32 v102, v244, v240, s41
v_perm_b32 v103, v252, v248, s41
s_waitcnt lgkmcnt(2)
v_mfma_f32_16x16x16_bf16 v[68:71], v[84:85], v[138:139], v[68:71]
v_mov_b32_dpp v84, v48 row_shl:8 row_mask:0xf bank_mask:0x2
v_mov_b32_dpp v85, v49 row_shl:8 row_mask:0xf bank_mask:0x2
v_perm_b32 v116, v230, v226, s41
v_perm_b32 v117, v238, v234, s41
v_perm_b32 v118, v246, v242, s41
v_perm_b32 v119, v254, v250, s41
v_mov_b32_dpp v88, v48 quad_perm:[0,1,2,3] row_mask:0xf bank_mask:0x4
v_mov_b32_dpp v89, v49 quad_perm:[0,1,2,3] row_mask:0xf bank_mask:0x4
v_mfma_f32_16x16x32_bf16 v[72:75], v[84:87], v[100:103], v[72:75]
v_mov_b32_dpp v90, v50 row_shr:8 row_mask:0xf bank_mask:0x4
v_mov_b32_dpp v91, v51 row_shr:8 row_mask:0xf bank_mask:0x4
v_mfma_f32_16x16x32_bf16 v[76:79], v[84:87], v[116:119], v[76:79]
v_perm_b32 v104, v229, v225, s40
v_perm_b32 v105, v237, v233, s40
v_mfma_f32_16x16x16_bf16 v[64:67], v[88:89], v[132:133], v[64:67]
v_perm_b32 v106, v245, v241, s40
v_perm_b32 v107, v253, v249, s40
s_waitcnt lgkmcnt(1)
v_mfma_f32_16x16x16_bf16 v[68:71], v[88:89], v[140:141], v[68:71]
v_mov_b32_dpp v88, v48 row_shl:4 row_mask:0xf bank_mask:0x4
v_mov_b32_dpp v89, v49 row_shl:4 row_mask:0xf bank_mask:0x4
v_perm_b32 v120, v231, v227, s40
v_perm_b32 v121, v239, v235, s40
v_perm_b32 v122, v247, v243, s40
v_perm_b32 v123, v255, v251, s40
v_mov_b32_dpp v92, v48 row_shr:4 row_mask:0xf bank_mask:0x8
v_mov_b32_dpp v93, v49 row_shr:4 row_mask:0xf bank_mask:0x8
v_add_f32 v26, v26, v27
v_add_f32 v28, v28, v29
v_add_f32 v26, v26, v28
v_mfma_f32_16x16x32_bf16 v[72:75], v[88:91], v[104:107], v[72:75]
v_mov_b32_dpp v94, v50 row_shr:12 row_mask:0xf bank_mask:0x8
v_mov_b32_dpp v95, v51 row_shr:12 row_mask:0xf bank_mask:0x8
v_mfma_f32_16x16x32_bf16 v[76:79], v[88:91], v[120:123], v[76:79]
v_perm_b32 v108, v229, v225, s41
v_perm_b32 v109, v237, v233, s41
v_mfma_f32_16x16x16_bf16 v[64:67], v[92:93], v[134:135], v[64:67]
v_perm_b32 v110, v245, v241, s41
v_perm_b32 v111, v253, v249, s41
s_waitcnt lgkmcnt(0)
v_mfma_f32_16x16x16_bf16 v[68:71], v[92:93], v[142:143], v[68:71]
v_mov_b32_dpp v92, v48 quad_perm:[0,1,2,3] row_mask:0xf bank_mask:0x8
v_mov_b32_dpp v93, v49 quad_perm:[0,1,2,3] row_mask:0xf bank_mask:0x8
v_perm_b32 v124, v231, v227, s41
v_perm_b32 v125, v239, v235, s41
v_perm_b32 v126, v247, v243, s41
v_perm_b32 v127, v255, v251, s41
v_add_f32_dpp v26, v26, v26 row_ror:4 row_mask:0xf bank_mask:0xf bound_ctrl:1
v_mfma_f32_16x16x32_bf16 v[72:75], v[92:95], v[108:111], v[72:75]
s_nop 0
v_add_f32_dpp v26, v26, v26 row_ror:8 row_mask:0xf bank_mask:0xf bound_ctrl:1
v_mov_b32 v27, v26
v_mfma_f32_16x16x32_bf16 v[76:79], v[92:95], v[124:127], v[76:79]
s_nop 0
v_permlane16_swap_b32 v26, v27
v_add_f32 v26, v26, v27
v_mov_b32 v27, v26
v_cvt_pk_bf16_f32 v128, v64, v68
v_cvt_pk_bf16_f32 v129, v65, v69
v_permlane32_swap_b32 v26, v27
v_add_f32 v26, v26, v27
v_mov_b32 v23, v26
v_cvt_pk_bf16_f32 v130, v66, v70
v_cvt_pk_bf16_f32 v131, v67, v71
ds_write2st64_b32 v9, v128, v129 offset0:112 offset1:113
v_cvt_pk_bf16_f32 v132, v72, v76
v_cvt_pk_bf16_f32 v133, v73, v77
v_cvt_pk_bf16_f32 v134, v74, v78
v_cvt_pk_bf16_f32 v135, v75, v79
ds_write2st64_b32 v9, v132, v133 offset0:116 offset1:117
ds_write2st64_b32 v9, v130, v131 offset0:114 offset1:115
ds_write2st64_b32 v9, v134, v135 offset0:118 offset1:119
ds_write_b64 v11, v[22:23] offset:30720
s_waitcnt lgkmcnt(0)
s_barrier
v_add_u32 v130, 0x7000, v13
v_add_u32 v133, 0x19000, v13
ds_read_b64 v[96:97], v130 offset:0
v_add_u32 v128, 0x7000, v12
ds_read_b64 v[98:99], v130 offset:36864
v_add_u32 v129, 0x7000, v24
ds_read_b64 v[100:101], v133 offset:0
v_add_u32 v131, 0x19000, v12
ds_read_b64 v[102:103], v133 offset:36864
v_add_u32 v132, 0x19000, v24
ds_read_b32 v104, v128 offset:0
v_mov_b32 v137, s12
ds_read2_b32 v[108:109], v129 offset0:0 offset1:8
v_mov_b32 v139, s12
ds_read_b32 v105, v128 offset:36864
ds_read_b32 v110, v129 offset:36864
ds_read_b32 v111, v129 offset:36896
ds_read_b32 v106, v131 offset:0
ds_read2_b32 v[112:113], v132 offset0:0 offset1:8
ds_read_b32 v107, v131 offset:36864
ds_read_b32 v114, v132 offset:36864
ds_read_b32 v115, v132 offset:36896
s_waitcnt lgkmcnt(11)
v_max3_f32 v26, v96, v98, v100
s_waitcnt lgkmcnt(10)
v_max_f32 v26, v26, v102
v_sub_f32 v96, v96, v26
v_sub_f32 v98, v98, v26
v_exp_f32 v96, v96
v_sub_f32 v100, v100, v26
v_exp_f32 v98, v98
s_waitcnt lgkmcnt(9)
v_lshlrev_b32 v116, 16, v104
v_and_b32 v117, 0xffff0000, v104
s_waitcnt lgkmcnt(8)
v_perm_b32 v118, v108, v108, v25
v_perm_b32 v119, v109, v109, v25
v_sub_f32 v102, v102, v26
v_exp_f32 v100, v100
v_pk_add_f32 v[116:117], v[116:117], v[118:119]
s_waitcnt lgkmcnt(7)
v_lshlrev_b32 v120, 16, v105
v_and_b32 v121, 0xffff0000, v105
s_waitcnt lgkmcnt(6)
v_perm_b32 v122, v110, v110, v25
s_waitcnt lgkmcnt(5)
v_perm_b32 v123, v111, v111, v25
v_exp_f32 v102, v102
v_pk_mul_f32 v[134:135], v[116:117], v[96:97] op_sel_hi:[1,0]
v_pk_add_f32 v[120:121], v[120:121], v[122:123]
s_waitcnt lgkmcnt(4)
v_lshlrev_b32 v116, 16, v106
v_and_b32 v117, 0xffff0000, v106
s_waitcnt lgkmcnt(3)
v_perm_b32 v118, v112, v112, v25
v_perm_b32 v119, v113, v113, v25
v_pk_fma_f32 v[134:135], v[120:121], v[98:99], v[134:135] op_sel_hi:[1,0,1]
v_pk_add_f32 v[116:117], v[116:117], v[118:119]
s_waitcnt lgkmcnt(2)
v_lshlrev_b32 v120, 16, v107
v_and_b32 v121, 0xffff0000, v107
s_waitcnt lgkmcnt(1)
v_perm_b32 v122, v114, v114, v25
s_waitcnt lgkmcnt(0)
v_perm_b32 v123, v115, v115, v25
v_pk_fma_f32 v[134:135], v[116:117], v[100:101], v[134:135] op_sel_hi:[1,0,1]
v_pk_add_f32 v[120:121], v[120:121], v[122:123]
v_pk_fma_f32 v[134:135], v[120:121], v[102:103], v[134:135] op_sel_hi:[1,0,1]
v_mul_f32 v27, v96, v97
v_cvt_pk_bf16_f32 v136, v134, v135
v_fma_f32 v27, v98, v99, v27
v_fma_f32 v27, v100, v101, v27
v_fma_f32 v27, v102, v103, v27
v_mov_b32_dpp v138, v136 quad_perm:[1,0,3,2] row_mask:0xf bank_mask:0xf bound_ctrl:1
v_cndmask_b32 v136, v136, v26, s[56:57]
v_cndmask_b32 v138, v138, v27, s[56:57]
s_mov_b32 exec_lo, 0x55555557
s_mov_b32 exec_hi, 0x55555555
buffer_store_dwordx4 v[136:139], v7, s[24:27], 0 offen sc1
s_mov_b64 exec, -1
s_nop 0
v_add_u32 v7, v7, v8
v_add_u32 v7, v7, v8
v_add_u32 v7, v7, v8
v_add_u32 v7, v7, v8
v_add_u32 v7, v7, v8
v_add_u32 v7, v7, v8
v_add_u32 v7, v7, v8
s_cmp_lg_u32 s13, 0
s_cbranch_scc1 Lqdo_109_%=
s_mov_b32 m0, 147456
s_nop 0
buffer_load_dwordx4 v52, s[24:27], s43 offen sc1 lds
buffer_load_dwordx4 v52, s[24:27], s43 offen offset:1024 sc1 lds
s_branch Lqdj_110_%=
Lqdo_109_%=:
buffer_load_dword v61, v0, s[24:27], 0 offen
buffer_load_dword v61, v0, s[24:27], 0 offen
Lqdj_110_%=:
s_waitcnt vmcnt(0)
s_barrier
s_mov_b32 s39, 0
Lqchk_111_%=:
ds_read_b128 v[96:99], v60 offset:0
ds_read_b128 v[100:103], v60 offset:512
ds_read_b128 v[104:107], v60 offset:1024
ds_read_b128 v[108:111], v60 offset:1536
s_waitcnt lgkmcnt(0)
v_bitop3_b32 v26, v97, v99, s12 bitop3:0x7e
v_bitop3_b32 v27, v101, v103, s12 bitop3:0x7e
v_bitop3_b32 v28, v105, v107, s12 bitop3:0x7e
v_bitop3_b32 v29, v109, v111, s12 bitop3:0x7e
v_or3_b32 v26, v26, v27, v28
v_or_b32 v26, v26, v29
v_cmp_ne_u32 vcc, 0, v26
v_mov_b32 v120, v96
v_mov_b32 v121, v98
v_mov_b32 v122, v100
v_mov_b32 v123, v102
v_mov_b32 v124, v104
v_mov_b32 v125, v106
v_mov_b32 v126, v108
v_mov_b32 v127, v110
s_cbranch_vccnz Lqslow_112_%=
Lqcont_113_%=:
v_mfma_f32_16x16x32_bf16 v[128:131], a[16:19], v[120:123], 0
v_mfma_f32_16x16x32_bf16 v[132:135], a[24:27], v[120:123], 0
v_mfma_f32_16x16x32_bf16 v[136:139], a[32:35], v[120:123], 0
v_mfma_f32_16x16x32_bf16 v[140:143], a[40:43], v[120:123], 0
v_mfma_f32_16x16x32_bf16 v[128:131], a[20:23], v[124:127], v[128:131]
v_mfma_f32_16x16x32_bf16 v[132:135], a[28:31], v[124:127], v[132:135]
v_mfma_f32_16x16x32_bf16 v[136:139], a[36:39], v[124:127], v[136:139]
v_mfma_f32_16x16x32_bf16 v[140:143], a[44:47], v[124:127], v[140:143]
v_mfma_f32_16x16x32_bf16 v[96:99], a[48:51], v[120:123], 0
s_nop 4
v_permlane32_swap_b32 v128, v132
v_permlane32_swap_b32 v129, v133
v_permlane32_swap_b32 v130, v134
v_permlane32_swap_b32 v131, v135
v_mfma_f32_16x16x32_bf16 v[100:103], a[56:59], v[120:123], 0
v_add_f32_dpp v40, v132, v128 row_shl:4 row_mask:0xf bank_mask:0x1
v_add_f32_dpp v41, v133, v129 row_shl:4 row_mask:0xf bank_mask:0x1
v_add_f32_dpp v42, v134, v130 row_shl:4 row_mask:0xf bank_mask:0x1
v_add_f32_dpp v43, v135, v131 row_shl:4 row_mask:0xf bank_mask:0x1
v_permlane32_swap_b32 v136, v140
v_permlane32_swap_b32 v137, v141
v_permlane32_swap_b32 v138, v142
v_permlane32_swap_b32 v139, v143
v_mfma_f32_16x16x32_bf16 v[128:131], a[64:67], v[120:123], v[56:59]
v_add_f32_dpp v40, v136, v140 row_shr:4 row_mask:0xf bank_mask:0x2
v_add_f32_dpp v41, v137, v141 row_shr:4 row_mask:0xf bank_mask:0x2
v_mfma_f32_16x16x32_bf16 v[132:135], a[72:75], v[120:123], v[56:59]
v_add_f32_dpp v42, v138, v142 row_shr:4 row_mask:0xf bank_mask:0x2
v_add_f32_dpp v43, v139, v143 row_shr:4 row_mask:0xf bank_mask:0x2
v_mfma_f32_16x16x32_bf16 v[96:99], a[52:55], v[124:127], v[96:99]
v_mfma_f32_16x16x32_bf16 v[100:103], a[60:63], v[124:127], v[100:103]
v_mfma_f32_16x16x32_bf16 v[136:139], a[80:83], v[120:123], v[56:59]
v_mfma_f32_16x16x32_bf16 v[140:143], a[88:91], v[120:123], v[56:59]
v_mfma_f32_16x16x32_bf16 v[128:131], a[68:71], v[124:127], v[128:131]
s_nop 4
v_permlane32_swap_b32 v96, v100
v_permlane32_swap_b32 v97, v101
v_mfma_f32_16x16x32_bf16 v[132:135], a[76:79], v[124:127], v[132:135]
v_permlane32_swap_b32 v98, v102
v_permlane32_swap_b32 v99, v103
v_mfma_f32_16x16x32_bf16 v[136:139], a[84:87], v[124:127], v[136:139]
v_add_f32_dpp v40, v100, v96 row_shl:4 row_mask:0xf bank_mask:0x4
v_add_f32_dpp v41, v101, v97 row_shl:4 row_mask:0xf bank_mask:0x4
v_mfma_f32_16x16x32_bf16 v[140:143], a[92:95], v[124:127], v[140:143]
v_add_f32_dpp v42, v102, v98 row_shl:4 row_mask:0xf bank_mask:0x4
v_add_f32_dpp v43, v103, v99 row_shl:4 row_mask:0xf bank_mask:0x4
ds_read_b64_tr_b16 v[96:97], v14 offset:12288
v_permlane32_swap_b32 v128, v132
v_permlane32_swap_b32 v129, v133
v_permlane32_swap_b32 v130, v134
v_permlane32_swap_b32 v131, v135
v_add_f32_dpp v40, v128, v132 row_shr:4 row_mask:0xf bank_mask:0x8
v_add_f32_dpp v41, v129, v133 row_shr:4 row_mask:0xf bank_mask:0x8
v_add_f32_dpp v42, v130, v134 row_shr:4 row_mask:0xf bank_mask:0x8
v_add_f32_dpp v43, v131, v135 row_shr:4 row_mask:0xf bank_mask:0x8
v_permlane32_swap_b32 v136, v140
v_permlane32_swap_b32 v137, v141
v_permlane32_swap_b32 v138, v142
v_permlane32_swap_b32 v139, v143
v_add_f32_dpp v44, v140, v136 row_shl:4 row_mask:0xf bank_mask:0x1
v_add_f32_dpp v45, v141, v137 row_shl:4 row_mask:0xf bank_mask:0x1
v_add_f32_dpp v46, v142, v138 row_shl:4 row_mask:0xf bank_mask:0x1
v_add_f32_dpp v47, v143, v139 row_shl:4 row_mask:0xf bank_mask:0x1
v_max3_f32 v26, v40, v41, v42
v_max3_f32 v27, v43, v44, v45
v_max3_f32 v26, v26, v46, v47
v_max_f32 v26, v26, v27
ds_read_b64_tr_b16 v[98:99], v14 offset:16384
s_nop 0
v_max_f32_dpp v26, v26, v26 row_ror:4 row_mask:0xf bank_mask:0xf bound_ctrl:1
ds_read_b64_tr_b16 v[100:101], v15 offset:12288
s_nop 0
v_max_f32_dpp v26, v26, v26 row_ror:8 row_mask:0xf bank_mask:0xf bound_ctrl:1
ds_read_b64_tr_b16 v[102:103], v15 offset:16384
v_mov_b32 v27, v26
ds_read_b64_tr_b16 v[104:105], v16 offset:12288
ds_read_b64_tr_b16 v[106:107], v16 offset:16384
v_permlane16_swap_b32 v26, v27
ds_read_b64_tr_b16 v[108:109], v17 offset:12288
v_max_f32 v26, v26, v27
ds_read_b64_tr_b16 v[110:111], v17 offset:16384
v_mov_b32 v27, v26
ds_read_b64_tr_b16 v[112:113], v18 offset:12288
ds_read_b64_tr_b16 v[114:115], v18 offset:16384
v_permlane32_swap_b32 v26, v27
ds_read_b64_tr_b16 v[116:117], v19 offset:12288
v_max_f32 v26, v26, v27
ds_read_b64_tr_b16 v[118:119], v19 offset:16384
v_mul_f32 v22, s34, v26
ds_read_b64_tr_b16 v[120:121], v20 offset:12288
v_fma_f32 v128, v40, s34, -v22
ds_read_b64_tr_b16 v[122:123], v20 offset:16384
v_fma_f32 v129, v41, s34, -v22
v_fma_f32 v130, v42, s34, -v22
v_fma_f32 v131, v43, s34, -v22
v_fma_f32 v132, v44, s34, -v22
v_fma_f32 v133, v45, s34, -v22
v_fma_f32 v134, v46, s34, -v22
v_fma_f32 v135, v47, s34, -v22
ds_read_b64_tr_b16 v[124:125], v21 offset:12288
v_exp_f32 v128, v128
ds_read_b64_tr_b16 v[126:127], v21 offset:16384
v_exp_f32 v129, v129
v_exp_f32 v130, v130
v_exp_f32 v131, v131
v_exp_f32 v132, v132
v_exp_f32 v133, v133
v_exp_f32 v134, v134
v_exp_f32 v135, v135
v_add_f32 v26, v128, v129
v_add_f32 v27, v130, v131
v_cvt_pk_bf16_f32 v48, v128, v129
v_cvt_pk_bf16_f32 v49, v130, v131
v_add_f32 v28, v132, v133
v_add_f32 v29, v134, v135
v_cvt_pk_bf16_f32 v50, v132, v133
v_cvt_pk_bf16_f32 v51, v134, v135
ds_read_b64_tr_b16 v[128:129], v14 offset:20480
v_mov_b32_dpp v80, v48 quad_perm:[0,1,2,3] row_mask:0xf bank_mask:0x1
ds_read_b64_tr_b16 v[130:131], v15 offset:20480
v_mov_b32_dpp v81, v49 quad_perm:[0,1,2,3] row_mask:0xf bank_mask:0x1
ds_read_b64_tr_b16 v[132:133], v16 offset:20480
v_mov_b32_dpp v82, v48 row_shl:4 row_mask:0xf bank_mask:0x1
ds_read_b64_tr_b16 v[134:135], v17 offset:20480
v_mov_b32_dpp v83, v49 row_shl:4 row_mask:0xf bank_mask:0x1
ds_read_b64_tr_b16 v[136:137], v18 offset:20480
v_mov_b32_dpp v84, v48 row_shr:4 row_mask:0xf bank_mask:0x2
ds_read_b64_tr_b16 v[138:139], v19 offset:20480
s_waitcnt lgkmcnt(14)
v_mfma_f32_16x16x32_bf16 v[64:67], v[80:83], v[96:99], 0
ds_read_b64_tr_b16 v[140:141], v20 offset:20480
v_mov_b32_dpp v85, v49 row_shr:4 row_mask:0xf bank_mask:0x2
ds_read_b64_tr_b16 v[142:143], v21 offset:20480
s_waitcnt lgkmcnt(14)
v_mfma_f32_16x16x32_bf16 v[68:71], v[80:83], v[112:115], 0
v_mov_b32_dpp v86, v48 quad_perm:[0,1,2,3] row_mask:0xf bank_mask:0x2
v_mov_b32_dpp v87, v49 quad_perm:[0,1,2,3] row_mask:0xf bank_mask:0x2
v_mov_b32_dpp v88, v48 row_shr:8 row_mask:0xf bank_mask:0x4
v_mov_b32_dpp v89, v49 row_shr:8 row_mask:0xf bank_mask:0x4
v_mfma_f32_16x16x32_bf16 v[64:67], v[84:87], v[100:103], v[64:67]
v_mov_b32_dpp v90, v48 row_shr:4 row_mask:0xf bank_mask:0x4
v_mov_b32_dpp v91, v49 row_shr:4 row_mask:0xf bank_mask:0x4
s_waitcnt lgkmcnt(12)
v_mfma_f32_16x16x32_bf16 v[68:71], v[84:87], v[116:119], v[68:71]
v_mov_b32_dpp v92, v48 row_shr:12 row_mask:0xf bank_mask:0x8
v_mov_b32_dpp v93, v49 row_shr:12 row_mask:0xf bank_mask:0x8
v_mfma_f32_16x16x32_bf16 v[64:67], v[88:91], v[104:107], v[64:67]
v_mov_b32_dpp v94, v48 row_shr:8 row_mask:0xf bank_mask:0x8
v_mov_b32_dpp v95, v49 row_shr:8 row_mask:0xf bank_mask:0x8
s_waitcnt lgkmcnt(10)
v_mfma_f32_16x16x32_bf16 v[68:71], v[88:91], v[120:123], v[68:71]
v_mov_b32_dpp v80, v48 row_shl:8 row_mask:0xf bank_mask:0x1
v_mov_b32_dpp v81, v49 row_shl:8 row_mask:0xf bank_mask:0x1
v_mfma_f32_16x16x32_bf16 v[64:67], v[92:95], v[108:111], v[64:67]
v_mov_b32_dpp v82, v50 quad_perm:[0,1,2,3] row_mask:0xf bank_mask:0x1
v_mov_b32_dpp v83, v51 quad_perm:[0,1,2,3] row_mask:0xf bank_mask:0x1
s_waitcnt lgkmcnt(8)
v_mfma_f32_16x16x32_bf16 v[68:71], v[92:95], v[124:127], v[68:71]
v_perm_b32 v96, v196, v192, s40
v_perm_b32 v97, v204, v200, s40
s_waitcnt lgkmcnt(7)
v_mfma_f32_16x16x16_bf16 v[64:67], v[80:81], v[128:129], v[64:67]
v_perm_b32 v98, v212, v208, s40
v_perm_b32 v99, v220, v216, s40
s_waitcnt lgkmcnt(3)
v_mfma_f32_16x16x16_bf16 v[68:71], v[80:81], v[136:137], v[68:71]
v_mov_b32_dpp v80, v48 row_shl:12 row_mask:0xf bank_mask:0x1
v_mov_b32_dpp v81, v49 row_shl:12 row_mask:0xf bank_mask:0x1
v_perm_b32 v112, v198, v194, s40
v_perm_b32 v113, v206, v202, s40
v_perm_b32 v114, v214, v210, s40
v_perm_b32 v115, v222, v218, s40
v_mov_b32_dpp v84, v48 row_shl:4 row_mask:0xf bank_mask:0x2
v_mov_b32_dpp v85, v49 row_shl:4 row_mask:0xf bank_mask:0x2
v_mfma_f32_16x16x32_bf16 v[72:75], v[80:83], v[96:99], 0
v_mov_b32_dpp v86, v50 row_shr:4 row_mask:0xf bank_mask:0x2
v_mov_b32_dpp v87, v51 row_shr:4 row_mask:0xf bank_mask:0x2
v_mfma_f32_16x16x32_bf16 v[76:79], v[80:83], v[112:115], 0
v_perm_b32 v100, v196, v192, s41
v_perm_b32 v101, v204, v200, s41
v_mfma_f32_16x16x16_bf16 v[64:67], v[84:85], v[130:131], v[64:67]
v_perm_b32 v102, v212, v208, s41
v_perm_b32 v103, v220, v216, s41
s_waitcnt lgkmcnt(2)
v_mfma_f32_16x16x16_bf16 v[68:71], v[84:85], v[138:139], v[68:71]
v_mov_b32_dpp v84, v48 row_shl:8 row_mask:0xf bank_mask:0x2
v_mov_b32_dpp v85, v49 row_shl:8 row_mask:0xf bank_mask:0x2
v_perm_b32 v116, v198, v194, s41
v_perm_b32 v117, v206, v202, s41
v_perm_b32 v118, v214, v210, s41
v_perm_b32 v119, v222, v218, s41
v_mov_b32_dpp v88, v48 quad_perm:[0,1,2,3] row_mask:0xf bank_mask:0x4
v_mov_b32_dpp v89, v49 quad_perm:[0,1,2,3] row_mask:0xf bank_mask:0x4
v_mfma_f32_16x16x32_bf16 v[72:75], v[84:87], v[100:103], v[72:75]
v_mov_b32_dpp v90, v50 row_shr:8 row_mask:0xf bank_mask:0x4
v_mov_b32_dpp v91, v51 row_shr:8 row_mask:0xf bank_mask:0x4
v_mfma_f32_16x16x32_bf16 v[76:79], v[84:87], v[116:119], v[76:79]
v_perm_b32 v104, v197, v193, s40
v_perm_b32 v105, v205, v201, s40
v_mfma_f32_16x16x16_bf16 v[64:67], v[88:89], v[132:133], v[64:67]
v_perm_b32 v106, v213, v209, s40
v_perm_b32 v107, v221, v217, s40
s_waitcnt lgkmcnt(1)
v_mfma_f32_16x16x16_bf16 v[68:71], v[88:89], v[140:141], v[68:71]
v_mov_b32_dpp v88, v48 row_shl:4 row_mask:0xf bank_mask:0x4
v_mov_b32_dpp v89, v49 row_shl:4 row_mask:0xf bank_mask:0x4
v_perm_b32 v120, v199, v195, s40
v_perm_b32 v121, v207, v203, s40
v_perm_b32 v122, v215, v211, s40
v_perm_b32 v123, v223, v219, s40
v_mov_b32_dpp v92, v48 row_shr:4 row_mask:0xf bank_mask:0x8
v_mov_b32_dpp v93, v49 row_shr:4 row_mask:0xf bank_mask:0x8
v_add_f32 v26, v26, v27
v_add_f32 v28, v28, v29
v_add_f32 v26, v26, v28
v_mfma_f32_16x16x32_bf16 v[72:75], v[88:91], v[104:107], v[72:75]
v_mov_b32_dpp v94, v50 row_shr:12 row_mask:0xf bank_mask:0x8
v_mov_b32_dpp v95, v51 row_shr:12 row_mask:0xf bank_mask:0x8
v_mfma_f32_16x16x32_bf16 v[76:79], v[88:91], v[120:123], v[76:79]
v_perm_b32 v108, v197, v193, s41
v_perm_b32 v109, v205, v201, s41
v_mfma_f32_16x16x16_bf16 v[64:67], v[92:93], v[134:135], v[64:67]
v_perm_b32 v110, v213, v209, s41
v_perm_b32 v111, v221, v217, s41
s_waitcnt lgkmcnt(0)
v_mfma_f32_16x16x16_bf16 v[68:71], v[92:93], v[142:143], v[68:71]
v_mov_b32_dpp v92, v48 quad_perm:[0,1,2,3] row_mask:0xf bank_mask:0x8
v_mov_b32_dpp v93, v49 quad_perm:[0,1,2,3] row_mask:0xf bank_mask:0x8
v_perm_b32 v124, v199, v195, s41
v_perm_b32 v125, v207, v203, s41
v_perm_b32 v126, v215, v211, s41
v_perm_b32 v127, v223, v219, s41
v_add_f32_dpp v26, v26, v26 row_ror:4 row_mask:0xf bank_mask:0xf bound_ctrl:1
v_mfma_f32_16x16x32_bf16 v[72:75], v[92:95], v[108:111], v[72:75]
s_nop 0
v_add_f32_dpp v26, v26, v26 row_ror:8 row_mask:0xf bank_mask:0xf bound_ctrl:1
v_mov_b32 v27, v26
v_mfma_f32_16x16x32_bf16 v[76:79], v[92:95], v[124:127], v[76:79]
s_nop 0
v_permlane16_swap_b32 v26, v27
v_add_f32 v26, v26, v27
v_mov_b32 v27, v26
v_cvt_pk_bf16_f32 v128, v64, v68
v_cvt_pk_bf16_f32 v129, v65, v69
v_permlane32_swap_b32 v26, v27
v_add_f32 v26, v26, v27
v_mov_b32 v23, v26
v_cvt_pk_bf16_f32 v130, v66, v70
v_cvt_pk_bf16_f32 v131, v67, v71
ds_write2st64_b32 v9, v128, v129 offset0:64 offset1:65
v_cvt_pk_bf16_f32 v132, v72, v76
v_cvt_pk_bf16_f32 v133, v73, v77
v_cvt_pk_bf16_f32 v134, v74, v78
v_cvt_pk_bf16_f32 v135, v75, v79
ds_write2st64_b32 v9, v132, v133 offset0:68 offset1:69
ds_write2st64_b32 v9, v130, v131 offset0:66 offset1:67
ds_write2st64_b32 v9, v134, v135 offset0:70 offset1:71
ds_write_b64 v11, v[22:23] offset:18432
s_waitcnt lgkmcnt(0)
s_barrier
v_add_u32 v130, 0x4000, v13
v_add_u32 v133, 0x16000, v13
ds_read_b64 v[96:97], v130 offset:0
v_add_u32 v128, 0x4000, v12
ds_read_b64 v[98:99], v130 offset:36864
v_add_u32 v129, 0x4000, v24
ds_read_b64 v[100:101], v133 offset:0
v_add_u32 v131, 0x16000, v12
ds_read_b64 v[102:103], v133 offset:36864
v_add_u32 v132, 0x16000, v24
ds_read_b32 v104, v128 offset:0
v_mov_b32 v137, s12
ds_read2_b32 v[108:109], v129 offset0:0 offset1:8
v_mov_b32 v139, s12
ds_read_b32 v105, v128 offset:36864
ds_read_b32 v110, v129 offset:36864
ds_read_b32 v111, v129 offset:36896
ds_read_b32 v106, v131 offset:0
ds_read2_b32 v[112:113], v132 offset0:0 offset1:8
ds_read_b32 v107, v131 offset:36864
ds_read_b32 v114, v132 offset:36864
ds_read_b32 v115, v132 offset:36896
s_waitcnt lgkmcnt(11)
v_max3_f32 v26, v96, v98, v100
s_waitcnt lgkmcnt(10)
v_max_f32 v26, v26, v102
v_sub_f32 v96, v96, v26
v_sub_f32 v98, v98, v26
v_exp_f32 v96, v96
v_sub_f32 v100, v100, v26
v_exp_f32 v98, v98
s_waitcnt lgkmcnt(9)
v_lshlrev_b32 v116, 16, v104
v_and_b32 v117, 0xffff0000, v104
s_waitcnt lgkmcnt(8)
v_perm_b32 v118, v108, v108, v25
v_perm_b32 v119, v109, v109, v25
v_sub_f32 v102, v102, v26
v_exp_f32 v100, v100
v_pk_add_f32 v[116:117], v[116:117], v[118:119]
s_waitcnt lgkmcnt(7)
v_lshlrev_b32 v120, 16, v105
v_and_b32 v121, 0xffff0000, v105
s_waitcnt lgkmcnt(6)
v_perm_b32 v122, v110, v110, v25
s_waitcnt lgkmcnt(5)
v_perm_b32 v123, v111, v111, v25
v_exp_f32 v102, v102
v_pk_mul_f32 v[134:135], v[116:117], v[96:97] op_sel_hi:[1,0]
v_pk_add_f32 v[120:121], v[120:121], v[122:123]
s_waitcnt lgkmcnt(4)
v_lshlrev_b32 v116, 16, v106
v_and_b32 v117, 0xffff0000, v106
s_waitcnt lgkmcnt(3)
v_perm_b32 v118, v112, v112, v25
v_perm_b32 v119, v113, v113, v25
v_pk_fma_f32 v[134:135], v[120:121], v[98:99], v[134:135] op_sel_hi:[1,0,1]
v_pk_add_f32 v[116:117], v[116:117], v[118:119]
s_waitcnt lgkmcnt(2)
v_lshlrev_b32 v120, 16, v107
v_and_b32 v121, 0xffff0000, v107
s_waitcnt lgkmcnt(1)
v_perm_b32 v122, v114, v114, v25
s_waitcnt lgkmcnt(0)
v_perm_b32 v123, v115, v115, v25
v_pk_fma_f32 v[134:135], v[116:117], v[100:101], v[134:135] op_sel_hi:[1,0,1]
v_pk_add_f32 v[120:121], v[120:121], v[122:123]
v_pk_fma_f32 v[134:135], v[120:121], v[102:103], v[134:135] op_sel_hi:[1,0,1]
v_mul_f32 v27, v96, v97
v_cvt_pk_bf16_f32 v136, v134, v135
v_fma_f32 v27, v98, v99, v27
v_fma_f32 v27, v100, v101, v27
v_fma_f32 v27, v102, v103, v27
v_mov_b32_dpp v138, v136 quad_perm:[1,0,3,2] row_mask:0xf bank_mask:0xf bound_ctrl:1
v_cndmask_b32 v136, v136, v26, s[56:57]
v_cndmask_b32 v138, v138, v27, s[56:57]
s_mov_b32 exec_lo, 0x55555557
s_mov_b32 exec_hi, 0x55555555
buffer_store_dwordx4 v[136:139], v7, s[24:27], 0 offen sc1
s_mov_b64 exec, -1
s_nop 0
v_add_u32 v7, v7, v8
s_waitcnt vmcnt(0)
s_endpgm
Lend_%=:
Lslow_2_%=:
s_add_u32 s39, s39, 1
s_cmp_gt_u32 s39, 16384
s_cbranch_scc1 Labort_4_%=
buffer_load_dwordx4 v[112:115], v5, s[24:27], s49 offen sc1
buffer_load_dwordx4 v[116:119], v5, s[24:27], s49 offen offset:512 sc1
buffer_load_dwordx4 v[120:123], v5, s[24:27], s49 offen offset:1024 sc1
buffer_load_dwordx4 v[124:127], v5, s[24:27], s49 offen offset:1536 sc1
s_waitcnt vmcnt(0)
s_branch Lchk_1_%=
Labort_4_%=:
v_mov_b32 v27, 256
v_mov_b32 v26, 1
buffer_store_dword v26, v27, s[24:27], 0 offen sc1
s_nop 4
s_branch Lcont_3_%=
Lslow_6_%=:
s_add_u32 s39, s39, 1
s_cmp_gt_u32 s39, 16384
s_cbranch_scc1 Labort_8_%=
buffer_load_dwordx4 v[96:99], v5, s[24:27], s42 offen sc1
buffer_load_dwordx4 v[100:103], v5, s[24:27], s42 offen offset:512 sc1
buffer_load_dwordx4 v[104:107], v5, s[24:27], s42 offen offset:1024 sc1
buffer_load_dwordx4 v[108:111], v5, s[24:27], s42 offen offset:1536 sc1
s_waitcnt vmcnt(0)
s_branch Lchk_5_%=
Labort_8_%=:
v_mov_b32 v27, 256
v_mov_b32 v26, 1
buffer_store_dword v26, v27, s[24:27], 0 offen sc1
s_nop 4
s_branch Lcont_7_%=
Lslow_10_%=:
s_add_u32 s39, s39, 1
s_cmp_gt_u32 s39, 16384
s_cbranch_scc1 Labort_12_%=
buffer_load_dwordx4 v[112:115], v5, s[24:27], s42 offen sc1
buffer_load_dwordx4 v[116:119], v5, s[24:27], s42 offen offset:512 sc1
buffer_load_dwordx4 v[120:123], v5, s[24:27], s42 offen offset:1024 sc1
buffer_load_dwordx4 v[124:127], v5, s[24:27], s42 offen offset:1536 sc1
s_waitcnt vmcnt(0)
s_branch Lchk_9_%=
Labort_12_%=:
v_mov_b32 v27, 256
v_mov_b32 v26, 1
buffer_store_dword v26, v27, s[24:27], 0 offen sc1
s_nop 4
s_branch Lcont_11_%=
Lqslow_16_%=:
s_add_u32 s39, s39, 1
s_cmp_gt_u32 s39, 16384
s_cbranch_scc1 Lqab_18_%=
s_barrier
s_cmp_lg_u32 s13, 0
s_cbranch_scc1 Lqo_19_%=
s_sleep 2
s_mov_b32 m0, 149504
s_nop 0
buffer_load_dwordx4 v52, s[24:27], s42 offen sc1 lds
buffer_load_dwordx4 v52, s[24:27], s42 offen offset:1024 sc1 lds
s_waitcnt vmcnt(0)
Lqo_19_%=:
s_barrier
s_branch Lqchk_15_%=
Lqab_18_%=:
v_mov_b32 v27, 256
v_mov_b32 v26, 1
buffer_store_dword v26, v27, s[24:27], 0 offen sc1
s_nop 4
s_branch Lqcont_17_%=
Lqslow_26_%=:
s_add_u32 s39, s39, 1
s_cmp_gt_u32 s39, 16384
s_cbranch_scc1 Lqab_28_%=
s_barrier
s_cmp_lg_u32 s13, 0
s_cbranch_scc1 Lqo_29_%=
s_sleep 2
s_mov_b32 m0, 147456
s_nop 0
buffer_load_dwordx4 v52, s[24:27], s43 offen sc1 lds
buffer_load_dwordx4 v52, s[24:27], s43 offen offset:1024 sc1 lds
s_waitcnt vmcnt(0)
Lqo_29_%=:
s_barrier
s_branch Lqchk_25_%=
Lqab_28_%=:
v_mov_b32 v27, 256
v_mov_b32 v26, 1
buffer_store_dword v26, v27, s[24:27], 0 offen sc1
s_nop 4
s_branch Lqcont_27_%=
Lqslow_32_%=:
s_add_u32 s39, s39, 1
s_cmp_gt_u32 s39, 16384
s_cbranch_scc1 Lqab_34_%=
s_barrier
s_cmp_lg_u32 s13, 0
s_cbranch_scc1 Lqo_35_%=
s_sleep 2
s_mov_b32 m0, 149504
s_nop 0
buffer_load_dwordx4 v52, s[24:27], s42 offen sc1 lds
buffer_load_dwordx4 v52, s[24:27], s42 offen offset:1024 sc1 lds
s_waitcnt vmcnt(0)
Lqo_35_%=:
s_barrier
s_branch Lqchk_31_%=
Lqab_34_%=:
v_mov_b32 v27, 256
v_mov_b32 v26, 1
buffer_store_dword v26, v27, s[24:27], 0 offen sc1
s_nop 4
s_branch Lqcont_33_%=
Lqslow_42_%=:
s_add_u32 s39, s39, 1
s_cmp_gt_u32 s39, 16384
s_cbranch_scc1 Lqab_44_%=
s_barrier
s_cmp_lg_u32 s13, 0
s_cbranch_scc1 Lqo_45_%=
s_sleep 2
s_mov_b32 m0, 147456
s_nop 0
buffer_load_dwordx4 v52, s[24:27], s43 offen sc1 lds
buffer_load_dwordx4 v52, s[24:27], s43 offen offset:1024 sc1 lds
s_waitcnt vmcnt(0)
Lqo_45_%=:
s_barrier
s_branch Lqchk_41_%=
Lqab_44_%=:
v_mov_b32 v27, 256
v_mov_b32 v26, 1
buffer_store_dword v26, v27, s[24:27], 0 offen sc1
s_nop 4
s_branch Lqcont_43_%=
Lqslow_48_%=:
s_add_u32 s39, s39, 1
s_cmp_gt_u32 s39, 16384
s_cbranch_scc1 Lqab_50_%=
s_barrier
s_cmp_lg_u32 s13, 0
s_cbranch_scc1 Lqo_51_%=
s_sleep 2
s_mov_b32 m0, 149504
s_nop 0
buffer_load_dwordx4 v52, s[24:27], s42 offen sc1 lds
buffer_load_dwordx4 v52, s[24:27], s42 offen offset:1024 sc1 lds
s_waitcnt vmcnt(0)
Lqo_51_%=:
s_barrier
s_branch Lqchk_47_%=
Lqab_50_%=:
v_mov_b32 v27, 256
v_mov_b32 v26, 1
buffer_store_dword v26, v27, s[24:27], 0 offen sc1
s_nop 4
s_branch Lqcont_49_%=
Lqslow_58_%=:
s_add_u32 s39, s39, 1
s_cmp_gt_u32 s39, 16384
s_cbranch_scc1 Lqab_60_%=
s_barrier
s_cmp_lg_u32 s13, 0
s_cbranch_scc1 Lqo_61_%=
s_sleep 2
s_mov_b32 m0, 147456
s_nop 0
buffer_load_dwordx4 v52, s[24:27], s43 offen sc1 lds
buffer_load_dwordx4 v52, s[24:27], s43 offen offset:1024 sc1 lds
s_waitcnt vmcnt(0)
Lqo_61_%=:
s_barrier
s_branch Lqchk_57_%=
Lqab_60_%=:
v_mov_b32 v27, 256
v_mov_b32 v26, 1
buffer_store_dword v26, v27, s[24:27], 0 offen sc1
s_nop 4
s_branch Lqcont_59_%=
Lqslow_64_%=:
s_add_u32 s39, s39, 1
s_cmp_gt_u32 s39, 16384
s_cbranch_scc1 Lqab_66_%=
s_barrier
s_cmp_lg_u32 s13, 0
s_cbranch_scc1 Lqo_67_%=
s_sleep 2
s_mov_b32 m0, 149504
s_nop 0
buffer_load_dwordx4 v52, s[24:27], s42 offen sc1 lds
buffer_load_dwordx4 v52, s[24:27], s42 offen offset:1024 sc1 lds
s_waitcnt vmcnt(0)
Lqo_67_%=:
s_barrier
s_branch Lqchk_63_%=
Lqab_66_%=:
v_mov_b32 v27, 256
v_mov_b32 v26, 1
buffer_store_dword v26, v27, s[24:27], 0 offen sc1
s_nop 4
s_branch Lqcont_65_%=
Lqslow_74_%=:
s_add_u32 s39, s39, 1
s_cmp_gt_u32 s39, 16384
s_cbranch_scc1 Lqab_76_%=
s_barrier
s_cmp_lg_u32 s13, 0
s_cbranch_scc1 Lqo_77_%=
s_sleep 2
s_mov_b32 m0, 147456
s_nop 0
buffer_load_dwordx4 v52, s[24:27], s43 offen sc1 lds
buffer_load_dwordx4 v52, s[24:27], s43 offen offset:1024 sc1 lds
s_waitcnt vmcnt(0)
Lqo_77_%=:
s_barrier
s_branch Lqchk_73_%=
Lqab_76_%=:
v_mov_b32 v27, 256
v_mov_b32 v26, 1
buffer_store_dword v26, v27, s[24:27], 0 offen sc1
s_nop 4
s_branch Lqcont_75_%=
Lqslow_80_%=:
s_add_u32 s39, s39, 1
s_cmp_gt_u32 s39, 16384
s_cbranch_scc1 Lqab_82_%=
s_barrier
s_cmp_lg_u32 s13, 0
s_cbranch_scc1 Lqo_83_%=
s_sleep 2
s_mov_b32 m0, 149504
s_nop 0
buffer_load_dwordx4 v52, s[24:27], s42 offen sc1 lds
buffer_load_dwordx4 v52, s[24:27], s42 offen offset:1024 sc1 lds
s_waitcnt vmcnt(0)
Lqo_83_%=:
s_barrier
s_branch Lqchk_79_%=
Lqab_82_%=:
v_mov_b32 v27, 256
v_mov_b32 v26, 1
buffer_store_dword v26, v27, s[24:27], 0 offen sc1
s_nop 4
s_branch Lqcont_81_%=
Lqslow_88_%=:
s_add_u32 s39, s39, 1
s_cmp_gt_u32 s39, 16384
s_cbranch_scc1 Lqab_90_%=
s_barrier
s_cmp_lg_u32 s13, 0
s_cbranch_scc1 Lqo_91_%=
s_sleep 2
s_mov_b32 m0, 147456
s_nop 0
buffer_load_dwordx4 v52, s[24:27], s43 offen sc1 lds
buffer_load_dwordx4 v52, s[24:27], s43 offen offset:1024 sc1 lds
s_waitcnt vmcnt(0)
Lqo_91_%=:
s_barrier
s_branch Lqchk_87_%=
Lqab_90_%=:
v_mov_b32 v27, 256
v_mov_b32 v26, 1
buffer_store_dword v26, v27, s[24:27], 0 offen sc1
s_nop 4
s_branch Lqcont_89_%=
Lqslow_96_%=:
s_add_u32 s39, s39, 1
s_cmp_gt_u32 s39, 16384
s_cbranch_scc1 Lqab_98_%=
s_barrier
s_cmp_lg_u32 s13, 0
s_cbranch_scc1 Lqo_99_%=
s_sleep 2
s_mov_b32 m0, 147456
s_nop 0
buffer_load_dwordx4 v52, s[24:27], s43 offen sc1 lds
buffer_load_dwordx4 v52, s[24:27], s43 offen offset:1024 sc1 lds
s_waitcnt vmcnt(0)
Lqo_99_%=:
s_barrier
s_branch Lqchk_95_%=
Lqab_98_%=:
v_mov_b32 v27, 256
v_mov_b32 v26, 1
buffer_store_dword v26, v27, s[24:27], 0 offen sc1
s_nop 4
s_branch Lqcont_97_%=
Lqslow_104_%=:
s_add_u32 s39, s39, 1
s_cmp_gt_u32 s39, 16384
s_cbranch_scc1 Lqab_106_%=
s_barrier
s_cmp_lg_u32 s13, 0
s_cbranch_scc1 Lqo_107_%=
s_sleep 2
s_mov_b32 m0, 147456
s_nop 0
buffer_load_dwordx4 v52, s[24:27], s49 offen sc1 lds
buffer_load_dwordx4 v52, s[24:27], s49 offen offset:1024 sc1 lds
s_waitcnt vmcnt(0)
Lqo_107_%=:
s_barrier
s_branch Lqchk_103_%=
Lqab_106_%=:
v_mov_b32 v27, 256
v_mov_b32 v26, 1
buffer_store_dword v26, v27, s[24:27], 0 offen sc1
s_nop 4
s_branch Lqcont_105_%=
Lqslow_112_%=:
s_add_u32 s39, s39, 1
s_cmp_gt_u32 s39, 16384
s_cbranch_scc1 Lqab_114_%=
s_barrier
s_cmp_lg_u32 s13, 0
s_cbranch_scc1 Lqo_115_%=
s_sleep 2
s_mov_b32 m0, 147456
s_nop 0
buffer_load_dwordx4 v52, s[24:27], s43 offen sc1 lds
buffer_load_dwordx4 v52, s[24:27], s43 offen offset:1024 sc1 lds
s_waitcnt vmcnt(0)
Lqo_115_%=:
s_barrier
s_branch Lqchk_111_%=
Lqab_114_%=:
v_mov_b32 v27, 256
v_mov_b32 v26, 1
buffer_store_dword v26, v27, s[24:27], 0 offen sc1
s_nop 4
s_branch Lqcont_113_%=
)ASM"
      :
      : "{v[0:3]}"(u32x4{(unsigned)lane, vK, vV, vVL0}), "{v[4:7]}"(u32x4{vVL1, vQ, pub1, pub2}), "{v[8:11]}"(u32x4{inc, vWA, vWB, vWML}),
        "{v[12:15]}"(u32x4{vRD, vRDML, tr[0], tr[1]}), "{v[16:19]}"(u32x4{tr[2], tr[3], tr[4], tr[5]}), "{v[20:23]}"(u32x4{tr[6], tr[7], 0u, 0u}),
        "{v[24:25]}"(u32x2{vRDB, vSEL}), "{v[60:63]}"(u32x4{(lane < 4 ? (unsigned)((u * 4 + lane) * 128) : 0x7fff0000u), pv, vBias, vSt}), "{s[56:59]}"(sPF),
        "{s[16:19]}"(sK), "{s[20:23]}"(sV), "{s[24:27]}"(sWS), "{s[28:31]}"(s28), "{s[32:35]}"(s32), "{s[36:39]}"(s36), "{s[40:43]}"(s40),
        "{s[12:13]}"(s12), "{s[48:49]}"(s48)
      : "v26", "v27", "v28", "v29", "v30", "v31", "v32", "v33", "v34", "v35", "v36", "v37", "v38", "v39", "v40", "v41", "v42", "v43", "v44", "v45", "v46", "v47", "v48", "v49", "v50", "v51", "v52", "v53", "v54", "v55", "v56", "v57", "v58", "v59", "v64", "v65", "v66", "v67", "v68", "v69", "v70", "v71", "v72", "v73", "v74", "v75", "v76", "v77", "v78", "v79", "v80", "v81", "v82", "v83", "v84", "v85", "v86", "v87", "v88", "v89", "v90", "v91", "v92", "v93", "v94", "v95", "v96", "v97", "v98", "v99", "v100", "v101", "v102", "v103", "v104", "v105", "v106", "v107", "v108", "v109", "v110", "v111", "v112", "v113", "v114", "v115", "v116", "v117", "v118", "v119", "v120", "v121", "v122", "v123", "v124", "v125", "v126", "v127", "v128", "v129", "v130", "v131", "v132", "v133", "v134", "v135", "v136", "v137", "v138", "v139", "v140", "v141", "v142", "v143", "v144", "v145", "v146", "v147", "v148", "v149", "v150", "v151", "v152", "v153", "v154", "v155", "v156", "v157", "v158", "v159", "v160", "v161", "v162", "v163", "v164", "v165", "v166", "v167", "v168", "v169", "v170", "v171", "v172", "v173", "v174", "v175", "v176", "v177", "v178", "v179", "v180", "v181", "v182", "v183", "v184", "v185", "v186", "v187", "v188", "v189", "v190", "v191", "v192", "v193", "v194", "v195", "v196", "v197", "v198", "v199", "v200", "v201", "v202", "v203", "v204", "v205", "v206", "v207", "v208", "v209", "v210", "v211", "v212", "v213", "v214", "v215", "v216", "v217", "v218", "v219", "v220", "v221", "v222", "v223", "v224", "v225", "v226", "v227", "v228", "v229", "v230", "v231", "v232", "v233", "v234", "v235", "v236", "v237", "v238", "v239", "v240", "v241", "v242", "v243", "v244", "v245", "v246", "v247", "v248", "v249", "v250", "v251", "v252", "v253", "v254", "v255", "a0", "a1", "a2", "a3", "a4", "a5", "a6", "a7", "a8", "a9", "a10", "a11", "a12", "a13", "a14", "a15", "a16", "a17", "a18", "a19", "a20", "a21", "a22", "a23", "a24", "a25", "a26", "a27", "a28", "a29", "a30", "a31", "a32", "a33", "a34", "a35", "a36", "a37", "a38", "a39", "a40", "a41", "a42", "a43", "a44", "a45", "a46", "a47", "a48", "a49", "a50", "a51", "a52", "a53", "a54", "a55", "a56", "a57", "a58", "a59", "a60", "a61", "a62", "a63", "a64", "a65", "a66", "a67", "a68", "a69", "a70", "a71", "a72", "a73", "a74", "a75", "a76", "a77", "a78", "a79", "a80", "a81", "a82", "a83", "a84", "a85", "a86", "a87", "a88", "a89", "a90", "a91", "a92", "a93", "a94", "a95", "a96", "a97", "a98", "a99", "a100", "a101", "a102", "a103", "a104", "a105", "a106", "a107", "a108", "a109", "a110", "a111", "a112", "a113", "a114", "a115", "a116", "a117", "a118", "a119", "a120", "a121", "a122", "a123", "a124", "a125", "a126", "a127", "a128", "a129", "a130", "a131", "a132", "a133", "a134", "a135", "a136", "a137", "a138", "a139", "a140", "a141", "a142", "a143", "a144", "a145", "a146", "a147", "a148", "a149", "a150", "a151", "a152", "a153", "a154", "a155", "a156", "a157", "a158", "a159", "a160", "a161", "a162", "a163", "a164", "a165", "a166", "a167", "a168", "a169", "a170", "a171", "a172", "a173", "a174", "a175", "a176", "a177", "a178", "a179", "a180", "a181", "a182", "a183", "a184", "a185", "a186", "a187", "a188", "a189", "a190", "a191", "a192", "a193", "a194", "a195", "a196", "a197", "a198", "a199", "a200", "a201", "a202", "a203", "a204", "a205", "a206", "a207", "a208", "a209", "a210", "a211", "a212", "a213", "a214", "a215", "a216", "a217", "a218", "a219", "a220", "a221", "a222", "a223", "a224", "a225", "a226", "a227", "a228", "a229", "a230", "a231", "a232", "a233", "a234", "a235", "a236", "a237", "a238", "a239", "a240", "a241", "a242", "a243", "a244", "a245", "a246", "a247", "a248", "a249", "a250", "a251", "a252", "a253", "a254", "a255", "s44", "s45", "s46", "s47", "s50", "s51", "s52", "s53", "s54", "s55", "s60", "s61", "s62", "s63", "s64", "s65", "s66", "s67", "s68", "s69", "s70", "s71", "s72", "s73", "s74", "s75", "s76", "s77", "s78", "s79", "s80", "s81", "s82", "s83", "s84", "s85", "s86", "s87", "s88", "s89", "s90", "s91", "s92", "s93", "s94", "s95", "vcc", "memory");
  __builtin_unreachable();
}

__global__ void __launch_bounds__(256, 1) fused_k(Params p) {
  extern __shared__ __attribute__((aligned(16))) unsigned char smem[];
  LAS unsigned char* lds = (LAS unsigned char*)smem;
  const int bid = blockIdx.x, xcd = bid & 7, r = bid >> 3;
  const int w = __builtin_amdgcn_readfirstlane(threadIdx.x >> 6);
  int lid;
  if (XMAP == 0) lid = r < NCHAIN_R ? r * 8 + xcd : 48 + (xcd * CPH + (r - NCHAIN_R));
  else if (XMAP == 1) lid = xcd == 0 ? 16 + r : (xcd == 1 && r < 16) ? r : 48 + (xcd == 1 ? r - 16 : 16 + (xcd - 2) * 32 + r);
  else if (XMAP == 3) {
    const int nq = xcd <= 2 ? 3 : 2, q0 = xcd <= 2 ? (xcd - 1) * 3 : 6 + (xcd - 3) * 2, s0 = xcd <= 2 ? (xcd - 1) * 29 : 58 + (xcd - 3) * 30;
    lid = xcd == 0 ? 16 + r : (r < nq ? q0 + r : 48 + s0 + (r - nq));
  }
  else lid = xcd == 0 ? (r < 16 ? r : r < 30 ? 32 + (r - 16) : 46 + (r - 30)) : (xcd == 1 && r < 16) ? 16 + r : 48 + (xcd == 1 ? r - 16 : 16 + (xcd - 2) * 32 + r);
  lid = __builtin_amdgcn_readfirstlane(lid);
  if (lid >= 48) role_s(p, (lid - 48) / CPH, (lid - 48) % CPH, lid, w);
  else {
    const int ci = lid;
    const int sw = XMAP == 0 ? 2 * (((ci & 7) + 4) & 7) + ((ci >> 3) & 1) : (ci & 15);
    if (ci < 16) role_q(p, sw, lid, w);
    else if (ci < 32) role_w(p, sw, lid, w);
    else if (ci < 46) role_m(p, ci - 32, lid, w);
    else if (ci == 46) role_f(p, 0, lid, w);
  }
}
}

extern "C" void kernel_launch(void* const* d_in, const int* in_sizes, int n_in, void* d_out, int out_size, void* d_ws, size_t ws_size, hipStream_t stream) {
  using namespace ik;
  Params p;
  p.x = (const u16*)d_in[0]; p.kc = (const u16*)d_in[1]; p.vc = (const u16*)d_in[2]; p.wq = (const u16*)d_in[3]; p.wo = (const u16*)d_in[4];
  p.w1 = (const u16*)d_in[6]; p.w2 = (const u16*)d_in[7]; p.out = (u16*)d_out; p.ws = (unsigned char*)d_ws;
  (void)hipFuncSetAttribute((const void*)fused_k, hipFuncAttributeMaxDynamicSharedMemorySize, LDS_BYTES);
  hipLaunchKernelGGL(fused_k, dim3(256), dim3(256), LDS_BYTES, stream, p);
}
```
